```python
import torch

def _softmax(x, axis=-1):
    return torch.softmax(x, dim=axis)
def _gelu(x):
    return torch.nn.functional.gelu(x, approximate="tanh")
def _silu(x):
    return torch.nn.functional.silu(x)
def _sigmoid(x):
    return torch.sigmoid(x)
def _layernorm(x, axis=-1, eps=1e-5):
    m = x.mean(dim=axis, keepdim=True)
    v = x.var(dim=axis, keepdim=True, unbiased=False)
    return (x - m) / torch.sqrt(v + eps)
def _scatter_add(tgt, idx, src, axis=0):
    return tgt.clone().index_add_(axis, idx, src)
def _segment_sum(src, seg, n):
    out = torch.zeros((n,) + src.shape[1:], dtype=src.dtype)
    return out.index_add_(0, seg, src)
def _pad(x, pw):
    flat = tuple(v for pair in reversed(pw) for v in pair)
    return torch.nn.functional.pad(x, flat)

kernel_name = 'standalone__dcn_v2_cross__recsys'

def setup_inputs():

    _w = (lambda a: (lambda t: t.to(torch.bfloat16) if t.is_floating_point() else t)(torch.as_tensor(a)))
    B, F, D, L = 4096, 100, 32, 3
    Din = min(F * D, 1024)
    return {
        "x0": _w((lambda *s: torch.randn(*s, dtype=torch.bfloat16) * 0.05)(B, Din)),
        "ws": _w((lambda *s: torch.randn(*s, dtype=torch.bfloat16) * 0.05)(L, Din, Din)),
        "bs": _w((lambda *s: torch.randn(*s, dtype=torch.bfloat16) * 0.05)(L, Din)),
    }

def reference(x0, ws, bs):
    x = x0
    for l in range(ws.shape[0]):
        x = x0 * (x @ ws[l] + bs[l]) + x
    return x
```

```cpp
#include <hip/hip_runtime.h>
#include <hip/hip_bf16.h>

__global__ __launch_bounds__(256, 1) __attribute__((aligned(4096))) void dcn_fused(const void* x0, const void* w, const void* bias, void* out, void* ws) {
  __shared__ __attribute__((aligned(1024))) char smem[163840];
  unsigned ldsb = (unsigned)(size_t)smem;
  asm volatile(
    "s_load_dwordx8 s[20:27], %0, 0x0\n"
    "s_load_dwordx2 s[28:29], %0, 0x20\n"
    "s_getpc_b64 s[60:61]\n"
    "L_pc_%=:\n"
    "s_lshr_b32 s89, %1, 3\n"
    "s_and_b32 s89, s89, 3\n"
    "s_lshl_b32 s89, s89, 14\n"
    "v_lshl_add_u32 v1, %2, 6, s89\n"
    "v_min_u32 v1, L_end_%=-L_pc_%=-4, v1\n"
    "global_load_dword v156, v1, s[60:61]\n"
    "v_lshrrev_b32 v1, 6, %2\n"
    "v_and_b32 v255, 63, %2\n"
    "s_nop 0\n"
    "v_readfirstlane_b32 s85, v1\n"
    "s_and_b32 s89, %1, 7\n"
    "s_lshr_b32 s90, %1, 3\n"
    "s_and_b32 s84, s90, 7\n"
    "s_lshr_b32 s90, s90, 3\n"
    "s_lshl_b32 s89, s89, 2\n"
    "s_add_u32 s86, s89, s90\n"
    "s_lshl_b32 s68, s84, 18\n"
    "v_lshrrev_b32 v1, 4, v255\n"
    "v_and_b32 v2, 7, v255\n"
    "v_xor_b32 v1, v1, v2\n"
    "v_lshlrev_b32 v1, 4, v1\n"
    "v_lshrrev_b32 v3, 3, v255\n"
    "v_lshlrev_b32 v3, 11, v3\n"
    "s_lshl_b32 s89, s86, 2\n"
    "s_add_u32 s91, s89, s85\n"
    "s_lshl_b32 s89, s91, 16\n"
    "v_add3_u32 v233, v1, v3, s89\n"
    "v_xor_b32 v234, 64, v233\n"
    "v_add_u32 v234, 15360, v234\n"
    "v_add_u32 v235, 30720, v233\n"
    "v_add_u32 v236, 30720, v234\n"
    "s_lshl_b32 s88, s85, 12\n"
    "s_add_u32 s88, s88, %3\n"
    "s_lshl_b32 s87, s85, 11\n"
    "s_add_u32 s87, s87, %3\n"
    "v_lshrrev_b32 v1, 4, v255\n"
    "v_lshlrev_b32 v1, 1, v1\n"
    "v_xor_b32 v1, v1, v2\n"
    "v_lshlrev_b32 v1, 4, v1\n"
    "s_lshl_b32 s89, s85, 15\n"
    "s_lshl_b32 s93, s84, 8\n"
    "s_add_u32 s89, s89, s93\n"
    "v_add3_u32 v228, v1, v3, s89\n"
    "v_xor_b32 v229, 64, v228\n"
    "v_add_u32 v229, 15360, v229\n"
    "v_xor_b32 v230, 128, v228\n"
    "v_xor_b32 v231, 128, v229\n"
    "s_and_b32 s89, %1, 7\n"
    "s_lshl_b32 s89, s89, 2\n"
    "s_mov_b32 s56, 0x17654321\n"
    "s_mov_b32 s57, 0x00765432\n"
    "s_lshr_b64 s[58:59], s[56:57], s89\n"
    "s_sub_u32 s89, 28, s89\n"
    "s_lshr_b64 s[56:57], s[56:57], s89\n"
    "s_lshl_b32 s95, s56, 4\n"
    "s_mul_i32 s89, s84, 0x1111111\n"
    "s_add_u32 s58, s58, s89\n"
    "s_or_b32 s69, s68, 0x20000\n"
    "s_bfe_u32 s70, s58, 0x30000\n"
    "s_lshl_b32 s70, s70, 18\n"
    "s_or_b32 s71, s70, 0x20000\n"
    "s_bfe_u32 s72, s58, 0x30004\n"
    "s_lshl_b32 s72, s72, 18\n"
    "s_or_b32 s73, s72, 0x20000\n"
    "s_bfe_u32 s74, s58, 0x30008\n"
    "s_lshl_b32 s74, s74, 18\n"
    "s_or_b32 s75, s74, 0x20000\n"
    "s_bfe_u32 s76, s58, 0x3000c\n"
    "s_lshl_b32 s76, s76, 18\n"
    "s_or_b32 s77, s76, 0x20000\n"
    "s_bfe_u32 s78, s58, 0x30010\n"
    "s_lshl_b32 s78, s78, 18\n"
    "s_or_b32 s79, s78, 0x20000\n"
    "s_bfe_u32 s80, s58, 0x30014\n"
    "s_lshl_b32 s80, s80, 18\n"
    "s_or_b32 s81, s80, 0x20000\n"
    "s_bfe_u32 s82, s58, 0x30018\n"
    "s_lshl_b32 s82, s82, 18\n"
    "s_or_b32 s83, s82, 0x20000\n"
    "s_waitcnt lgkmcnt(0)\n"
    "s_mov_b64 s[48:49], s[20:21]\n"
    "s_and_b32 s49, s49, 0xffff\n"
    "s_mov_b32 s50, 8388608\n"
    "s_mov_b32 s51, 0x20000\n"
    "s_lshr_b32 s90, s68, 10\n"
    "s_add_u32 m0, s88, 90112\n"
    "s_nop 0\n"
    "buffer_load_dwordx4 v233, s[48:51], s90 offen offset:0 nt lds\n"
    "buffer_load_dwordx4 v234, s[48:51], s90 offen offset:1024 nt lds\n"
    "buffer_load_dwordx4 v235, s[48:51], s90 offen offset:2048 nt lds\n"
    "buffer_load_dwordx4 v236, s[48:51], s90 offen offset:3072 nt lds\n"
    "s_mov_b64 s[52:53], s[24:25]\n"
    "s_and_b32 s53, s53, 0xffff\n"
    "s_mov_b32 s54, 6144\n"
    "s_mov_b32 s55, 0x20000\n"
    "v_lshrrev_b32 v144, 4, v255\n"
    "v_lshlrev_b32 v144, 11, v144\n"
    "v_and_b32 v145, 15, v255\n"
    "v_lshl_add_u32 v144, v145, 4, v144\n"
    "buffer_load_dwordx4 v[132:135], v144, s[52:55], s93 offen\n"
    "s_mov_b64 s[36:37], s[22:23]\n"
    "s_and_b32 s37, s37, 0xffff\n"
    "s_mov_b32 s38, 2097152\n"
    "s_mov_b32 s39, 0x20000\n"
    "s_add_u32 m0, s87, 0\n"
    "s_nop 0\n"
    "buffer_load_dwordx4 v228, s[36:39], s68 offen offset:0 lds\n"
    "buffer_load_dwordx4 v229, s[36:39], s68 offen offset:1024 lds\n"
    "s_add_u32 m0, s87, 8192\n"
    "s_nop 0\n"
    "buffer_load_dwordx4 v230, s[36:39], s68 offen offset:0 lds\n"
    "buffer_load_dwordx4 v231, s[36:39], s68 offen offset:1024 lds\n"
    "s_lshl_b32 s89, %1, 2\n"
    "s_add_u32 s89, s89, s85\n"
    "s_lshl_b32 s89, s89, 11\n"
    "v_and_b32 v157, 15, v255\n"
    "v_lshl_add_u32 v157, v157, 7, s89\n"
    "v_lshrrev_b32 v146, 4, v255\n"
    "v_lshl_add_u32 v157, v146, 21, v157\n"
    "s_mov_b64 exec, 0xffff\n"
    "global_load_dword v156, v157, s[22:23]\n"
    "s_mov_b64 exec, -1\n"
    "s_mov_b64 s[64:65], s[28:29]\n"
    "s_and_b32 s65, s65, 0xffff\n"
    "s_mov_b32 s66, 131072\n"
    "s_mov_b32 s67, 0x20000\n"
    "v_lshlrev_b32 v252, 2, v2\n"
    "v_bfe_u32 v252, s95, v252, 4\n"
    "v_add_u32 v253, s84, v2\n"
    "v_and_b32 v253, 7, v253\n"
    "v_sub_u32 v1, 8, v2\n"
    "v_and_b32 v1, 7, v1\n"
    "v_lshlrev_b32 v1, 2, v1\n"
    "v_bfe_u32 v1, s95, v1, 4\n"
    "v_lshl_add_u32 v253, v253, 7, v1\n"
    "v_lshrrev_b32 v1, 3, v255\n"
    "v_lshl_add_u32 v253, v1, 20, v253\n"
    "s_lshl_b32 s92, s91, 10\n"
    "s_lshl_b32 s89, s84, 7\n"
    "s_add_u32 s91, s92, s89\n"
    "s_buffer_load_dwordx2 s[16:17], s[64:67], s91 glc\n"
    "v_lshrrev_b32 v1, 5, v255\n"
    "v_lshl_add_u32 v1, v1, 20, v255\n"
    "v_add_u32 v1, s89, v1\n"
    "v_add_u32 v158, 32, v253\n"
    "v_cmp_gt_u32 vcc, 8, v255\n"
    "v_cndmask_b32 v158, v1, v158, vcc\n"
    "v_cndmask_b32 v159, 0, 1, vcc\n"
    "v_lshlrev_b32 v162, 3, v252\n"
    "s_add_u32 m0, s87, 16384\n"
    "s_nop 0\n"
    "buffer_load_dwordx4 v228, s[36:39], s70 offen offset:0 lds\n"
    "buffer_load_dwordx4 v229, s[36:39], s70 offen offset:1024 lds\n"
    "s_add_u32 m0, s87, 24576\n"
    "s_nop 0\n"
    "buffer_load_dwordx4 v230, s[36:39], s70 offen offset:0 lds\n"
    "buffer_load_dwordx4 v231, s[36:39], s70 offen offset:1024 lds\n"
    "s_lshr_b32 s98, %1, 3\n"
    "s_lshl_b32 s98, s98, 2\n"
    "s_add_u32 s98, s98, s85\n"
    "s_lshl_b32 s98, s98, 11\n"
    "s_sub_u32 s89, 384, s22\n"
    "s_and_b32 s89, s89, 0x380\n"
    "s_lshr_b32 s99, s98, 17\n"
    "s_lshl_b32 s99, s99, 10\n"
    "s_add_u32 s89, s89, s99\n"
    "s_and_b32 s98, s98, 0x1ffff\n"
    "s_add_u32 s98, s98, s89\n"
    "s_lshr_b32 s89, s89, 8\n"
    "s_lshl_b32 s89, s89, 18\n"
    "s_add_u32 s98, s98, s89\n"
    "s_lshl_b32 s89, s84, 18\n"
    "s_sub_u32 s98, s98, s89\n"
    "s_mov_b32 s94, 0xfffff000\n"
    "v_lshrrev_b32 v148, 4, v255\n"
    "v_bfe_u32 v149, v255, 2, 2\n"
    "v_lshl_add_u32 v150, v148, 3, v149\n"
    "v_lshlrev_b32 v150, 7, v150\n"
    "v_and_b32 v151, 3, v255\n"
    "v_lshl_add_u32 v151, v151, 3, %3\n"
    "v_bfe_u32 v238, v255, 3, 1\n"
    "v_lshl_add_u32 v151, v238, 5, v151\n"
    "v_and_b32 v238, 1, v148\n"
    "v_lshl_add_u32 v237, v238, 6, v151\n"
    "v_add_u32 v237, v237, v150\n"
    "v_xor_b32 v238, 32, v237\n"
    "v_xor_b32 v239, 64, v237\n"
    "v_xor_b32 v240, 96, v237\n"
    "v_add_u32 v241, 0x10000, v237\n"
    "v_add_u32 v242, 0x10000, v238\n"
    "v_add_u32 v243, 0x10000, v239\n"
    "v_add_u32 v244, 0x10000, v240\n"
    "v_add_u32 v245, 0x20000, v237\n"
    "v_add_u32 v246, 0x20000, v238\n"
    "v_add_u32 v247, 0x20000, v239\n"
    "v_add_u32 v248, 0x20000, v240\n"
    "v_lshl_add_u32 v150, v148, 2, v149\n"
    "v_lshlrev_b32 v150, 7, v150\n"
    "v_lshrrev_b32 v149, 1, v148\n"
    "v_xor_b32 v149, v149, v148\n"
    "v_and_b32 v149, 1, v149\n"
    "v_lshl_add_u32 v149, v149, 6, v151\n"
    "v_add_u32 v3, v149, v150\n"
    "v_bfe_u32 v149, v255, 1, 3\n"
    "v_and_b32 v150, 15, v255\n"
    "v_lshlrev_b32 v150, 7, v150\n"
    "s_add_u32 s89, s88, 90112\n"
    "v_add_u32 v150, s89, v150\n"
    "v_xor_b32 v151, v148, v149\n"
    "v_lshl_add_u32 v249, v151, 4, v150\n"
    "v_xor_b32 v250, 64, v249\n"
    "v_lshrrev_b32 v151, 1, v148\n"
    "v_xor_b32 v151, v151, v149\n"
    "v_lshl_add_u32 v151, v151, 4, v150\n"
    "v_and_b32 v149, 1, v148\n"
    "v_lshl_add_u32 v251, v149, 3, v151\n"
    "s_lshr_b32 s89, s86, 3\n"
    "s_lshl_b32 s89, s89, 21\n"
    "s_and_b32 s93, s86, 7\n"
    "s_lshl_b32 s93, s93, 14\n"
    "s_add_u32 s89, s89, s93\n"
    "s_lshl_b32 s93, s85, 12\n"
    "s_add_u32 s89, s89, s93\n"
    "v_lshl_add_u32 v232, v255, 4, s89\n"
    "s_add_u32 s89, s87, 155648\n"
    "v_lshl_add_u32 v2, v148, 4, s89\n"
    "v_lshl_add_u32 v1, v255, 5, s89\n"
    "s_mov_b64 s[40:41], s[22:23]\n"
    "s_add_u32 s40, s40, 2097152\n"
    "s_addc_u32 s41, s41, 0\n"
    "s_and_b32 s41, s41, 0xffff\n"
    "s_mov_b32 s42, 2097152\n"
    "s_mov_b32 s43, 0x20000\n"
    "s_mov_b64 s[44:45], s[22:23]\n"
    "s_add_u32 s44, s44, 4194304\n"
    "s_addc_u32 s45, s45, 0\n"
    "s_and_b32 s45, s45, 0xffff\n"
    "s_mov_b32 s46, 2097152\n"
    "s_mov_b32 s47, 0x20000\n"
    "s_mov_b64 s[60:61], s[26:27]\n"
    "s_and_b32 s61, s61, 0xffff\n"
    "s_mov_b32 s62, 8388608\n"
    "s_mov_b32 s63, 0x20000\n"
    "s_mov_b64 s[56:57], s[28:29]\n"
    "s_add_u32 s56, s56, 9437184\n"
    "s_addc_u32 s57, s57, 0\n"
    "s_and_b32 s57, s57, 0xffff\n"
    "s_mov_b32 s58, 8388608\n"
    "s_mov_b32 s59, 0x20000\n"
    "s_mov_b64 s[52:53], s[28:29]\n"
    "s_add_u32 s52, s52, 1048576\n"
    "s_addc_u32 s53, s53, 0\n"
    "s_and_b32 s53, s53, 0xffff\n"
    "s_mov_b32 s54, 8388608\n"
    "s_mov_b32 s55, 0x20000\n"
    "s_waitcnt vmcnt(14)\n"
    "s_mov_b64 exec, 0\n"
    "s_cmp_eq_u32 s85, 0\n"
    "s_cbranch_scc1 L_ip1_%=\n"
    "s_cmp_eq_u32 s85, 1\n"
    "s_cbranch_scc1 L_ip2_%=\n"
    "s_cmp_eq_u32 s85, 2\n"
    "s_cbranch_scc1 L_ip3_%=\n"
    "s_branch L_ip4_%=\n"
    "L_ipback_%=:\n"
    "s_mov_b64 exec, -1\n"
    "s_lshr_b32 s90, s70, 10\n"
    "s_add_u32 m0, s88, 106496\n"
    "s_nop 0\n"
    "buffer_load_dwordx4 v233, s[48:51], s90 offen offset:0 lds\n"
    "buffer_load_dwordx4 v234, s[48:51], s90 offen offset:1024 lds\n"
    "buffer_load_dwordx4 v235, s[48:51], s90 offen offset:2048 lds\n"
    "buffer_load_dwordx4 v236, s[48:51], s90 offen offset:3072 lds\n"
    "s_mov_b64 s[28:29], s[22:23]\n"
    "s_mov_b64 s[10:11], 0\n"
    "s_mov_b64 s[12:13], 0\n"
    "s_mov_b64 s[14:15], 0\n"
    "s_mov_b64 s[18:19], 0\n"
    "s_mov_b64 s[20:21], 0\n"
    "s_mov_b64 s[22:23], 0\n"
    "s_waitcnt vmcnt(13)\n"
    "v_lshlrev_b32 v136, 16, v132\n"
    "v_and_b32 v137, 0xffff0000, v132\n"
    "v_lshlrev_b32 v138, 16, v133\n"
    "v_and_b32 v139, 0xffff0000, v133\n"
    "v_lshlrev_b32 v140, 16, v134\n"
    "v_and_b32 v141, 0xffff0000, v134\n"
    "v_lshlrev_b32 v142, 16, v135\n"
    "v_and_b32 v143, 0xffff0000, v135\n"
    "ds_write_b128 v1, v[136:139]\n"
    "ds_write_b128 v1, v[140:143] offset:16\n"
    "ds_read_b128 v[4:7], v2 offset:0\n"
    "ds_read_b128 v[12:15], v2 offset:64\n"
    "ds_read_b128 v[20:23], v2 offset:128\n"
    "ds_read_b128 v[28:31], v2 offset:192\n"
    "ds_read_b128 v[36:39], v2 offset:256\n"
    "ds_read_b128 v[44:47], v2 offset:320\n"
    "ds_read_b128 v[52:55], v2 offset:384\n"
    "ds_read_b128 v[60:63], v2 offset:448\n"
    "ds_read_b128 v[196:199], v249 offset:0\n"
    "ds_read_b128 v[200:203], v249 offset:2048\n"
    "ds_read_b128 v[204:207], v250 offset:0\n"
    "ds_read_b128 v[208:211], v250 offset:2048\n"
    "s_waitcnt vmcnt(9)\n"
    "s_barrier\n"
    "s_waitcnt lgkmcnt(0)\n"
    "ds_read_b64_tr_b16 a[0:1], v237 offset:0\n"
    "ds_read_b64_tr_b16 a[2:3], v239 offset:512\n"
    "ds_read_b64_tr_b16 a[4:5], v238 offset:0\n"
    "ds_read_b64_tr_b16 a[6:7], v240 offset:512\n"
    "ds_read_b64_tr_b16 a[8:9], v239 offset:0\n"
    "ds_read_b64_tr_b16 a[10:11], v237 offset:512\n"
    "ds_read_b64_tr_b16 a[12:13], v240 offset:0\n"
    "ds_read_b64_tr_b16 a[14:15], v238 offset:512\n"
    "ds_read_b64_tr_b16 a[16:17], v237 offset:4096\n"
    "ds_read_b64_tr_b16 a[18:19], v239 offset:4608\n"
    "ds_read_b64_tr_b16 a[20:21], v238 offset:4096\n"
    "ds_read_b64_tr_b16 a[22:23], v240 offset:4608\n"
    "s_waitcnt lgkmcnt(10)\n"
    "v_mfma_f32_16x16x32_bf16 v[8:11], a[0:3], v[200:203], v[4:7]\n"
    "ds_read_b64_tr_b16 a[24:25], v239 offset:4096\n"
    "v_xor_b32 v1, 0, v251\n"
    "v_mfma_f32_16x16x32_bf16 v[4:7], a[0:3], v[196:199], v[4:7]\n"
    "s_add_u32 m0, s87, 32768\n"
    "ds_read_b64_tr_b16 a[26:27], v237 offset:4608\n"
    "buffer_load_dwordx4 v228, s[36:39], s72 offen offset:0 lds\n"
    "s_waitcnt lgkmcnt(10)\n"
    "v_mfma_f32_16x16x32_bf16 v[16:19], a[4:7], v[200:203], v[12:15]\n"
    "ds_read_b64_tr_b16 a[28:29], v240 offset:4096\n"
    "ds_read_b64 v[164:165], v1 offset:0\n"
    "v_mfma_f32_16x16x32_bf16 v[12:15], a[4:7], v[196:199], v[12:15]\n"
    "s_waitcnt lgkmcnt(8)\n"
    "ds_read_b64_tr_b16 a[30:31], v238 offset:4608\n"
    "ds_read_b64 v[166:167], v1 offset:2048\n"
    "buffer_load_dwordx4 v229, s[36:39], s72 offen offset:1024 lds\n"
    "v_mfma_f32_16x16x32_bf16 v[24:27], a[8:11], v[200:203], v[20:23]\n"
    "ds_read_b64_tr_b16 a[0:1], v237 offset:8192\n"
    "v_xor_b32 v1, 32, v251\n"
    "v_mfma_f32_16x16x32_bf16 v[20:23], a[8:11], v[196:199], v[20:23]\n"
    "s_add_u32 m0, s87, 40960\n"
    "ds_read_b64_tr_b16 a[2:3], v239 offset:8704\n"
    "buffer_load_dwordx4 v230, s[36:39], s72 offen offset:0 lds\n"
    "v_mfma_f32_16x16x32_bf16 v[32:35], a[12:15], v[200:203], v[28:31]\n"
    "ds_read_b64_tr_b16 a[4:5], v238 offset:8192\n"
    "ds_read_b64 v[168:169], v1 offset:0\n"
    "v_mfma_f32_16x16x32_bf16 v[28:31], a[12:15], v[196:199], v[28:31]\n"
    "s_waitcnt lgkmcnt(10)\n"
    "ds_read_b64_tr_b16 a[6:7], v240 offset:8704\n"
    "ds_read_b64 v[170:171], v1 offset:2048\n"
    "buffer_load_dwordx4 v231, s[36:39], s72 offen offset:1024 lds\n"
    "v_mfma_f32_16x16x32_bf16 v[4:7], a[16:19], v[204:207], v[4:7]\n"
    "s_add_u32 m0, s88, 122880\n"
    "ds_read_b64_tr_b16 a[8:9], v239 offset:8192\n"
    "v_xor_b32 v1, 64, v251\n"
    "v_mfma_f32_16x16x32_bf16 v[8:11], a[16:19], v[208:211], v[8:11]\n"
    "s_lshr_b32 s90, s72, 10\n"
    "ds_read_b64_tr_b16 a[10:11], v237 offset:8704\n"
    "buffer_load_dwordx4 v233, s[48:51], s90 offen offset:0 lds\n"
    "v_mfma_f32_16x16x32_bf16 v[12:15], a[20:23], v[204:207], v[12:15]\n"
    "ds_read_b64_tr_b16 a[12:13], v240 offset:8192\n"
    "ds_read_b64 v[172:173], v1 offset:0\n"
    "v_mfma_f32_16x16x32_bf16 v[16:19], a[20:23], v[208:211], v[16:19]\n"
    "s_waitcnt lgkmcnt(11)\n"
    "ds_read_b64_tr_b16 a[14:15], v238 offset:8704\n"
    "ds_read_b64 v[174:175], v1 offset:2048\n"
    "buffer_load_dwordx4 v234, s[48:51], s90 offen offset:1024 lds\n"
    "v_mfma_f32_16x16x32_bf16 v[20:23], a[24:27], v[204:207], v[20:23]\n"
    "ds_read_b64_tr_b16 a[16:17], v237 offset:12288\n"
    "v_xor_b32 v1, 96, v251\n"
    "v_mfma_f32_16x16x32_bf16 v[24:27], a[24:27], v[208:211], v[24:27]\n"
    "ds_read_b64_tr_b16 a[18:19], v239 offset:12800\n"
    "buffer_load_dwordx4 v235, s[48:51], s90 offen offset:2048 lds\n"
    "v_mfma_f32_16x16x32_bf16 v[28:31], a[28:31], v[204:207], v[28:31]\n"
    "ds_read_b64_tr_b16 a[20:21], v238 offset:12288\n"
    "ds_read_b64 v[176:177], v1 offset:0\n"
    "v_mfma_f32_16x16x32_bf16 v[32:35], a[28:31], v[208:211], v[32:35]\n"
    "s_waitcnt vmcnt(7) lgkmcnt(11)\n"
    "ds_read_b64_tr_b16 a[22:23], v240 offset:12800\n"
    "ds_read_b64 v[178:179], v1 offset:2048\n"
    "buffer_load_dwordx4 v236, s[48:51], s90 offen offset:3072 lds\n"
    "v_mfma_f32_16x16x32_bf16 v[40:43], a[0:3], v[200:203], v[36:39]\n"
    "ds_read_b64_tr_b16 a[24:25], v239 offset:12288\n"
    "ds_read_b128 a[32:35], v249 offset:16384\n"
    "v_mfma_f32_16x16x32_bf16 v[36:39], a[0:3], v[196:199], v[36:39]\n"
    "s_add_u32 m0, s87, 49152\n"
    "ds_read_b64_tr_b16 a[26:27], v237 offset:12800\n"
    "buffer_load_dwordx4 v228, s[36:39], s74 offen offset:0 lds\n"
    "v_mfma_f32_16x16x32_bf16 v[48:51], a[4:7], v[200:203], v[44:47]\n"
    "ds_read_b64_tr_b16 a[28:29], v240 offset:12288\n"
    "ds_read_b128 a[36:39], v249 offset:18432\n"
    "v_mfma_f32_16x16x32_bf16 v[44:47], a[4:7], v[196:199], v[44:47]\n"
    "s_waitcnt lgkmcnt(12)\n"
    "ds_read_b64_tr_b16 a[30:31], v238 offset:12800\n"
    "ds_read_b128 a[40:43], v250 offset:16384\n"
    "buffer_load_dwordx4 v229, s[36:39], s74 offen offset:1024 lds\n"
    "v_mfma_f32_16x16x32_bf16 v[56:59], a[8:11], v[200:203], v[52:55]\n"
    "s_barrier\n"
    "ds_read_b64_tr_b16 a[0:1], v237 offset:16384\n"
    "ds_read_b128 a[44:47], v250 offset:18432\n"
    "v_mfma_f32_16x16x32_bf16 v[52:55], a[8:11], v[196:199], v[52:55]\n"
    "s_add_u32 m0, s87, 57344\n"
    "ds_read_b64_tr_b16 a[2:3], v239 offset:16896\n"
    "buffer_load_dwordx4 v230, s[36:39], s74 offen offset:0 lds\n"
    "v_mfma_f32_16x16x32_bf16 v[64:67], a[12:15], v[200:203], v[60:63]\n"
    "ds_read_b64_tr_b16 a[4:5], v238 offset:16384\n"
    "v_mfma_f32_16x16x32_bf16 v[60:63], a[12:15], v[196:199], v[60:63]\n"
    "s_waitcnt lgkmcnt(11)\n"
    "ds_read_b64_tr_b16 a[6:7], v240 offset:16896\n"
    "buffer_load_dwordx4 v231, s[36:39], s74 offen offset:1024 lds\n"
    "v_mfma_f32_16x16x32_bf16 v[36:39], a[16:19], v[204:207], v[36:39]\n"
    "s_add_u32 m0, s88, 139264\n"
    "ds_read_b64_tr_b16 a[8:9], v239 offset:16384\n"
    "v_lshlrev_b32 v68, 16, v164\n"
    "v_mfma_f32_16x16x32_bf16 v[40:43], a[16:19], v[208:211], v[40:43]\n"
    "s_lshr_b32 s90, s74, 10\n"
    "ds_read_b64_tr_b16 a[10:11], v237 offset:16896\n"
    "buffer_load_dwordx4 v233, s[48:51], s90 offen offset:0 lds\n"
    "v_mfma_f32_16x16x32_bf16 v[44:47], a[20:23], v[204:207], v[44:47]\n"
    "ds_read_b64_tr_b16 a[12:13], v240 offset:16384\n"
    "v_and_b32 v69, 0xffff0000, v164\n"
    "v_mfma_f32_16x16x32_bf16 v[48:51], a[20:23], v[208:211], v[48:51]\n"
    "s_waitcnt lgkmcnt(6)\n"
    "ds_read_b64_tr_b16 a[14:15], v238 offset:16896\n"
    "buffer_load_dwordx4 v234, s[48:51], s90 offen offset:1024 lds\n"
    "v_mfma_f32_16x16x32_bf16 v[52:55], a[24:27], v[204:207], v[52:55]\n"
    "ds_read_b64_tr_b16 a[16:17], v237 offset:20480\n"
    "v_lshlrev_b32 v70, 16, v165\n"
    "v_mfma_f32_16x16x32_bf16 v[56:59], a[24:27], v[208:211], v[56:59]\n"
    "ds_read_b64_tr_b16 a[18:19], v239 offset:20992\n"
    "buffer_load_dwordx4 v235, s[48:51], s90 offen offset:2048 lds\n"
    "v_mfma_f32_16x16x32_bf16 v[60:63], a[28:31], v[204:207], v[60:63]\n"
    "ds_read_b64_tr_b16 a[20:21], v238 offset:20480\n"
    "v_and_b32 v71, 0xffff0000, v165\n"
    "v_mfma_f32_16x16x32_bf16 v[64:67], a[28:31], v[208:211], v[64:67]\n"
    "s_waitcnt lgkmcnt(7)\n"
    "ds_read_b64_tr_b16 a[22:23], v240 offset:20992\n"
    "buffer_load_dwordx4 v236, s[48:51], s90 offen offset:3072 lds\n"
    "v_mfma_f32_16x16x32_bf16 v[4:7], a[0:3], a[32:35], v[4:7]\n"
    "ds_read_b64_tr_b16 a[24:25], v239 offset:20480\n"
    "v_lshlrev_b32 v72, 16, v166\n"
    "v_mfma_f32_16x16x32_bf16 v[8:11], a[0:3], a[36:39], v[8:11]\n"
    "s_add_u32 m0, s87, 65536\n"
    "ds_read_b64_tr_b16 a[26:27], v237 offset:20992\n"
    "buffer_load_dwordx4 v228, s[36:39], s76 offen offset:0 lds\n"
    "v_mfma_f32_16x16x32_bf16 v[12:15], a[4:7], a[32:35], v[12:15]\n"
    "ds_read_b64_tr_b16 a[28:29], v240 offset:20480\n"
    "v_and_b32 v73, 0xffff0000, v166\n"
    "v_mfma_f32_16x16x32_bf16 v[16:19], a[4:7], a[36:39], v[16:19]\n"
    "s_waitcnt lgkmcnt(7)\n"
    "ds_read_b64_tr_b16 a[30:31], v238 offset:20992\n"
    "buffer_load_dwordx4 v229, s[36:39], s76 offen offset:1024 lds\n"
    "v_mfma_f32_16x16x32_bf16 v[20:23], a[8:11], a[32:35], v[20:23]\n"
    "ds_read_b64_tr_b16 a[0:1], v237 offset:24576\n"
    "v_lshlrev_b32 v74, 16, v167\n"
    "v_mfma_f32_16x16x32_bf16 v[24:27], a[8:11], a[36:39], v[24:27]\n"
    "s_add_u32 m0, s87, 73728\n"
    "ds_read_b64_tr_b16 a[2:3], v239 offset:25088\n"
    "buffer_load_dwordx4 v230, s[36:39], s76 offen offset:0 lds\n"
    "v_mfma_f32_16x16x32_bf16 v[28:31], a[12:15], a[32:35], v[28:31]\n"
    "ds_read_b64_tr_b16 a[4:5], v238 offset:24576\n"
    "v_and_b32 v75, 0xffff0000, v167\n"
    "v_mfma_f32_16x16x32_bf16 v[32:35], a[12:15], a[36:39], v[32:35]\n"
    "s_waitcnt lgkmcnt(7)\n"
    "ds_read_b64_tr_b16 a[6:7], v240 offset:25088\n"
    "buffer_load_dwordx4 v231, s[36:39], s76 offen offset:1024 lds\n"
    "v_mfma_f32_16x16x32_bf16 v[4:7], a[16:19], a[40:43], v[4:7]\n"
    "s_not_b64 s[26:27], s[16:17]\n"
    "ds_read_b64_tr_b16 a[8:9], v239 offset:24576\n"
    "v_lshrrev_b64 v[160:161], v162, s[26:27]\n"
    "v_and_b32 v255, v159, v160\n"
    "v_mfma_f32_16x16x32_bf16 v[8:11], a[16:19], a[44:47], v[8:11]\n"
    "s_lshr_b32 s90, s76, 10\n"
    "s_add_u32 m0, s88, 90112\n"
    "ds_read_b64_tr_b16 a[10:11], v237 offset:25088\n"
    "buffer_load_dwordx4 v233, s[48:51], s90 offen offset:0 lds\n"
    "v_mfma_f32_16x16x32_bf16 v[12:15], a[20:23], a[40:43], v[12:15]\n"
    "ds_read_b64_tr_b16 a[12:13], v240 offset:24576\n"
    "buffer_store_byte v255, v158, s[64:67], s92 offen sc1\n"
    "v_mfma_f32_16x16x32_bf16 v[16:19], a[20:23], a[44:47], v[16:19]\n"
    "s_waitcnt lgkmcnt(7)\n"
    "ds_read_b64_tr_b16 a[14:15], v238 offset:25088\n"
    "buffer_load_dwordx4 v234, s[48:51], s90 offen offset:1024 lds\n"
    "v_mfma_f32_16x16x32_bf16 v[20:23], a[24:27], a[40:43], v[20:23]\n"
    "ds_read_b64_tr_b16 a[16:17], v237 offset:28672\n"
    "v_lshlrev_b32 v76, 16, v168\n"
    "v_mfma_f32_16x16x32_bf16 v[24:27], a[24:27], a[44:47], v[24:27]\n"
    "ds_read_b64_tr_b16 a[18:19], v239 offset:29184\n"
    "buffer_load_dwordx4 v235, s[48:51], s90 offen offset:2048 lds\n"
    "v_mfma_f32_16x16x32_bf16 v[28:31], a[28:31], a[40:43], v[28:31]\n"
    "ds_read_b64_tr_b16 a[20:21], v238 offset:28672\n"
    "v_and_b32 v77, 0xffff0000, v168\n"
    "v_mfma_f32_16x16x32_bf16 v[32:35], a[28:31], a[44:47], v[32:35]\n"
    "s_waitcnt vmcnt(16) lgkmcnt(7)\n"
    "ds_read_b64_tr_b16 a[22:23], v240 offset:29184\n"
    "ds_read_b128 a[48:51], v249 offset:32768\n"
    "buffer_load_dwordx4 v236, s[48:51], s90 offen offset:3072 lds\n"
    "v_mfma_f32_16x16x32_bf16 v[36:39], a[0:3], a[32:35], v[36:39]\n"
    "ds_read_b64_tr_b16 a[24:25], v239 offset:28672\n"
    "ds_read_b128 a[52:55], v249 offset:34816\n"
    "v_mfma_f32_16x16x32_bf16 v[40:43], a[0:3], a[36:39], v[40:43]\n"
    "s_add_u32 m0, s87, 81920\n"
    "ds_read_b64_tr_b16 a[26:27], v237 offset:29184\n"
    "buffer_load_dwordx4 v228, s[36:39], s69 offen offset:0 lds\n"
    "v_mfma_f32_16x16x32_bf16 v[44:47], a[4:7], a[32:35], v[44:47]\n"
    "ds_read_b64_tr_b16 a[28:29], v240 offset:28672\n"
    "ds_read_b128 a[56:59], v250 offset:32768\n"
    "v_mfma_f32_16x16x32_bf16 v[48:51], a[4:7], a[36:39], v[48:51]\n"
    "s_waitcnt lgkmcnt(10)\n"
    "ds_read_b64_tr_b16 a[30:31], v238 offset:29184\n"
    "ds_read_b128 a[60:63], v250 offset:34816\n"
    "buffer_load_dwordx4 v229, s[36:39], s69 offen offset:1024 lds\n"
    "v_mfma_f32_16x16x32_bf16 v[52:55], a[8:11], a[32:35], v[52:55]\n"
    "s_barrier\n"
    "ds_read_b64_tr_b16 a[0:1], v237 offset:32768\n"
    "v_lshlrev_b32 v78, 16, v169\n"
    "v_mfma_f32_16x16x32_bf16 v[56:59], a[8:11], a[36:39], v[56:59]\n"
    "s_add_u32 m0, s87, 0\n"
    "ds_read_b64_tr_b16 a[2:3], v239 offset:33280\n"
    "buffer_load_dwordx4 v230, s[36:39], s69 offen offset:0 lds\n"
    "v_mfma_f32_16x16x32_bf16 v[60:63], a[12:15], a[32:35], v[60:63]\n"
    "ds_read_b64_tr_b16 a[4:5], v238 offset:32768\n"
    "v_and_b32 v79, 0xffff0000, v169\n"
    "v_mfma_f32_16x16x32_bf16 v[64:67], a[12:15], a[36:39], v[64:67]\n"
    "s_waitcnt lgkmcnt(11)\n"
    "ds_read_b64_tr_b16 a[6:7], v240 offset:33280\n"
    "buffer_load_dwordx4 v231, s[36:39], s69 offen offset:1024 lds\n"
    "v_mfma_f32_16x16x32_bf16 v[36:39], a[16:19], a[40:43], v[36:39]\n"
    "s_add_u32 m0, s88, 106496\n"
    "ds_read_b64_tr_b16 a[8:9], v239 offset:32768\n"
    "v_lshlrev_b32 v80, 16, v170\n"
    "v_mfma_f32_16x16x32_bf16 v[40:43], a[16:19], a[44:47], v[40:43]\n"
    "s_lshr_b32 s90, s69, 10\n"
    "ds_read_b64_tr_b16 a[10:11], v237 offset:33280\n"
    "buffer_load_dwordx4 v233, s[48:51], s90 offen offset:0 nt lds\n"
    "v_mfma_f32_16x16x32_bf16 v[44:47], a[20:23], a[40:43], v[44:47]\n"
    "ds_read_b64_tr_b16 a[12:13], v240 offset:32768\n"
    "v_and_b32 v81, 0xffff0000, v170\n"
    "v_mfma_f32_16x16x32_bf16 v[48:51], a[20:23], a[44:47], v[48:51]\n"
    "s_waitcnt lgkmcnt(7)\n"
    "ds_read_b64_tr_b16 a[14:15], v238 offset:33280\n"
    "buffer_load_dwordx4 v234, s[48:51], s90 offen offset:1024 nt lds\n"
    "v_mfma_f32_16x16x32_bf16 v[52:55], a[24:27], a[40:43], v[52:55]\n"
    "ds_read_b64_tr_b16 a[16:17], v237 offset:36864\n"
    "v_lshlrev_b32 v82, 16, v171\n"
    "v_mfma_f32_16x16x32_bf16 v[56:59], a[24:27], a[44:47], v[56:59]\n"
    "ds_read_b64_tr_b16 a[18:19], v239 offset:37376\n"
    "buffer_load_dwordx4 v235, s[48:51], s90 offen offset:2048 nt lds\n"
    "v_mfma_f32_16x16x32_bf16 v[60:63], a[28:31], a[40:43], v[60:63]\n"
    "ds_read_b64_tr_b16 a[20:21], v238 offset:36864\n"
    "v_and_b32 v83, 0xffff0000, v171\n"
    "v_mfma_f32_16x16x32_bf16 v[64:67], a[28:31], a[44:47], v[64:67]\n"
    "s_waitcnt lgkmcnt(7)\n"
    "ds_read_b64_tr_b16 a[22:23], v240 offset:37376\n"
    "buffer_load_dwordx4 v236, s[48:51], s90 offen offset:3072 nt lds\n"
    "v_mfma_f32_16x16x32_bf16 v[4:7], a[0:3], a[48:51], v[4:7]\n"
    "ds_read_b64_tr_b16 a[24:25], v239 offset:36864\n"
    "v_lshlrev_b32 v84, 16, v172\n"
    "v_mfma_f32_16x16x32_bf16 v[8:11], a[0:3], a[52:55], v[8:11]\n"
    "s_add_u32 m0, s87, 8192\n"
    "ds_read_b64_tr_b16 a[26:27], v237 offset:37376\n"
    "buffer_load_dwordx4 v228, s[36:39], s78 offen offset:0 lds\n"
    "v_mfma_f32_16x16x32_bf16 v[12:15], a[4:7], a[48:51], v[12:15]\n"
    "ds_read_b64_tr_b16 a[28:29], v240 offset:36864\n"
    "v_and_b32 v85, 0xffff0000, v172\n"
    "v_mfma_f32_16x16x32_bf16 v[16:19], a[4:7], a[52:55], v[16:19]\n"
    "s_waitcnt lgkmcnt(7)\n"
    "ds_read_b64_tr_b16 a[30:31], v238 offset:37376\n"
    "buffer_load_dwordx4 v229, s[36:39], s78 offen offset:1024 lds\n"
    "v_mfma_f32_16x16x32_bf16 v[20:23], a[8:11], a[48:51], v[20:23]\n"
    "ds_read_b64_tr_b16 a[0:1], v237 offset:40960\n"
    "v_lshlrev_b32 v86, 16, v173\n"
    "v_mfma_f32_16x16x32_bf16 v[24:27], a[8:11], a[52:55], v[24:27]\n"
    "s_add_u32 m0, s87, 16384\n"
    "ds_read_b64_tr_b16 a[2:3], v239 offset:41472\n"
    "buffer_load_dwordx4 v228, s[36:39], s80 offen offset:0 lds\n"
    "v_mfma_f32_16x16x32_bf16 v[28:31], a[12:15], a[48:51], v[28:31]\n"
    "ds_read_b64_tr_b16 a[4:5], v238 offset:40960\n"
    "v_and_b32 v87, 0xffff0000, v173\n"
    "v_mfma_f32_16x16x32_bf16 v[32:35], a[12:15], a[52:55], v[32:35]\n"
    "s_waitcnt lgkmcnt(7)\n"
    "ds_read_b64_tr_b16 a[6:7], v240 offset:41472\n"
    "buffer_load_dwordx4 v229, s[36:39], s80 offen offset:1024 lds\n"
    "v_mfma_f32_16x16x32_bf16 v[4:7], a[16:19], a[56:59], v[4:7]\n"
    "s_add_u32 m0, s88, 122880\n"
    "ds_read_b64_tr_b16 a[8:9], v239 offset:40960\n"
    "v_lshlrev_b32 v88, 16, v174\n"
    "v_mfma_f32_16x16x32_bf16 v[8:11], a[16:19], a[60:63], v[8:11]\n"
    "s_lshr_b32 s90, s78, 10\n"
    "ds_read_b64_tr_b16 a[10:11], v237 offset:41472\n"
    "buffer_load_dwordx4 v233, s[48:51], s90 offen offset:0 lds\n"
    "v_mfma_f32_16x16x32_bf16 v[12:15], a[20:23], a[56:59], v[12:15]\n"
    "ds_read_b64_tr_b16 a[12:13], v240 offset:40960\n"
    "v_and_b32 v89, 0xffff0000, v174\n"
    "v_mfma_f32_16x16x32_bf16 v[16:19], a[20:23], a[60:63], v[16:19]\n"
    "s_waitcnt lgkmcnt(7)\n"
    "ds_read_b64_tr_b16 a[14:15], v238 offset:41472\n"
    "buffer_load_dwordx4 v234, s[48:51], s90 offen offset:1024 lds\n"
    "v_mfma_f32_16x16x32_bf16 v[20:23], a[24:27], a[56:59], v[20:23]\n"
    "ds_read_b64_tr_b16 a[16:17], v237 offset:45056\n"
    "v_lshlrev_b32 v90, 16, v175\n"
    "v_mfma_f32_16x16x32_bf16 v[24:27], a[24:27], a[60:63], v[24:27]\n"
    "ds_read_b64_tr_b16 a[18:19], v239 offset:45568\n"
    "buffer_load_dwordx4 v235, s[48:51], s90 offen offset:2048 lds\n"
    "v_mfma_f32_16x16x32_bf16 v[28:31], a[28:31], a[56:59], v[28:31]\n"
    "ds_read_b64_tr_b16 a[20:21], v238 offset:45056\n"
    "v_and_b32 v91, 0xffff0000, v175\n"
    "v_mfma_f32_16x16x32_bf16 v[32:35], a[28:31], a[60:63], v[32:35]\n"
    "s_waitcnt vmcnt(24) lgkmcnt(7)\n"
    "ds_read_b64_tr_b16 a[22:23], v240 offset:45568\n"
    "ds_read_b128 a[64:67], v249 offset:49152\n"
    "buffer_load_dwordx4 v236, s[48:51], s90 offen offset:3072 lds\n"
    "v_mfma_f32_16x16x32_bf16 v[36:39], a[0:3], a[48:51], v[36:39]\n"
    "ds_read_b64_tr_b16 a[24:25], v239 offset:45056\n"
    "ds_read_b128 a[68:71], v249 offset:51200\n"
    "v_mfma_f32_16x16x32_bf16 v[40:43], a[0:3], a[52:55], v[40:43]\n"
    "L_ip1_%=:\n"
    "s_cbranch_execz L_ip5_%=\n"
    "ds_read_b64_tr_b16 a[26:27], v237 offset:45568\n"
    "v_mfma_f32_16x16x32_bf16 v[44:47], a[4:7], a[48:51], v[44:47]\n"
    "ds_read_b64_tr_b16 a[28:29], v240 offset:45056\n"
    "ds_read_b128 a[72:75], v250 offset:49152\n"
    "v_mfma_f32_16x16x32_bf16 v[48:51], a[4:7], a[52:55], v[48:51]\n"
    "s_waitcnt lgkmcnt(10)\n"
    "ds_read_b64_tr_b16 a[30:31], v238 offset:45568\n"
    "ds_read_b128 a[76:79], v250 offset:51200\n"
    "v_mfma_f32_16x16x32_bf16 v[52:55], a[8:11], a[48:51], v[52:55]\n"
    "s_barrier\n"
    "ds_read_b64_tr_b16 a[0:1], v237 offset:49152\n"
    "v_lshlrev_b32 v92, 16, v176\n"
    "v_mfma_f32_16x16x32_bf16 v[56:59], a[8:11], a[52:55], v[56:59]\n"
    "s_add_u32 m0, s87, 24576\n"
    "ds_read_b64_tr_b16 a[2:3], v239 offset:49664\n"
    "buffer_load_dwordx4 v228, s[36:39], s82 offen offset:0 lds\n"
    "v_mfma_f32_16x16x32_bf16 v[60:63], a[12:15], a[48:51], v[60:63]\n"
    "ds_read_b64_tr_b16 a[4:5], v238 offset:49152\n"
    "v_and_b32 v93, 0xffff0000, v176\n"
    "v_mfma_f32_16x16x32_bf16 v[64:67], a[12:15], a[52:55], v[64:67]\n"
    "s_waitcnt lgkmcnt(11)\n"
    "ds_read_b64_tr_b16 a[6:7], v240 offset:49664\n"
    "buffer_load_dwordx4 v229, s[36:39], s82 offen offset:1024 lds\n"
    "v_mfma_f32_16x16x32_bf16 v[36:39], a[16:19], a[56:59], v[36:39]\n"
    "ds_read_b64_tr_b16 a[8:9], v239 offset:49152\n"
    "v_lshlrev_b32 v94, 16, v177\n"
    "v_mfma_f32_16x16x32_bf16 v[40:43], a[16:19], a[60:63], v[40:43]\n"
    "s_add_u32 m0, s87, 32768\n"
    "ds_read_b64_tr_b16 a[10:11], v237 offset:49664\n"
    "buffer_load_dwordx4 v228, s[36:39], s71 offen offset:0 lds\n"
    "v_mfma_f32_16x16x32_bf16 v[44:47], a[20:23], a[56:59], v[44:47]\n"
    "ds_read_b64_tr_b16 a[12:13], v240 offset:49152\n"
    "v_and_b32 v95, 0xffff0000, v177\n"
    "v_mfma_f32_16x16x32_bf16 v[48:51], a[20:23], a[60:63], v[48:51]\n"
    "s_waitcnt lgkmcnt(7)\n"
    "ds_read_b64_tr_b16 a[14:15], v238 offset:49664\n"
    "buffer_load_dwordx4 v229, s[36:39], s71 offen offset:1024 lds\n"
    "v_mfma_f32_16x16x32_bf16 v[52:55], a[24:27], a[56:59], v[52:55]\n"
    "s_add_u32 m0, s88, 139264\n"
    "ds_read_b64_tr_b16 a[16:17], v237 offset:53248\n"
    "v_lshlrev_b32 v96, 16, v178\n"
    "v_mfma_f32_16x16x32_bf16 v[56:59], a[24:27], a[60:63], v[56:59]\n"
    "s_lshr_b32 s90, s80, 10\n"
    "ds_read_b64_tr_b16 a[18:19], v239 offset:53760\n"
    "buffer_load_dwordx4 v233, s[48:51], s90 offen offset:0 lds\n"
    "v_mfma_f32_16x16x32_bf16 v[60:63], a[28:31], a[56:59], v[60:63]\n"
    "ds_read_b64_tr_b16 a[20:21], v238 offset:53248\n"
    "v_and_b32 v97, 0xffff0000, v178\n"
    "v_mfma_f32_16x16x32_bf16 v[64:67], a[28:31], a[60:63], v[64:67]\n"
    "s_waitcnt vmcnt(21) lgkmcnt(7)\n"
    "ds_read_b64_tr_b16 a[22:23], v240 offset:53760\n"
    "ds_read_b128 a[80:83], v249 offset:0\n"
    "buffer_load_dwordx4 v234, s[48:51], s90 offen offset:1024 lds\n"
    "v_mfma_f32_16x16x32_bf16 v[4:7], a[0:3], a[64:67], v[4:7]\n"
    "ds_read_b64_tr_b16 a[24:25], v239 offset:53248\n"
    "ds_read_b128 a[84:87], v249 offset:2048\n"
    "v_mfma_f32_16x16x32_bf16 v[8:11], a[0:3], a[68:71], v[8:11]\n"
    "ds_read_b64_tr_b16 a[26:27], v237 offset:53760\n"
    "buffer_load_dwordx4 v235, s[48:51], s90 offen offset:2048 lds\n"
    "v_mfma_f32_16x16x32_bf16 v[12:15], a[4:7], a[64:67], v[12:15]\n"
    "ds_read_b64_tr_b16 a[28:29], v240 offset:53248\n"
    "ds_read_b128 a[88:91], v250 offset:0\n"
    "v_mfma_f32_16x16x32_bf16 v[16:19], a[4:7], a[68:71], v[16:19]\n"
    "s_waitcnt lgkmcnt(10)\n"
    "ds_read_b64_tr_b16 a[30:31], v238 offset:53760\n"
    "ds_read_b128 a[92:95], v250 offset:2048\n"
    "buffer_load_dwordx4 v236, s[48:51], s90 offen offset:3072 lds\n"
    "v_mfma_f32_16x16x32_bf16 v[20:23], a[8:11], a[64:67], v[20:23]\n"
    "ds_read_b64_tr_b16 a[0:1], v237 offset:57344\n"
    "v_lshlrev_b32 v98, 16, v179\n"
    "v_mfma_f32_16x16x32_bf16 v[24:27], a[8:11], a[68:71], v[24:27]\n"
    "ds_read_b64_tr_b16 a[2:3], v239 offset:57856\n"
    "v_mfma_f32_16x16x32_bf16 v[28:31], a[12:15], a[64:67], v[28:31]\n"
    "ds_read_b64_tr_b16 a[4:5], v238 offset:57344\n"
    "v_and_b32 v99, 0xffff0000, v179\n"
    "v_mfma_f32_16x16x32_bf16 v[32:35], a[12:15], a[68:71], v[32:35]\n"
    "s_waitcnt lgkmcnt(11)\n"
    "ds_read_b64_tr_b16 a[6:7], v240 offset:57856\n"
    "v_mfma_f32_16x16x32_bf16 v[4:7], a[16:19], a[72:75], v[4:7]\n"
    "ds_read_b64_tr_b16 a[8:9], v239 offset:57344\n"
    "v_mfma_f32_16x16x32_bf16 v[8:11], a[16:19], a[76:79], v[8:11]\n"
    "ds_read_b64_tr_b16 a[10:11], v237 offset:57856\n"
    "v_mfma_f32_16x16x32_bf16 v[12:15], a[20:23], a[72:75], v[12:15]\n"
    "ds_read_b64_tr_b16 a[12:13], v240 offset:57344\n"
    "v_mfma_f32_16x16x32_bf16 v[16:19], a[20:23], a[76:79], v[16:19]\n"
    "s_waitcnt lgkmcnt(7)\n"
    "ds_read_b64_tr_b16 a[14:15], v238 offset:57856\n"
    "v_mfma_f32_16x16x32_bf16 v[20:23], a[24:27], a[72:75], v[20:23]\n"
    "s_add_u32 m0, s88, 90112\n"
    "ds_read_b64_tr_b16 a[16:17], v237 offset:61440\n"
    "v_mfma_f32_16x16x32_bf16 v[24:27], a[24:27], a[76:79], v[24:27]\n"
    "s_lshr_b32 s90, s82, 10\n"
    "ds_read_b64_tr_b16 a[18:19], v239 offset:61952\n"
    "buffer_load_dwordx4 v233, s[48:51], s90 offen offset:0 lds\n"
    "v_mfma_f32_16x16x32_bf16 v[28:31], a[28:31], a[72:75], v[28:31]\n"
    "ds_read_b64_tr_b16 a[20:21], v238 offset:61440\n"
    "v_mfma_f32_16x16x32_bf16 v[32:35], a[28:31], a[76:79], v[32:35]\n"
    "s_waitcnt vmcnt(17) lgkmcnt(7)\n"
    "ds_read_b64_tr_b16 a[22:23], v240 offset:61952\n"
    "ds_read_b128 v[212:215], v249 offset:16384\n"
    "buffer_load_dwordx4 v234, s[48:51], s90 offen offset:1024 lds\n"
    "v_mfma_f32_16x16x32_bf16 v[36:39], a[0:3], a[64:67], v[36:39]\n"
    "ds_read_b64_tr_b16 a[24:25], v239 offset:61440\n"
    "ds_read_b128 v[216:219], v249 offset:18432\n"
    "v_mfma_f32_16x16x32_bf16 v[40:43], a[0:3], a[68:71], v[40:43]\n"
    "ds_read_b64_tr_b16 a[26:27], v237 offset:61952\n"
    "buffer_load_dwordx4 v235, s[48:51], s90 offen offset:2048 lds\n"
    "v_mfma_f32_16x16x32_bf16 v[44:47], a[4:7], a[64:67], v[44:47]\n"
    "ds_read_b64_tr_b16 a[28:29], v240 offset:61440\n"
    "ds_read_b128 v[220:223], v250 offset:16384\n"
    "v_mfma_f32_16x16x32_bf16 v[48:51], a[4:7], a[68:71], v[48:51]\n"
    "s_waitcnt lgkmcnt(10)\n"
    "ds_read_b64_tr_b16 a[30:31], v238 offset:61952\n"
    "ds_read_b128 v[224:227], v250 offset:18432\n"
    "buffer_load_dwordx4 v236, s[48:51], s90 offen offset:3072 lds\n"
    "v_mfma_f32_16x16x32_bf16 v[52:55], a[8:11], a[64:67], v[52:55]\n"
    "s_barrier\n"
    "ds_read_b64_tr_b16 a[0:1], v241 offset:0\n"
    "v_xor_b32 v1, 0, v251\n"
    "v_mfma_f32_16x16x32_bf16 v[56:59], a[8:11], a[68:71], v[56:59]\n"
    "s_add_u32 m0, s87, 40960\n"
    "ds_read_b64_tr_b16 a[2:3], v243 offset:512\n"
    "buffer_load_dwordx4 v228, s[36:39], s73 offen offset:0 lds\n"
    "v_mfma_f32_16x16x32_bf16 v[60:63], a[12:15], a[64:67], v[60:63]\n"
    "s_add_u32 s99, s98, s78\n"
    "ds_read_b64_tr_b16 a[4:5], v242 offset:0\n"
    "ds_read_b64 v[180:181], v1 offset:16384\n"
    "v_mfma_f32_16x16x32_bf16 v[64:67], a[12:15], a[68:71], v[64:67]\n"
    "s_waitcnt lgkmcnt(12)\n"
    "ds_read_b64_tr_b16 a[6:7], v244 offset:512\n"
    "ds_read_b64 v[182:183], v1 offset:18432\n"
    "buffer_load_dwordx4 v229, s[36:39], s73 offen offset:1024 lds\n"
    "v_mfma_f32_16x16x32_bf16 v[36:39], a[16:19], a[72:75], v[36:39]\n"
    "s_and_b32 s99, s99, 0x1fffff\n"
    "ds_read_b64_tr_b16 a[8:9], v243 offset:0\n"
    "v_xor_b32 v1, 32, v251\n"
    "v_mfma_f32_16x16x32_bf16 v[40:43], a[16:19], a[76:79], v[40:43]\n"
    "s_add_u32 m0, s87, 49152\n"
    "ds_read_b64_tr_b16 a[10:11], v241 offset:512\n"
    "buffer_load_dwordx4 v228, s[36:39], s75 offen offset:0 lds\n"
    "v_mfma_f32_16x16x32_bf16 v[44:47], a[20:23], a[72:75], v[44:47]\n"
    "s_buffer_load_dwordx2 s[30:31], s[36:39], s99 offset:60\n"
    "ds_read_b64_tr_b16 a[12:13], v244 offset:0\n"
    "ds_read_b64 v[184:185], v1 offset:16384\n"
    "v_mfma_f32_16x16x32_bf16 v[48:51], a[20:23], a[76:79], v[48:51]\n"
    "s_waitcnt lgkmcnt(11)\n"
    "ds_read_b64_tr_b16 a[14:15], v242 offset:512\n"
    "ds_read_b64 v[186:187], v1 offset:18432\n"
    "buffer_load_dwordx4 v229, s[36:39], s75 offen offset:1024 lds\n"
    "v_mfma_f32_16x16x32_bf16 v[52:55], a[24:27], a[72:75], v[52:55]\n"
    "ds_read_b64_tr_b16 a[16:17], v241 offset:4096\n"
    "v_xor_b32 v1, 64, v251\n"
    "v_mfma_f32_16x16x32_bf16 v[56:59], a[24:27], a[76:79], v[56:59]\n"
    "ds_read_b64_tr_b16 a[18:19], v243 offset:4608\n"
    "v_mfma_f32_16x16x32_bf16 v[60:63], a[28:31], a[72:75], v[60:63]\n"
    "ds_read_b64_tr_b16 a[20:21], v242 offset:4096\n"
    "ds_read_b64 v[188:189], v1 offset:16384\n"
    "v_mfma_f32_16x16x32_bf16 v[64:67], a[28:31], a[76:79], v[64:67]\n"
    "s_waitcnt lgkmcnt(11)\n"
    "ds_read_b64_tr_b16 a[22:23], v244 offset:4608\n"
    "ds_read_b64 v[190:191], v1 offset:18432\n"
    "v_mfma_f32_16x16x32_bf16 v[4:7], a[0:3], a[80:83], v[4:7]\n"
    "ds_read_b64_tr_b16 a[24:25], v243 offset:4096\n"
    "v_xor_b32 v1, 96, v251\n"
    "v_mfma_f32_16x16x32_bf16 v[8:11], a[0:3], a[84:87], v[8:11]\n"
    "s_add_u32 s99, s98, s80\n"
    "ds_read_b64_tr_b16 a[26:27], v241 offset:4608\n"
    "v_mfma_f32_16x16x32_bf16 v[12:15], a[4:7], a[80:83], v[12:15]\n"
    "s_and_b32 s99, s99, 0x1fffff\n"
    "ds_read_b64_tr_b16 a[28:29], v244 offset:4096\n"
    "ds_read_b64 v[192:193], v1 offset:16384\n"
    "v_mfma_f32_16x16x32_bf16 v[16:19], a[4:7], a[84:87], v[16:19]\n"
    "s_waitcnt vmcnt(16) lgkmcnt(11)\n"
    "ds_read_b64_tr_b16 a[30:31], v242 offset:4608\n"
    "ds_read_b64 v[194:195], v1 offset:18432\n"
    "v_mfma_f32_16x16x32_bf16 v[20:23], a[8:11], a[80:83], v[20:23]\n"
    "s_buffer_load_dwordx2 s[30:31], s[36:39], s99 offset:60\n"
    "ds_read_b64_tr_b16 a[0:1], v241 offset:8192\n"
    "ds_read_b128 a[96:99], v249 offset:32768\n"
    "v_mfma_f32_16x16x32_bf16 v[24:27], a[8:11], a[84:87], v[24:27]\n"
    "ds_read_b64_tr_b16 a[2:3], v243 offset:8704\n"
    "v_mfma_f32_16x16x32_bf16 v[28:31], a[12:15], a[80:83], v[28:31]\n"
    "ds_read_b64_tr_b16 a[4:5], v242 offset:8192\n"
    "ds_read_b128 a[100:103], v249 offset:34816\n"
    "v_mfma_f32_16x16x32_bf16 v[32:35], a[12:15], a[84:87], v[32:35]\n"
    "s_waitcnt lgkmcnt(12)\n"
    "ds_read_b64_tr_b16 a[6:7], v244 offset:8704\n"
    "ds_read_b128 a[104:107], v250 offset:32768\n"
    "v_mfma_f32_16x16x32_bf16 v[4:7], a[16:19], a[88:91], v[4:7]\n"
    "ds_read_b64_tr_b16 a[8:9], v243 offset:8192\n"
    "ds_read_b128 a[108:111], v250 offset:34816\n"
    "v_mfma_f32_16x16x32_bf16 v[8:11], a[16:19], a[92:95], v[8:11]\n"
    "ds_read_b64_tr_b16 a[10:11], v241 offset:8704\n"
    "v_mfma_f32_16x16x32_bf16 v[12:15], a[20:23], a[88:91], v[12:15]\n"
    "ds_read_b64_tr_b16 a[12:13], v244 offset:8192\n"
    "v_mfma_f32_16x16x32_bf16 v[16:19], a[20:23], a[92:95], v[16:19]\n"
    "s_waitcnt lgkmcnt(11)\n"
    "ds_read_b64_tr_b16 a[14:15], v242 offset:8704\n"
    "v_mfma_f32_16x16x32_bf16 v[20:23], a[24:27], a[88:91], v[20:23]\n"
    "s_add_u32 m0, s88, 106496\n"
    "ds_read_b64_tr_b16 a[16:17], v241 offset:12288\n"
    "v_lshlrev_b32 v100, 16, v180\n"
    "v_mfma_f32_16x16x32_bf16 v[24:27], a[24:27], a[92:95], v[24:27]\n"
    "s_lshr_b32 s90, s71, 10\n"
    "ds_read_b64_tr_b16 a[18:19], v243 offset:12800\n"
    "buffer_load_dwordx4 v233, s[48:51], s90 offen offset:0 lds\n"
    "v_mfma_f32_16x16x32_bf16 v[28:31], a[28:31], a[88:91], v[28:31]\n"
    "ds_read_b64_tr_b16 a[20:21], v242 offset:12288\n"
    "v_and_b32 v101, 0xffff0000, v180\n"
    "v_mfma_f32_16x16x32_bf16 v[32:35], a[28:31], a[92:95], v[32:35]\n"
    "s_waitcnt lgkmcnt(6)\n"
    "ds_read_b64_tr_b16 a[22:23], v244 offset:12800\n"
    "buffer_load_dwordx4 v234, s[48:51], s90 offen offset:1024 lds\n"
    "v_mfma_f32_16x16x32_bf16 v[36:39], a[0:3], a[80:83], v[36:39]\n"
    "ds_read_b64_tr_b16 a[24:25], v243 offset:12288\n"
    "v_lshlrev_b32 v102, 16, v181\n"
    "v_mfma_f32_16x16x32_bf16 v[40:43], a[0:3], a[84:87], v[40:43]\n"
    "s_add_u32 s99, s98, s82\n"
    "ds_read_b64_tr_b16 a[26:27], v241 offset:12800\n"
    "buffer_load_dwordx4 v235, s[48:51], s90 offen offset:2048 lds\n"
    "v_mfma_f32_16x16x32_bf16 v[44:47], a[4:7], a[80:83], v[44:47]\n"
    "s_and_b32 s99, s99, 0x1fffff\n"
    "ds_read_b64_tr_b16 a[28:29], v244 offset:12288\n"
    "v_and_b32 v103, 0xffff0000, v181\n"
    "v_mfma_f32_16x16x32_bf16 v[48:51], a[4:7], a[84:87], v[48:51]\n"
    "s_waitcnt vmcnt(11) lgkmcnt(7)\n"
    "ds_read_b64_tr_b16 a[30:31], v242 offset:12800\n"
    "ds_read_b128 a[112:115], v249 offset:49152\n"
    "buffer_load_dwordx4 v236, s[48:51], s90 offen offset:3072 lds\n"
    "v_mfma_f32_16x16x32_bf16 v[52:55], a[8:11], a[80:83], v[52:55]\n"
    "s_barrier\n"
    "ds_read_b64_tr_b16 a[0:1], v241 offset:16384\n"
    "ds_read_b128 a[116:119], v249 offset:51200\n"
    "v_mfma_f32_16x16x32_bf16 v[56:59], a[8:11], a[84:87], v[56:59]\n"
    "s_lshr_b32 s90, s73, 10\n"
    "s_add_u32 m0, s88, 122880\n"
    "ds_read_b64_tr_b16 a[2:3], v243 offset:16896\n"
    "buffer_load_dwordx4 v233, s[48:51], s90 offen offset:0 lds\n"
    "v_mfma_f32_16x16x32_bf16 v[60:63], a[12:15], a[80:83], v[60:63]\n"
    "s_buffer_load_dwordx2 s[30:31], s[36:39], s99 offset:60\n"
    "ds_read_b64_tr_b16 a[4:5], v242 offset:16384\n"
    "ds_read_b128 a[120:123], v250 offset:49152\n"
    "v_mfma_f32_16x16x32_bf16 v[64:67], a[12:15], a[84:87], v[64:67]\n"
    "s_waitcnt lgkmcnt(10)\n"
    "ds_read_b64_tr_b16 a[6:7], v244 offset:16896\n"
    "ds_read_b128 a[124:127], v250 offset:51200\n"
    "buffer_load_dwordx4 v234, s[48:51], s90 offen offset:1024 lds\n"
    "v_mfma_f32_16x16x32_bf16 v[36:39], a[16:19], a[88:91], v[36:39]\n"
    "ds_read_b64_tr_b16 a[8:9], v243 offset:16384\n"
    "v_lshlrev_b32 v104, 16, v182\n"
    "v_mfma_f32_16x16x32_bf16 v[40:43], a[16:19], a[92:95], v[40:43]\n"
    "s_add_u32 s99, s98, s71\n"
    "ds_read_b64_tr_b16 a[10:11], v241 offset:16896\n"
    "buffer_load_dwordx4 v235, s[48:51], s90 offen offset:2048 lds\n"
    "v_mfma_f32_16x16x32_bf16 v[44:47], a[20:23], a[88:91], v[44:47]\n"
    "s_and_b32 s99, s99, 0x1fffff\n"
    "ds_read_b64_tr_b16 a[12:13], v244 offset:16384\n"
    "v_and_b32 v105, 0xffff0000, v182\n"
    "v_mfma_f32_16x16x32_bf16 v[48:51], a[20:23], a[92:95], v[48:51]\n"
    "s_waitcnt lgkmcnt(11)\n"
    "ds_read_b64_tr_b16 a[14:15], v242 offset:16896\n"
    "buffer_load_dwordx4 v236, s[48:51], s90 offen offset:3072 lds\n"
    "v_mfma_f32_16x16x32_bf16 v[52:55], a[24:27], a[88:91], v[52:55]\n"
    "s_buffer_load_dwordx2 s[30:31], s[36:39], s99 offset:60\n"
    "ds_read_b64_tr_b16 a[16:17], v241 offset:20480\n"
    "v_lshlrev_b32 v106, 16, v183\n"
    "v_mfma_f32_16x16x32_bf16 v[56:59], a[24:27], a[92:95], v[56:59]\n"
    "s_add_u32 m0, s87, 57344\n"
    "ds_read_b64_tr_b16 a[18:19], v243 offset:20992\n"
    "buffer_load_dwordx4 v228, s[36:39], s77 offen offset:0 lds\n"
    "v_mfma_f32_16x16x32_bf16 v[60:63], a[28:31], a[88:91], v[60:63]\n"
    "ds_read_b64_tr_b16 a[20:21], v242 offset:20480\n"
    "v_and_b32 v107, 0xffff0000, v183\n"
    "v_mfma_f32_16x16x32_bf16 v[64:67], a[28:31], a[92:95], v[64:67]\n"
    "s_waitcnt lgkmcnt(7)\n"
    "ds_read_b64_tr_b16 a[22:23], v244 offset:20992\n"
    "buffer_load_dwordx4 v229, s[36:39], s77 offen offset:1024 lds\n"
    "v_mfma_f32_16x16x32_bf16 v[4:7], a[0:3], v[212:215], v[4:7]\n"
    "ds_read_b64_tr_b16 a[24:25], v243 offset:20480\n"
    "v_lshlrev_b32 v108, 16, v184\n"
    "v_mfma_f32_16x16x32_bf16 v[8:11], a[0:3], v[216:219], v[8:11]\n"
    "s_add_u32 m0, s87, 65536\n"
    "ds_read_b64_tr_b16 a[26:27], v241 offset:20992\n"
    "buffer_load_dwordx4 v228, s[36:39], s79 offen offset:0 lds\n"
    "v_mfma_f32_16x16x32_bf16 v[12:15], a[4:7], v[212:215], v[12:15]\n"
    "ds_read_b64_tr_b16 a[28:29], v244 offset:20480\n"
    "v_and_b32 v109, 0xffff0000, v184\n"
    "v_mfma_f32_16x16x32_bf16 v[16:19], a[4:7], v[216:219], v[16:19]\n"
    "s_waitcnt vmcnt(15) lgkmcnt(7)\n"
    "ds_read_b64_tr_b16 a[30:31], v242 offset:20992\n"
    "ds_read_b128 a[128:131], v249 offset:0\n"
    "buffer_load_dwordx4 v229, s[36:39], s79 offen offset:1024 lds\n"
    "v_mfma_f32_16x16x32_bf16 v[20:23], a[8:11], v[212:215], v[20:23]\n"
    "s_add_u32 m0, s88, 139264\n"
    "ds_read_b64_tr_b16 a[0:1], v237 offset:0\n"
    "ds_read_b128 a[132:135], v249 offset:2048\n"
    "v_mfma_f32_16x16x32_bf16 v[24:27], a[8:11], v[216:219], v[24:27]\n"
    "s_lshr_b32 s90, s75, 10\n"
    "ds_read_b64_tr_b16 a[2:3], v239 offset:512\n"
    "buffer_load_dwordx4 v233, s[48:51], s90 offen offset:0 lds\n"
    "v_mfma_f32_16x16x32_bf16 v[28:31], a[12:15], v[212:215], v[28:31]\n"
    "ds_read_b64_tr_b16 a[4:5], v238 offset:0\n"
    "ds_read_b128 a[136:139], v250 offset:0\n"
    "v_mfma_f32_16x16x32_bf16 v[32:35], a[12:15], v[216:219], v[32:35]\n"
    "s_waitcnt lgkmcnt(10)\n"
    "ds_read_b64_tr_b16 a[6:7], v240 offset:512\n"
    "ds_read_b128 a[140:143], v250 offset:2048\n"
    "buffer_load_dwordx4 v234, s[48:51], s90 offen offset:1024 lds\n"
    "v_mfma_f32_16x16x32_bf16 v[4:7], a[16:19], v[220:223], v[4:7]\n"
    "ds_read_b64_tr_b16 a[8:9], v239 offset:0\n"
    "v_lshlrev_b32 v110, 16, v185\n"
    "v_mfma_f32_16x16x32_bf16 v[8:11], a[16:19], v[224:227], v[8:11]\n"
    "ds_read_b64_tr_b16 a[10:11], v237 offset:512\n"
    "buffer_load_dwordx4 v235, s[48:51], s90 offen offset:2048 lds\n"
    "v_mfma_f32_16x16x32_bf16 v[12:15], a[20:23], v[220:223], v[12:15]\n"
    "ds_read_b64_tr_b16 a[12:13], v240 offset:0\n"
    "v_and_b32 v111, 0xffff0000, v185\n"
    "v_mfma_f32_16x16x32_bf16 v[16:19], a[20:23], v[224:227], v[16:19]\n"
    "s_waitcnt lgkmcnt(11)\n"
    "ds_read_b64_tr_b16 a[14:15], v238 offset:512\n"
    "buffer_load_dwordx4 v236, s[48:51], s90 offen offset:3072 lds\n"
    "v_mfma_f32_16x16x32_bf16 v[20:23], a[24:27], v[220:223], v[20:23]\n"
    "ds_read_b64_tr_b16 a[16:17], v237 offset:4096\n"
    "v_lshlrev_b32 v112, 16, v186\n"
    "v_mfma_f32_16x16x32_bf16 v[24:27], a[24:27], v[224:227], v[24:27]\n"
    "ds_read_b64_tr_b16 a[18:19], v239 offset:4608\n"
    "v_mfma_f32_16x16x32_bf16 v[28:31], a[28:31], v[220:223], v[28:31]\n"
    "ds_read_b64_tr_b16 a[20:21], v238 offset:4096\n"
    "v_and_b32 v113, 0xffff0000, v186\n"
    "v_mfma_f32_16x16x32_bf16 v[32:35], a[28:31], v[224:227], v[32:35]\n"
    "s_waitcnt lgkmcnt(7)\n"
    "ds_read_b64_tr_b16 a[22:23], v240 offset:4608\n"
    "v_lshlrev_b32 v114, 16, v187\n"
    "v_mfma_f32_16x16x32_bf16 v[36:39], a[0:3], v[212:215], v[36:39]\n"
    "s_add_u32 m0, s88, 90112\n"
    "ds_read_b64_tr_b16 a[24:25], v239 offset:4096\n"
    "v_and_b32 v115, 0xffff0000, v187\n"
    "v_mfma_f32_16x16x32_bf16 v[40:43], a[0:3], v[216:219], v[40:43]\n"
    "s_lshr_b32 s90, s77, 10\n"
    "ds_read_b64_tr_b16 a[26:27], v237 offset:4608\n"
    "buffer_load_dwordx4 v233, s[48:51], s90 offen offset:0 lds\n"
    "v_mfma_f32_16x16x32_bf16 v[44:47], a[4:7], v[212:215], v[44:47]\n"
    "s_add_u32 s99, s98, s73\n"
    "ds_read_b64_tr_b16 a[28:29], v240 offset:4096\n"
    "v_lshlrev_b32 v116, 16, v188\n"
    "v_mfma_f32_16x16x32_bf16 v[48:51], a[4:7], v[216:219], v[48:51]\n"
    "s_waitcnt lgkmcnt(7)\n"
    "ds_read_b64_tr_b16 a[30:31], v238 offset:4608\n"
    "buffer_load_dwordx4 v234, s[48:51], s90 offen offset:1024 lds\n"
    "v_mfma_f32_16x16x32_bf16 v[52:55], a[8:11], v[212:215], v[52:55]\n"
    "s_barrier\n"
    "ds_read_b64_tr_b16 a[0:1], v237 offset:8192\n"
    "v_and_b32 v117, 0xffff0000, v188\n"
    "v_mfma_f32_16x16x32_bf16 v[56:59], a[8:11], v[216:219], v[56:59]\n"
    "s_and_b32 s99, s99, 0x1fffff\n"
    "ds_read_b64_tr_b16 a[2:3], v239 offset:8704\n"
    "buffer_load_dwordx4 v235, s[48:51], s90 offen offset:2048 lds\n"
    "v_mfma_f32_16x16x32_bf16 v[60:63], a[12:15], v[212:215], v[60:63]\n"
    "s_buffer_load_dwordx2 s[30:31], s[36:39], s99 offset:60\n"
    "ds_read_b64_tr_b16 a[4:5], v238 offset:8192\n"
    "v_lshlrev_b32 v118, 16, v189\n"
    "v_mfma_f32_16x16x32_bf16 v[64:67], a[12:15], v[216:219], v[64:67]\n"
    "s_waitcnt lgkmcnt(7)\n"
    "ds_read_b64_tr_b16 a[6:7], v240 offset:8704\n"
    "buffer_load_dwordx4 v236, s[48:51], s90 offen offset:3072 lds\n"
    "v_mfma_f32_16x16x32_bf16 v[36:39], a[16:19], v[220:223], v[36:39]\n"
    "ds_read_b64_tr_b16 a[8:9], v239 offset:8192\n"
    "v_and_b32 v119, 0xffff0000, v189\n"
    "v_mfma_f32_16x16x32_bf16 v[40:43], a[16:19], v[224:227], v[40:43]\n"
    "s_add_u32 m0, s87, 73728\n"
    "ds_read_b64_tr_b16 a[10:11], v237 offset:8704\n"
    "buffer_load_dwordx4 v228, s[36:39], s81 offen offset:0 lds\n"
    "v_mfma_f32_16x16x32_bf16 v[44:47], a[20:23], v[220:223], v[44:47]\n"
    "s_add_u32 s99, s98, s75\n"
    "ds_read_b64_tr_b16 a[12:13], v240 offset:8192\n"
    "v_lshlrev_b32 v120, 16, v190\n"
    "v_mfma_f32_16x16x32_bf16 v[48:51], a[20:23], v[224:227], v[48:51]\n"
    "s_waitcnt lgkmcnt(7)\n"
    "ds_read_b64_tr_b16 a[14:15], v238 offset:8704\n"
    "buffer_load_dwordx4 v229, s[36:39], s81 offen offset:1024 lds\n"
    "v_mfma_f32_16x16x32_bf16 v[52:55], a[24:27], v[220:223], v[52:55]\n"
    "s_and_b32 s99, s99, 0x1fffff\n"
    "ds_read_b64_tr_b16 a[16:17], v237 offset:12288\n"
    "v_and_b32 v121, 0xffff0000, v190\n"
    "v_mfma_f32_16x16x32_bf16 v[56:59], a[24:27], v[224:227], v[56:59]\n"
    "s_add_u32 m0, s87, 81920\n"
    "ds_read_b64_tr_b16 a[18:19], v239 offset:12800\n"
    "buffer_load_dwordx4 v228, s[36:39], s83 offen offset:0 lds\n"
    "v_mfma_f32_16x16x32_bf16 v[60:63], a[28:31], v[220:223], v[60:63]\n"
    "s_buffer_load_dwordx2 s[30:31], s[36:39], s99 offset:60\n"
    "ds_read_b64_tr_b16 a[20:21], v238 offset:12288\n"
    "v_lshlrev_b32 v122, 16, v191\n"
    "v_mfma_f32_16x16x32_bf16 v[64:67], a[28:31], v[224:227], v[64:67]\n"
    "s_waitcnt lgkmcnt(7)\n"
    "ds_read_b64_tr_b16 a[22:23], v240 offset:12800\n"
    "buffer_load_dwordx4 v229, s[36:39], s83 offen offset:1024 lds\n"
    "v_mfma_f32_16x16x32_bf16 v[4:7], a[0:3], a[96:99], v[4:7]\n"
    "ds_read_b64_tr_b16 a[24:25], v239 offset:12288\n"
    "v_and_b32 v123, 0xffff0000, v191\n"
    "v_mfma_f32_16x16x32_bf16 v[8:11], a[0:3], a[100:103], v[8:11]\n"
    "ds_read_b64_tr_b16 a[26:27], v237 offset:12800\n"
    "v_mfma_f32_16x16x32_bf16 v[12:15], a[4:7], a[96:99], v[12:15]\n"
    "ds_read_b64_tr_b16 a[28:29], v240 offset:12288\n"
    "v_lshlrev_b32 v124, 16, v192\n"
    "v_mfma_f32_16x16x32_bf16 v[16:19], a[4:7], a[100:103], v[16:19]\n"
    "s_waitcnt vmcnt(20) lgkmcnt(7)\n"
    "ds_read_b64_tr_b16 a[30:31], v238 offset:12800\n"
    "ds_read_b128 a[144:147], v249 offset:16384\n"
    "v_mfma_f32_16x16x32_bf16 v[20:23], a[8:11], a[96:99], v[20:23]\n"
    "ds_read_b64_tr_b16 a[0:1], v237 offset:16384\n"
    "ds_read_b128 a[148:151], v249 offset:18432\n"
    "v_mfma_f32_16x16x32_bf16 v[24:27], a[8:11], a[100:103], v[24:27]\n"
    "ds_read_b64_tr_b16 a[2:3], v239 offset:16896\n"
    "v_mfma_f32_16x16x32_bf16 v[28:31], a[12:15], a[96:99], v[28:31]\n"
    "ds_read_b64_tr_b16 a[4:5], v238 offset:16384\n"
    "ds_read_b128 a[152:155], v250 offset:16384\n"
    "v_mfma_f32_16x16x32_bf16 v[32:35], a[12:15], a[100:103], v[32:35]\n"
    "s_waitcnt lgkmcnt(10)\n"
    "ds_read_b64_tr_b16 a[6:7], v240 offset:16896\n"
    "ds_read_b128 a[156:159], v250 offset:18432\n"
    "v_mfma_f32_16x16x32_bf16 v[4:7], a[16:19], a[104:107], v[4:7]\n"
    "s_mov_b32 s4, s16\n"
    "ds_read_b64_tr_b16 a[8:9], v239 offset:16384\n"
    "s_mov_b32 s5, s16\n"
    "s_mov_b32 s6, s16\n"
    "v_mfma_f32_16x16x32_bf16 v[8:11], a[16:19], a[108:111], v[8:11]\n"
    "ds_read_b64_tr_b16 a[10:11], v237 offset:16896\n"
    "v_mfma_f32_16x16x32_bf16 v[12:15], a[20:23], a[104:107], v[12:15]\n"
    "s_mov_b32 s7, s16\n"
    "ds_read_b64_tr_b16 a[12:13], v240 offset:16384\n"
    "s_mov_b32 s8, s17\n"
    "s_mov_b32 s9, s17\n"
    "v_mfma_f32_16x16x32_bf16 v[16:19], a[20:23], a[108:111], v[16:19]\n"
    "s_waitcnt lgkmcnt(11)\n"
    "ds_read_b64_tr_b16 a[14:15], v238 offset:16896\n"
    "s_and_b32 s26, s26, 0x1010101\n"
    "s_and_b32 s27, s27, 0x1010101\n"
    "v_mfma_f32_16x16x32_bf16 v[20:23], a[24:27], a[104:107], v[20:23]\n"
    "ds_read_b64_tr_b16 a[16:17], v237 offset:20480\n"
    "v_mbcnt_lo_u32_b32 v254, -1, 0\n"
    "v_mbcnt_hi_u32_b32 v254, -1, v254\n"
    "v_mfma_f32_16x16x32_bf16 v[24:27], a[24:27], a[108:111], v[24:27]\n"
    "ds_read_b64_tr_b16 a[18:19], v239 offset:20992\n"
    "v_mfma_f32_16x16x32_bf16 v[28:31], a[28:31], a[104:107], v[28:31]\n"
    "ds_read_b64_tr_b16 a[20:21], v238 offset:20480\n"
    "v_and_b32 v254, 31, v254\n"
    "v_lshlrev_b32 v254, 7, v254\n"
    "v_mfma_f32_16x16x32_bf16 v[32:35], a[28:31], a[108:111], v[32:35]\n"
    "s_waitcnt vmcnt(16) lgkmcnt(7)\n"
    "ds_read_b64_tr_b16 a[22:23], v240 offset:20992\n"
    "v_and_or_b32 v254, v232, s94, v254\n"
    "v_mfma_f32_16x16x32_bf16 v[4:7], a[0:3], a[112:115], v[4:7]\n"
    "s_add_u32 m0, s88, 106496\n"
    "ds_read_b64_tr_b16 a[24:25], v239 offset:20480\n"
    "ds_read_b128 a[160:163], v249 offset:32768\n"
    "v_mfma_f32_16x16x32_bf16 v[8:11], a[0:3], a[116:119], v[8:11]\n"
    "s_lshr_b32 s90, s79, 10\n"
    "ds_read_b64_tr_b16 a[26:27], v237 offset:20992\n"
    "buffer_load_dwordx4 v233, s[48:51], s90 offen offset:0 lds\n"
    "v_mfma_f32_16x16x32_bf16 v[12:15], a[4:7], a[112:115], v[12:15]\n"
    "s_add_u32 s99, s98, s77\n"
    "ds_read_b64_tr_b16 a[28:29], v240 offset:20480\n"
    "ds_read_b128 a[164:167], v249 offset:34816\n"
    "v_mfma_f32_16x16x32_bf16 v[16:19], a[4:7], a[116:119], v[16:19]\n"
    "s_waitcnt lgkmcnt(9)\n"
    "ds_read_b64_tr_b16 a[30:31], v238 offset:20992\n"
    "ds_read_b128 a[168:171], v250 offset:32768\n"
    "buffer_load_dwordx4 v234, s[48:51], s90 offen offset:1024 lds\n"
    "v_mfma_f32_16x16x32_bf16 v[20:23], a[8:11], a[112:115], v[20:23]\n"
    "s_barrier\n"
    "ds_read_b64_tr_b16 a[0:1], v237 offset:24576\n"
    "ds_read_b128 a[172:175], v250 offset:34816\n"
    "v_mfma_f32_16x16x32_bf16 v[24:27], a[8:11], a[116:119], v[24:27]\n"
    "s_and_b32 s99, s99, 0x1fffff\n"
    "ds_read_b64_tr_b16 a[2:3], v239 offset:25088\n"
    "buffer_load_dwordx4 v235, s[48:51], s90 offen offset:2048 lds\n"
    "v_mfma_f32_16x16x32_bf16 v[28:31], a[12:15], a[112:115], v[28:31]\n"
    "s_buffer_load_dwordx2 s[30:31], s[36:39], s99 offset:60\n"
    "ds_read_b64_tr_b16 a[4:5], v238 offset:24576\n"
    "v_and_b32 v125, 0xffff0000, v192\n"
    "v_mfma_f32_16x16x32_bf16 v[32:35], a[12:15], a[116:119], v[32:35]\n"
    "s_waitcnt lgkmcnt(11)\n"
    "ds_read_b64_tr_b16 a[6:7], v240 offset:25088\n"
    "buffer_load_dwordx4 v236, s[48:51], s90 offen offset:3072 lds\n"
    "v_mfma_f32_16x16x32_bf16 v[4:7], a[16:19], a[120:123], v[4:7]\n"
    "ds_read_b64_tr_b16 a[8:9], v239 offset:24576\n"
    "v_lshlrev_b32 v126, 16, v193\n"
    "v_mfma_f32_16x16x32_bf16 v[8:11], a[16:19], a[124:127], v[8:11]\n"
    "s_add_u32 m0, s87, 0\n"
    "ds_read_b64_tr_b16 a[10:11], v237 offset:25088\n"
    "buffer_load_dwordx4 v230, s[36:39], s78 offen offset:0 lds\n"
    "v_mfma_f32_16x16x32_bf16 v[12:15], a[20:23], a[120:123], v[12:15]\n"
    "s_add_u32 s99, s98, s79\n"
    "ds_read_b64_tr_b16 a[12:13], v240 offset:24576\n"
    "v_and_b32 v127, 0xffff0000, v193\n"
    "v_mfma_f32_16x16x32_bf16 v[16:19], a[20:23], a[124:127], v[16:19]\n"
    "s_waitcnt vmcnt(13) lgkmcnt(6)\n"
    "ds_read_b64_tr_b16 a[14:15], v238 offset:25088\n"
    "ds_read_b128 a[176:179], v249 offset:49152\n"
    "buffer_load_dwordx4 v231, s[36:39], s78 offen offset:1024 lds\n"
    "v_mfma_f32_16x16x32_bf16 v[20:23], a[24:27], a[120:123], v[20:23]\n"
    "s_add_u32 m0, s88, 122880\n"
    "ds_read_b64_tr_b16 a[16:17], v237 offset:28672\n"
    "ds_read_b128 a[180:183], v249 offset:51200\n"
    "v_mfma_f32_16x16x32_bf16 v[24:27], a[24:27], a[124:127], v[24:27]\n"
    "s_lshr_b32 s90, s81, 10\n"
    "ds_read_b64_tr_b16 a[18:19], v239 offset:29184\n"
    "buffer_load_dwordx4 v233, s[48:51], s90 offen offset:0 lds\n"
    "v_mfma_f32_16x16x32_bf16 v[28:31], a[28:31], a[120:123], v[28:31]\n"
    "s_and_b32 s99, s99, 0x1fffff\n"
    "ds_read_b64_tr_b16 a[20:21], v238 offset:28672\n"
    "ds_read_b128 a[184:187], v250 offset:49152\n"
    "v_mfma_f32_16x16x32_bf16 v[32:35], a[28:31], a[124:127], v[32:35]\n"
    "s_waitcnt lgkmcnt(10)\n"
    "ds_read_b64_tr_b16 a[22:23], v240 offset:29184\n"
    "ds_read_b128 a[188:191], v250 offset:51200\n"
    "buffer_load_dwordx4 v234, s[48:51], s90 offen offset:1024 lds\n"
    "v_mfma_f32_16x16x32_bf16 v[4:7], a[0:3], a[128:131], v[4:7]\n"
    "s_buffer_load_dwordx2 s[30:31], s[36:39], s99 offset:60\n"
    "ds_read_b64_tr_b16 a[24:25], v239 offset:28672\n"
    "v_lshlrev_b32 v128, 16, v194\n"
    "v_mfma_f32_16x16x32_bf16 v[8:11], a[0:3], a[132:135], v[8:11]\n"
    "ds_read_b64_tr_b16 a[26:27], v237 offset:29184\n"
    "buffer_load_dwordx4 v235, s[48:51], s90 offen offset:2048 lds\n"
    "v_mfma_f32_16x16x32_bf16 v[12:15], a[4:7], a[128:131], v[12:15]\n"
    "ds_read_b64_tr_b16 a[28:29], v240 offset:28672\n"
    "v_and_b32 v129, 0xffff0000, v194\n"
    "v_mfma_f32_16x16x32_bf16 v[16:19], a[4:7], a[132:135], v[16:19]\n"
    "s_waitcnt lgkmcnt(11)\n"
    "ds_read_b64_tr_b16 a[30:31], v238 offset:29184\n"
    "buffer_load_dwordx4 v236, s[48:51], s90 offen offset:3072 lds\n"
    "v_mfma_f32_16x16x32_bf16 v[20:23], a[8:11], a[128:131], v[20:23]\n"
    "s_add_u32 s99, s98, s68\n"
    "ds_read_b64_tr_b16 a[0:1], v237 offset:32768\n"
    "v_lshlrev_b32 v130, 16, v195\n"
    "v_mfma_f32_16x16x32_bf16 v[24:27], a[8:11], a[132:135], v[24:27]\n"
    "s_add_u32 m0, s87, 8192\n"
    "ds_read_b64_tr_b16 a[2:3], v239 offset:33280\n"
    "buffer_load_dwordx4 v230, s[36:39], s80 offen offset:0 lds\n"
    "v_mfma_f32_16x16x32_bf16 v[28:31], a[12:15], a[128:131], v[28:31]\n"
    "s_buffer_load_dwordx2 s[24:25], s[64:67], s91 offset:32 glc\n"
    "ds_read_b64_tr_b16 a[4:5], v238 offset:32768\n"
    "v_and_b32 v131, 0xffff0000, v195\n"
    "v_mfma_f32_16x16x32_bf16 v[32:35], a[12:15], a[132:135], v[32:35]\n"
    "s_waitcnt vmcnt(15) lgkmcnt(7)\n"
    "ds_read_b64_tr_b16 a[6:7], v240 offset:33280\n"
    "ds_read_b128 a[192:195], v249 offset:0\n"
    "buffer_load_dwordx4 v231, s[36:39], s80 offen offset:1024 lds\n"
    "v_mfma_f32_16x16x32_bf16 v[4:7], a[16:19], a[136:139], v[4:7]\n"
    "s_add_u32 m0, s88, 139264\n"
    "ds_read_b64_tr_b16 a[8:9], v239 offset:32768\n"
    "ds_read_b128 a[196:199], v249 offset:2048\n"
    "v_mfma_f32_16x16x32_bf16 v[8:11], a[16:19], a[140:143], v[8:11]\n"
    "s_lshr_b32 s90, s83, 10\n"
    "ds_read_b64_tr_b16 a[10:11], v237 offset:33280\n"
    "buffer_load_dwordx4 v233, s[48:51], s90 offen offset:0 lds\n"
    "v_mfma_f32_16x16x32_bf16 v[12:15], a[20:23], a[136:139], v[12:15]\n"
    "s_add_u32 s99, s98, s81\n"
    "ds_read_b64_tr_b16 a[12:13], v240 offset:32768\n"
    "ds_read_b128 a[200:203], v250 offset:0\n"
    "v_mfma_f32_16x16x32_bf16 v[16:19], a[20:23], a[140:143], v[16:19]\n"
    "L_ip2_%=:\n"
    "s_cbranch_execz L_ip6_%=\n"
    "s_waitcnt lgkmcnt(10)\n"
    "ds_read_b64_tr_b16 a[14:15], v238 offset:33280\n"
    "ds_read_b128 a[204:207], v250 offset:2048\n"
    "buffer_load_dwordx4 v234, s[48:51], s90 offen offset:1024 lds\n"
    "v_mfma_f32_16x16x32_bf16 v[20:23], a[24:27], a[136:139], v[20:23]\n"
    "s_and_b32 s99, s99, 0x1fffff\n"
    "ds_read_b64_tr_b16 a[16:17], v237 offset:36864\n"
    "v_mfma_f32_16x16x32_bf16 v[24:27], a[24:27], a[140:143], v[24:27]\n"
    "s_buffer_load_dwordx2 s[30:31], s[36:39], s99 offset:60\n"
    "ds_read_b64_tr_b16 a[18:19], v239 offset:37376\n"
    "buffer_load_dwordx4 v235, s[48:51], s90 offen offset:2048 lds\n"
    "v_mfma_f32_16x16x32_bf16 v[28:31], a[28:31], a[136:139], v[28:31]\n"
    "s_add_u32 s99, s98, s83\n"
    "ds_read_b64_tr_b16 a[20:21], v238 offset:36864\n"
    "v_mfma_f32_16x16x32_bf16 v[32:35], a[28:31], a[140:143], v[32:35]\n"
    "s_waitcnt lgkmcnt(11)\n"
    "ds_read_b64_tr_b16 a[22:23], v240 offset:37376\n"
    "buffer_load_dwordx4 v236, s[48:51], s90 offen offset:3072 lds\n"
    "v_mfma_f32_16x16x32_bf16 v[4:7], a[0:3], a[144:147], v[4:7]\n"
    "s_and_b32 s99, s99, 0x1fffff\n"
    "ds_read_b64_tr_b16 a[24:25], v239 offset:36864\n"
    "v_mfma_f32_16x16x32_bf16 v[8:11], a[0:3], a[148:151], v[8:11]\n"
    "s_buffer_load_dwordx2 s[30:31], s[36:39], s99 offset:60\n"
    "ds_read_b64_tr_b16 a[26:27], v237 offset:37376\n"
    "v_mfma_f32_16x16x32_bf16 v[12:15], a[4:7], a[144:147], v[12:15]\n"
    "s_and_b32 s99, s99, 0x1fffff\n"
    "ds_read_b64_tr_b16 a[28:29], v240 offset:36864\n"
    "v_mfma_f32_16x16x32_bf16 v[16:19], a[4:7], a[148:151], v[16:19]\n"
    "s_waitcnt lgkmcnt(7)\n"
    "ds_read_b64_tr_b16 a[30:31], v238 offset:37376\n"
    "v_mfma_f32_16x16x32_bf16 v[20:23], a[8:11], a[144:147], v[20:23]\n"
    "s_barrier\n"
    "ds_read_b64_tr_b16 a[0:1], v237 offset:40960\n"
    "v_mfma_f32_16x16x32_bf16 v[24:27], a[8:11], a[148:151], v[24:27]\n"
    "s_add_u32 m0, s87, 16384\n"
    "ds_read_b64_tr_b16 a[2:3], v239 offset:41472\n"
    "buffer_load_dwordx4 v230, s[36:39], s82 offen offset:0 lds\n"
    "v_mfma_f32_16x16x32_bf16 v[28:31], a[12:15], a[144:147], v[28:31]\n"
    "s_buffer_load_dwordx2 s[30:31], s[40:43], s99 offset:60\n"
    "ds_read_b64_tr_b16 a[4:5], v238 offset:40960\n"
    "v_mfma_f32_16x16x32_bf16 v[32:35], a[12:15], a[148:151], v[32:35]\n"
    "s_waitcnt lgkmcnt(7)\n"
    "ds_read_b64_tr_b16 a[6:7], v240 offset:41472\n"
    "v_mfma_f32_16x16x32_bf16 v[4:7], a[16:19], a[152:155], v[4:7]\n"
    "s_add_u32 s99, s98, s69\n"
    "ds_read_b64_tr_b16 a[8:9], v239 offset:40960\n"
    "v_mfma_f32_16x16x32_bf16 v[8:11], a[16:19], a[156:159], v[8:11]\n"
    "s_and_b32 s99, s99, 0x1fffff\n"
    "ds_read_b64_tr_b16 a[10:11], v237 offset:41472\n"
    "buffer_load_dwordx4 v231, s[36:39], s82 offen offset:1024 lds\n"
    "v_mfma_f32_16x16x32_bf16 v[12:15], a[20:23], a[152:155], v[12:15]\n"
    "s_buffer_load_dwordx2 s[30:31], s[40:43], s99 offset:60\n"
    "ds_read_b64_tr_b16 a[12:13], v240 offset:40960\n"
    "s_mov_b32 exec_lo, 0xffff0000\n"
    "s_mov_b32 exec_hi, 0xffff\n"
    "global_load_dword v1, v157, s[28:29]\n"
    "s_mov_b64 exec, -1\n"
    "v_mfma_f32_16x16x32_bf16 v[16:19], a[20:23], a[156:159], v[16:19]\n"
    "s_waitcnt lgkmcnt(7)\n"
    "ds_read_b64_tr_b16 a[14:15], v238 offset:41472\n"
    "v_mfma_f32_16x16x32_bf16 v[20:23], a[24:27], a[152:155], v[20:23]\n"
    "ds_read_b64_tr_b16 a[16:17], v237 offset:45056\n"
    "v_mfma_f32_16x16x32_bf16 v[24:27], a[24:27], a[156:159], v[24:27]\n"
    "s_add_u32 m0, s87, 24576\n"
    "ds_read_b64_tr_b16 a[18:19], v239 offset:45568\n"
    "buffer_load_dwordx4 v230, s[36:39], s71 offen offset:0 lds\n"
    "v_mfma_f32_16x16x32_bf16 v[28:31], a[28:31], a[152:155], v[28:31]\n"
    "ds_read_b64_tr_b16 a[20:21], v238 offset:45056\n"
    "v_mfma_f32_16x16x32_bf16 v[32:35], a[28:31], a[156:159], v[32:35]\n"
    "s_waitcnt lgkmcnt(7)\n"
    "ds_read_b64_tr_b16 a[22:23], v240 offset:45568\n"
    "v_mfma_f32_16x16x32_bf16 v[4:7], a[0:3], a[160:163], v[4:7]\n"
    "ds_read_b64_tr_b16 a[24:25], v239 offset:45056\n"
    "v_mfma_f32_16x16x32_bf16 v[8:11], a[0:3], a[164:167], v[8:11]\n"
    "ds_read_b64_tr_b16 a[26:27], v237 offset:45568\n"
    "buffer_load_dwordx4 v231, s[36:39], s71 offen offset:1024 lds\n"
    "v_mfma_f32_16x16x32_bf16 v[12:15], a[4:7], a[160:163], v[12:15]\n"
    "ds_read_b64_tr_b16 a[28:29], v240 offset:45056\n"
    "v_mfma_f32_16x16x32_bf16 v[16:19], a[4:7], a[164:167], v[16:19]\n"
    "s_waitcnt lgkmcnt(7)\n"
    "ds_read_b64_tr_b16 a[30:31], v238 offset:45568\n"
    "v_mfma_f32_16x16x32_bf16 v[20:23], a[8:11], a[160:163], v[20:23]\n"
    "ds_read_b64_tr_b16 a[0:1], v237 offset:49152\n"
    "v_mfma_f32_16x16x32_bf16 v[24:27], a[8:11], a[164:167], v[24:27]\n"
    "ds_read_b64_tr_b16 a[2:3], v239 offset:49664\n"
    "v_mfma_f32_16x16x32_bf16 v[28:31], a[12:15], a[160:163], v[28:31]\n"
    "ds_read_b64_tr_b16 a[4:5], v238 offset:49152\n"
    "v_mfma_f32_16x16x32_bf16 v[32:35], a[12:15], a[164:167], v[32:35]\n"
    "s_waitcnt vmcnt(17) lgkmcnt(7)\n"
    "ds_read_b64_tr_b16 a[6:7], v240 offset:49664\n"
    "ds_read_b128 a[208:211], v249 offset:16384\n"
    "v_mfma_f32_16x16x32_bf16 v[4:7], a[16:19], a[168:171], v[4:7]\n"
    "ds_read_b64_tr_b16 a[8:9], v239 offset:49152\n"
    "ds_read_b128 a[212:215], v249 offset:18432\n"
    "v_mfma_f32_16x16x32_bf16 v[8:11], a[16:19], a[172:175], v[8:11]\n"
    "s_add_u32 s99, s98, s70\n"
    "ds_read_b64_tr_b16 a[10:11], v237 offset:49664\n"
    "v_mfma_f32_16x16x32_bf16 v[12:15], a[20:23], a[168:171], v[12:15]\n"
    "s_and_b32 s99, s99, 0x1fffff\n"
    "ds_read_b64_tr_b16 a[12:13], v240 offset:49152\n"
    "ds_read_b128 a[216:219], v250 offset:16384\n"
    "v_mfma_f32_16x16x32_bf16 v[16:19], a[20:23], a[172:175], v[16:19]\n"
    "s_waitcnt lgkmcnt(10)\n"
    "ds_read_b64_tr_b16 a[14:15], v238 offset:49664\n"
    "ds_read_b128 a[220:223], v250 offset:18432\n"
    "v_mfma_f32_16x16x32_bf16 v[20:23], a[24:27], a[168:171], v[20:23]\n"
    "s_buffer_load_dwordx2 s[30:31], s[40:43], s99 offset:60\n"
    "ds_read_b64_tr_b16 a[16:17], v237 offset:53248\n"
    "v_mfma_f32_16x16x32_bf16 v[24:27], a[24:27], a[172:175], v[24:27]\n"
    "ds_read_b64_tr_b16 a[18:19], v239 offset:53760\n"
    "v_mfma_f32_16x16x32_bf16 v[28:31], a[28:31], a[168:171], v[28:31]\n"
    "ds_read_b64_tr_b16 a[20:21], v238 offset:53248\n"
    "v_mfma_f32_16x16x32_bf16 v[32:35], a[28:31], a[172:175], v[32:35]\n"
    "s_waitcnt lgkmcnt(11)\n"
    "ds_read_b64_tr_b16 a[22:23], v240 offset:53760\n"
    "v_mfma_f32_16x16x32_bf16 v[4:7], a[0:3], a[176:179], v[4:7]\n"
    "ds_read_b64_tr_b16 a[24:25], v239 offset:53248\n"
    "v_mfma_f32_16x16x32_bf16 v[8:11], a[0:3], a[180:183], v[8:11]\n"
    "ds_read_b64_tr_b16 a[26:27], v237 offset:53760\n"
    "v_mfma_f32_16x16x32_bf16 v[12:15], a[4:7], a[176:179], v[12:15]\n"
    "ds_read_b64_tr_b16 a[28:29], v240 offset:53248\n"
    "v_mfma_f32_16x16x32_bf16 v[16:19], a[4:7], a[180:183], v[16:19]\n"
    "s_waitcnt vmcnt(11) lgkmcnt(7)\n"
    "ds_read_b64_tr_b16 a[30:31], v238 offset:53760\n"
    "ds_read_b128 a[224:227], v249 offset:32768\n"
    "v_mfma_f32_16x16x32_bf16 v[20:23], a[8:11], a[176:179], v[20:23]\n"
    "s_barrier\n"
    "ds_read_b64_tr_b16 a[0:1], v237 offset:57344\n"
    "ds_read_b128 a[228:231], v249 offset:34816\n"
    "v_mfma_f32_16x16x32_bf16 v[24:27], a[8:11], a[180:183], v[24:27]\n"
    "s_add_u32 m0, s87, 32768\n"
    "ds_read_b64_tr_b16 a[2:3], v239 offset:57856\n"
    "buffer_load_dwordx4 v230, s[36:39], s73 offen offset:0 lds\n"
    "v_mfma_f32_16x16x32_bf16 v[28:31], a[12:15], a[176:179], v[28:31]\n"
    "ds_read_b64_tr_b16 a[4:5], v238 offset:57344\n"
    "ds_read_b128 a[232:235], v250 offset:32768\n"
    "v_mfma_f32_16x16x32_bf16 v[32:35], a[12:15], a[180:183], v[32:35]\n"
    "s_waitcnt lgkmcnt(10)\n"
    "ds_read_b64_tr_b16 a[6:7], v240 offset:57856\n"
    "ds_read_b128 a[236:239], v250 offset:34816\n"
    "v_mfma_f32_16x16x32_bf16 v[4:7], a[16:19], a[184:187], v[4:7]\n"
    "ds_read_b64_tr_b16 a[8:9], v239 offset:57344\n"
    "v_mfma_f32_16x16x32_bf16 v[8:11], a[16:19], a[188:191], v[8:11]\n"
    "ds_read_b64_tr_b16 a[10:11], v237 offset:57856\n"
    "buffer_load_dwordx4 v231, s[36:39], s73 offen offset:1024 lds\n"
    "v_mfma_f32_16x16x32_bf16 v[12:15], a[20:23], a[184:187], v[12:15]\n"
    "ds_read_b64_tr_b16 a[12:13], v240 offset:57344\n"
    "v_mfma_f32_16x16x32_bf16 v[16:19], a[20:23], a[188:191], v[16:19]\n"
    "s_waitcnt lgkmcnt(11)\n"
    "ds_read_b64_tr_b16 a[14:15], v238 offset:57856\n"
    "v_mfma_f32_16x16x32_bf16 v[20:23], a[24:27], a[184:187], v[20:23]\n"
    "s_add_u32 s99, s98, s72\n"
    "ds_read_b64_tr_b16 a[16:17], v237 offset:61440\n"
    "v_mfma_f32_16x16x32_bf16 v[24:27], a[24:27], a[188:191], v[24:27]\n"
    "s_add_u32 m0, s87, 40960\n"
    "ds_read_b64_tr_b16 a[18:19], v239 offset:61952\n"
    "buffer_load_dwordx4 v230, s[36:39], s75 offen offset:0 lds\n"
    "v_mfma_f32_16x16x32_bf16 v[28:31], a[28:31], a[184:187], v[28:31]\n"
    "s_and_b32 s99, s99, 0x1fffff\n"
    "ds_read_b64_tr_b16 a[20:21], v238 offset:61440\n"
    "v_mfma_f32_16x16x32_bf16 v[32:35], a[28:31], a[188:191], v[32:35]\n"
    "s_waitcnt vmcnt(8) lgkmcnt(7)\n"
    "ds_read_b64_tr_b16 a[22:23], v240 offset:61952\n"
    "ds_read_b128 a[240:243], v249 offset:49152\n"
    "v_mfma_f32_16x16x32_bf16 v[4:7], a[0:3], a[192:195], v[4:7]\n"
    "s_buffer_load_dwordx2 s[30:31], s[40:43], s99 offset:60\n"
    "ds_read_b64_tr_b16 a[24:25], v239 offset:61440\n"
    "ds_read_b128 a[244:247], v249 offset:51200\n"
    "v_mfma_f32_16x16x32_bf16 v[8:11], a[0:3], a[196:199], v[8:11]\n"
    "ds_read_b64_tr_b16 a[26:27], v237 offset:61952\n"
    "buffer_load_dwordx4 v231, s[36:39], s75 offen offset:1024 lds\n"
    "v_mfma_f32_16x16x32_bf16 v[12:15], a[4:7], a[192:195], v[12:15]\n"
    "ds_read_b64_tr_b16 a[28:29], v240 offset:61440\n"
    "ds_read_b128 a[248:251], v250 offset:49152\n"
    "v_mfma_f32_16x16x32_bf16 v[16:19], a[4:7], a[196:199], v[16:19]\n"
    "s_waitcnt lgkmcnt(10)\n"
    "ds_read_b64_tr_b16 a[30:31], v238 offset:61952\n"
    "ds_read_b128 a[252:255], v250 offset:51200\n"
    "v_mfma_f32_16x16x32_bf16 v[20:23], a[8:11], a[192:195], v[20:23]\n"
    "ds_read_b64_tr_b16 a[0:1], v241 offset:0\n"
    "v_mfma_f32_16x16x32_bf16 v[24:27], a[8:11], a[196:199], v[24:27]\n"
    "ds_read_b64_tr_b16 a[2:3], v243 offset:512\n"
    "v_mfma_f32_16x16x32_bf16 v[28:31], a[12:15], a[192:195], v[28:31]\n"
    "ds_read_b64_tr_b16 a[4:5], v242 offset:0\n"
    "v_mfma_f32_16x16x32_bf16 v[32:35], a[12:15], a[196:199], v[32:35]\n"
    "s_waitcnt lgkmcnt(11)\n"
    "ds_read_b64_tr_b16 a[6:7], v244 offset:512\n"
    "v_mfma_f32_16x16x32_bf16 v[4:7], a[16:19], a[200:203], v[4:7]\n"
    "ds_read_b64_tr_b16 a[8:9], v243 offset:0\n"
    "v_mfma_f32_16x16x32_bf16 v[8:11], a[16:19], a[204:207], v[8:11]\n"
    "ds_read_b64_tr_b16 a[10:11], v241 offset:512\n"
    "v_mfma_f32_16x16x32_bf16 v[12:15], a[20:23], a[200:203], v[12:15]\n"
    "ds_read_b64_tr_b16 a[12:13], v244 offset:0\n"
    "v_mfma_f32_16x16x32_bf16 v[16:19], a[20:23], a[204:207], v[16:19]\n"
    "s_waitcnt lgkmcnt(7)\n"
    "ds_read_b64_tr_b16 a[14:15], v242 offset:512\n"
    "v_mfma_f32_16x16x32_bf16 v[20:23], a[24:27], a[200:203], v[20:23]\n"
    "ds_read_b64_tr_b16 a[16:17], v241 offset:4096\n"
    "v_mfma_f32_16x16x32_bf16 v[24:27], a[24:27], a[204:207], v[24:27]\n"
    "ds_read_b64_tr_b16 a[18:19], v243 offset:4608\n"
    "v_mfma_f32_16x16x32_bf16 v[28:31], a[28:31], a[200:203], v[28:31]\n"
    "ds_read_b64_tr_b16 a[20:21], v242 offset:4096\n"
    "v_mfma_f32_16x16x32_bf16 v[32:35], a[28:31], a[204:207], v[32:35]\n"
    "s_waitcnt lgkmcnt(7)\n"
    "ds_read_b64_tr_b16 a[22:23], v244 offset:4608\n"
    "v_mfma_f32_16x16x32_bf16 v[4:7], a[0:3], a[208:211], v[4:7]\n"
    "s_add_u32 s99, s98, s74\n"
    "ds_read_b64_tr_b16 a[24:25], v243 offset:4096\n"
    "v_mfma_f32_16x16x32_bf16 v[8:11], a[0:3], a[212:215], v[8:11]\n"
    "s_and_b32 s99, s99, 0x1fffff\n"
    "ds_read_b64_tr_b16 a[26:27], v241 offset:4608\n"
    "v_mfma_f32_16x16x32_bf16 v[12:15], a[4:7], a[208:211], v[12:15]\n"
    "s_buffer_load_dwordx2 s[30:31], s[40:43], s99 offset:60\n"
    "ds_read_b64_tr_b16 a[28:29], v244 offset:4096\n"
    "v_mfma_f32_16x16x32_bf16 v[16:19], a[4:7], a[212:215], v[16:19]\n"
    "s_waitcnt lgkmcnt(7)\n"
    "ds_read_b64_tr_b16 a[30:31], v242 offset:4608\n"
    "v_mfma_f32_16x16x32_bf16 v[20:23], a[8:11], a[208:211], v[20:23]\n"
    "s_barrier\n"
    "ds_read_b64_tr_b16 a[0:1], v241 offset:8192\n"
    "v_mfma_f32_16x16x32_bf16 v[24:27], a[8:11], a[212:215], v[24:27]\n"
    "s_add_u32 m0, s87, 49152\n"
    "ds_read_b64_tr_b16 a[2:3], v243 offset:8704\n"
    "buffer_load_dwordx4 v230, s[36:39], s77 offen offset:0 lds\n"
    "v_mfma_f32_16x16x32_bf16 v[28:31], a[12:15], a[208:211], v[28:31]\n"
    "ds_read_b64_tr_b16 a[4:5], v242 offset:8192\n"
    "v_mfma_f32_16x16x32_bf16 v[32:35], a[12:15], a[212:215], v[32:35]\n"
    "s_waitcnt lgkmcnt(7)\n"
    "ds_read_b64_tr_b16 a[6:7], v244 offset:8704\n"
    "v_mfma_f32_16x16x32_bf16 v[4:7], a[16:19], a[216:219], v[4:7]\n"
    "ds_read_b64_tr_b16 a[8:9], v243 offset:8192\n"
    "v_mfma_f32_16x16x32_bf16 v[8:11], a[16:19], a[220:223], v[8:11]\n"
    "ds_read_b64_tr_b16 a[10:11], v241 offset:8704\n"
    "buffer_load_dwordx4 v231, s[36:39], s77 offen offset:1024 lds\n"
    "v_mfma_f32_16x16x32_bf16 v[12:15], a[20:23], a[216:219], v[12:15]\n"
    "ds_read_b64_tr_b16 a[12:13], v244 offset:8192\n"
    "v_mfma_f32_16x16x32_bf16 v[16:19], a[20:23], a[220:223], v[16:19]\n"
    "s_waitcnt lgkmcnt(7)\n"
    "ds_read_b64_tr_b16 a[14:15], v242 offset:8704\n"
    "v_mfma_f32_16x16x32_bf16 v[20:23], a[24:27], a[216:219], v[20:23]\n"
    "ds_read_b64_tr_b16 a[16:17], v241 offset:12288\n"
    "v_mfma_f32_16x16x32_bf16 v[24:27], a[24:27], a[220:223], v[24:27]\n"
    "s_add_u32 m0, s87, 57344\n"
    "ds_read_b64_tr_b16 a[18:19], v243 offset:12800\n"
    "buffer_load_dwordx4 v230, s[36:39], s79 offen offset:0 lds\n"
    "v_mfma_f32_16x16x32_bf16 v[28:31], a[28:31], a[216:219], v[28:31]\n"
    "ds_read_b64_tr_b16 a[20:21], v242 offset:12288\n"
    "v_mfma_f32_16x16x32_bf16 v[32:35], a[28:31], a[220:223], v[32:35]\n"
    "s_waitcnt lgkmcnt(7)\n"
    "ds_read_b64_tr_b16 a[22:23], v244 offset:12800\n"
    "v_mfma_f32_16x16x32_bf16 v[4:7], a[0:3], a[224:227], v[4:7]\n"
    "ds_read_b64_tr_b16 a[24:25], v243 offset:12288\n"
    "v_mfma_f32_16x16x32_bf16 v[8:11], a[0:3], a[228:231], v[8:11]\n"
    "s_add_u32 s99, s98, s76\n"
    "ds_read_b64_tr_b16 a[26:27], v241 offset:12800\n"
    "buffer_load_dwordx4 v231, s[36:39], s79 offen offset:1024 lds\n"
    "v_mfma_f32_16x16x32_bf16 v[12:15], a[4:7], a[224:227], v[12:15]\n"
    "s_and_b32 s99, s99, 0x1fffff\n"
    "ds_read_b64_tr_b16 a[28:29], v244 offset:12288\n"
    "v_mfma_f32_16x16x32_bf16 v[16:19], a[4:7], a[228:231], v[16:19]\n"
    "s_waitcnt lgkmcnt(7)\n"
    "ds_read_b64_tr_b16 a[30:31], v242 offset:12800\n"
    "v_mfma_f32_16x16x32_bf16 v[20:23], a[8:11], a[224:227], v[20:23]\n"
    "s_buffer_load_dwordx2 s[30:31], s[40:43], s99 offset:60\n"
    "ds_read_b64_tr_b16 a[0:1], v241 offset:16384\n"
    "v_mfma_f32_16x16x32_bf16 v[24:27], a[8:11], a[228:231], v[24:27]\n"
    "ds_read_b64_tr_b16 a[2:3], v243 offset:16896\n"
    "v_mfma_f32_16x16x32_bf16 v[28:31], a[12:15], a[224:227], v[28:31]\n"
    "s_and_b32 s16, s24, 0x1010101\n"
    "ds_read_b64_tr_b16 a[4:5], v242 offset:16384\n"
    "s_and_b32 s17, s25, 0x1010101\n"
    "s_lshl1_add_u32 s26, s16, s26\n"
    "v_mfma_f32_16x16x32_bf16 v[32:35], a[12:15], a[228:231], v[32:35]\n"
    "s_waitcnt lgkmcnt(7)\n"
    "ds_read_b64_tr_b16 a[6:7], v244 offset:16896\n"
    "s_lshl1_add_u32 s27, s17, s27\n"
    "v_lshrrev_b64 v[160:161], v162, s[16:17]\n"
    "v_mfma_f32_16x16x32_bf16 v[4:7], a[16:19], a[232:235], v[4:7]\n"
    "ds_read_b64_tr_b16 a[8:9], v243 offset:16384\n"
    "v_and_b32 v160, 1, v160\n"
    "v_lshl_or_b32 v255, v255, 1, v160\n"
    "v_mfma_f32_16x16x32_bf16 v[8:11], a[16:19], a[236:239], v[8:11]\n"
    "s_add_u32 m0, s87, 90112\n"
    "ds_read_b64_tr_b16 a[10:11], v241 offset:16896\n"
    "buffer_load_dwordx4 v230, s[36:39], s81 offen offset:0 lds\n"
    "v_mfma_f32_16x16x32_bf16 v[12:15], a[20:23], a[232:235], v[12:15]\n"
    "ds_read_b64_tr_b16 a[12:13], v244 offset:16384\n"
    "v_mfma_f32_16x16x32_bf16 v[16:19], a[20:23], a[236:239], v[16:19]\n"
    "s_waitcnt lgkmcnt(7)\n"
    "ds_read_b64_tr_b16 a[14:15], v242 offset:16896\n"
    "v_mfma_f32_16x16x32_bf16 v[20:23], a[24:27], a[232:235], v[20:23]\n"
    "ds_read_b64_tr_b16 a[16:17], v241 offset:20480\n"
    "v_mfma_f32_16x16x32_bf16 v[24:27], a[24:27], a[236:239], v[24:27]\n"
    "ds_read_b64_tr_b16 a[18:19], v243 offset:20992\n"
    "buffer_load_dwordx4 v231, s[36:39], s81 offen offset:1024 lds\n"
    "v_mfma_f32_16x16x32_bf16 v[28:31], a[28:31], a[232:235], v[28:31]\n"
    "ds_read_b64_tr_b16 a[20:21], v242 offset:20480\n"
    "v_mfma_f32_16x16x32_bf16 v[32:35], a[28:31], a[236:239], v[32:35]\n"
    "s_waitcnt lgkmcnt(7)\n"
    "ds_read_b64_tr_b16 a[22:23], v244 offset:20992\n"
    "v_mfma_f32_16x16x32_bf16 v[4:7], a[0:3], a[240:243], v[4:7]\n"
    "ds_read_b64_tr_b16 a[24:25], v243 offset:20480\n"
    "v_mfma_f32_16x16x32_bf16 v[8:11], a[0:3], a[244:247], v[8:11]\n"
    "s_add_u32 m0, s87, 98304\n"
    "ds_read_b64_tr_b16 a[26:27], v241 offset:20992\n"
    "buffer_load_dwordx4 v230, s[36:39], s83 offen offset:0 lds\n"
    "v_mfma_f32_16x16x32_bf16 v[12:15], a[4:7], a[240:243], v[12:15]\n"
    "ds_read_b64_tr_b16 a[28:29], v244 offset:20480\n"
    "v_mfma_f32_16x16x32_bf16 v[16:19], a[4:7], a[244:247], v[16:19]\n"
    "s_waitcnt lgkmcnt(7)\n"
    "ds_read_b64_tr_b16 a[30:31], v242 offset:20992\n"
    "v_mfma_f32_16x16x32_bf16 v[20:23], a[8:11], a[240:243], v[20:23]\n"
    "s_barrier\n"
    "ds_read_b64_tr_b16 a[0:1], v237 offset:0\n"
    "v_mfma_f32_16x16x32_bf16 v[24:27], a[8:11], a[244:247], v[24:27]\n"
    "ds_read_b64_tr_b16 a[2:3], v239 offset:512\n"
    "buffer_load_dwordx4 v231, s[36:39], s83 offen offset:1024 lds\n"
    "v_mfma_f32_16x16x32_bf16 v[28:31], a[12:15], a[240:243], v[28:31]\n"
    "ds_read_b64_tr_b16 a[4:5], v238 offset:0\n"
    "v_mfma_f32_16x16x32_bf16 v[32:35], a[12:15], a[244:247], v[32:35]\n"
    "s_waitcnt lgkmcnt(7)\n"
    "ds_read_b64_tr_b16 a[6:7], v240 offset:512\n"
    "v_mfma_f32_16x16x32_bf16 v[4:7], a[16:19], a[248:251], v[4:7]\n"
    "ds_read_b64_tr_b16 a[8:9], v239 offset:0\n"
    "v_mfma_f32_16x16x32_bf16 v[8:11], a[16:19], a[252:255], v[8:11]\n"
    "ds_read_b64_tr_b16 a[10:11], v237 offset:512\n"
    "v_mfma_f32_16x16x32_bf16 v[12:15], a[20:23], a[248:251], v[12:15]\n"
    "s_add_u32 m0, s87, 106496\n"
    "ds_read_b64_tr_b16 a[12:13], v240 offset:0\n"
    "v_mfma_f32_16x16x32_bf16 v[16:19], a[20:23], a[252:255], v[16:19]\n"
    "s_waitcnt lgkmcnt(7)\n"
    "ds_read_b64_tr_b16 a[14:15], v238 offset:512\n"
    "buffer_load_dwordx4 v228, s[40:43], s68 offen offset:0 lds\n"
    "v_mfma_f32_16x16x32_bf16 v[20:23], a[24:27], a[248:251], v[20:23]\n"
    "ds_read_b64_tr_b16 a[16:17], v237 offset:4096\n"
    "v_fma_f32 v132, v68, v4, v68\n"
    "v_fma_f32 v133, v69, v5, v69\n"
    "v_fma_f32 v134, v70, v6, v70\n"
    "v_mfma_f32_16x16x32_bf16 v[24:27], a[24:27], a[252:255], v[24:27]\n"
    "ds_read_b64_tr_b16 a[18:19], v239 offset:4608\n"
    "v_mfma_f32_16x16x32_bf16 v[28:31], a[28:31], a[248:251], v[28:31]\n"
    "ds_read_b64_tr_b16 a[20:21], v238 offset:4096\n"
    "v_fma_f32 v135, v71, v7, v71\n"
    "v_cvt_pk_bf16_f32 v196, v132, v133\n"
    "v_cvt_pk_bf16_f32 v197, v134, v135\n"
    "v_mfma_f32_16x16x32_bf16 v[32:35], a[28:31], a[252:255], v[32:35]\n"
    "s_waitcnt lgkmcnt(7)\n"
    "ds_read_b64_tr_b16 a[22:23], v240 offset:4608\n"
    "v_fma_f32 v140, v76, v12, v76\n"
    "v_fma_f32 v141, v77, v13, v77\n"
    "v_fma_f32 v142, v78, v14, v78\n"
    "v_mfma_f32_16x16x32_bf16 v[36:39], a[0:3], a[96:99], v[36:39]\n"
    "ds_read_b64_tr_b16 a[24:25], v239 offset:4096\n"
    "v_fma_f32 v143, v79, v15, v79\n"
    "v_cvt_pk_bf16_f32 v198, v140, v141\n"
    "v_cvt_pk_bf16_f32 v199, v142, v143\n"
    "v_mfma_f32_16x16x32_bf16 v[40:43], a[0:3], a[100:103], v[40:43]\n"
    "ds_read_b64_tr_b16 a[26:27], v237 offset:4608\n"
    "buffer_load_dwordx4 v229, s[40:43], s68 offen offset:1024 lds\n"
    "v_mfma_f32_16x16x32_bf16 v[44:47], a[4:7], a[96:99], v[44:47]\n"
    "ds_read_b64_tr_b16 a[28:29], v240 offset:4096\n"
    "buffer_store_dwordx4 v[196:199], v232, s[52:55], s68 offen offset:0 sc1\n"
    "v_fma_f32 v136, v72, v8, v72\n"
    "v_fma_f32 v137, v73, v9, v73\n"
    "v_mfma_f32_16x16x32_bf16 v[48:51], a[4:7], a[100:103], v[48:51]\n"
    "s_waitcnt lgkmcnt(7)\n"
    "ds_read_b64_tr_b16 a[30:31], v238 offset:4608\n"
    "v_fma_f32 v138, v74, v10, v74\n"
    "v_fma_f32 v139, v75, v11, v75\n"
    "ds_read_b128 v[4:7], v2 offset:512\n"
    "v_mfma_f32_16x16x32_bf16 v[52:55], a[8:11], a[96:99], v[52:55]\n"
    "ds_read_b64_tr_b16 a[0:1], v237 offset:8192\n"
    "v_cvt_pk_bf16_f32 v200, v136, v137\n"
    "v_cvt_pk_bf16_f32 v201, v138, v139\n"
    "v_fma_f32 v144, v80, v16, v80\n"
    "v_mfma_f32_16x16x32_bf16 v[56:59], a[8:11], a[100:103], v[56:59]\n"
    "s_add_u32 m0, s87, 114688\n"
    "ds_read_b64_tr_b16 a[2:3], v239 offset:8704\n"
    "buffer_load_dwordx4 v230, s[40:43], s68 offen offset:0 lds\n"
    "v_mfma_f32_16x16x32_bf16 v[60:63], a[12:15], a[96:99], v[60:63]\n"
    "ds_read_b64_tr_b16 a[4:5], v238 offset:8192\n"
    "v_fma_f32 v145, v81, v17, v81\n"
    "v_fma_f32 v146, v82, v18, v82\n"
    "v_fma_f32 v147, v83, v19, v83\n"
    "v_mfma_f32_16x16x32_bf16 v[64:67], a[12:15], a[100:103], v[64:67]\n"
    "s_waitcnt lgkmcnt(8)\n"
    "ds_read_b64_tr_b16 a[6:7], v240 offset:8704\n"
    "ds_read_b128 v[12:15], v2 offset:576\n"
    "v_cvt_pk_bf16_f32 v202, v144, v145\n"
    "v_cvt_pk_bf16_f32 v203, v146, v147\n"
    "v_mfma_f32_16x16x32_bf16 v[36:39], a[16:19], a[104:107], v[36:39]\n"
    "ds_read_b64_tr_b16 a[8:9], v239 offset:8192\n"
    "buffer_store_dwordx4 v[200:203], v232, s[52:55], s68 offen offset:1024 sc1\n"
    "v_fma_f32 v148, v84, v20, v84\n"
    "v_fma_f32 v149, v85, v21, v85\n"
    "v_mfma_f32_16x16x32_bf16 v[40:43], a[16:19], a[108:111], v[40:43]\n"
    "ds_read_b64_tr_b16 a[10:11], v237 offset:8704\n"
    "buffer_load_dwordx4 v231, s[40:43], s68 offen offset:1024 lds\n"
    "v_mfma_f32_16x16x32_bf16 v[44:47], a[20:23], a[104:107], v[44:47]\n"
    "ds_read_b64_tr_b16 a[12:13], v240 offset:8192\n"
    "v_fma_f32 v150, v86, v22, v86\n"
    "v_fma_f32 v151, v87, v23, v87\n"
    "v_cvt_pk_bf16_f32 v204, v148, v149\n"
    "v_mfma_f32_16x16x32_bf16 v[48:51], a[20:23], a[108:111], v[48:51]\n"
    "s_waitcnt lgkmcnt(9)\n"
    "ds_read_b64_tr_b16 a[14:15], v238 offset:8704\n"
    "v_cvt_pk_bf16_f32 v205, v150, v151\n"
    "v_fma_f32 v156, v92, v28, v92\n"
    "v_fma_f32 v157, v93, v29, v93\n"
    "v_mfma_f32_16x16x32_bf16 v[52:55], a[24:27], a[104:107], v[52:55]\n"
    "ds_read_b64_tr_b16 a[16:17], v237 offset:12288\n"
    "v_fma_f32 v158, v94, v30, v94\n"
    "v_fma_f32 v159, v95, v31, v95\n"
    "v_cvt_pk_bf16_f32 v206, v156, v157\n"
    "v_mfma_f32_16x16x32_bf16 v[56:59], a[24:27], a[108:111], v[56:59]\n"
    "s_add_u32 m0, s87, 122880\n"
    "ds_read_b64_tr_b16 a[18:19], v239 offset:12800\n"
    "buffer_load_dwordx4 v228, s[40:43], s69 offen offset:0 lds\n"
    "v_mfma_f32_16x16x32_bf16 v[60:63], a[28:31], a[104:107], v[60:63]\n"
    "ds_read_b64_tr_b16 a[20:21], v238 offset:12288\n"
    "v_cvt_pk_bf16_f32 v207, v158, v159\n"
    "buffer_store_dwordx4 v[204:207], v232, s[52:55], s68 offen offset:2048 sc1\n"
    "v_fma_f32 v152, v88, v24, v88\n"
    "v_mfma_f32_16x16x32_bf16 v[64:67], a[28:31], a[108:111], v[64:67]\n"
    "s_waitcnt lgkmcnt(8)\n"
    "ds_read_b64_tr_b16 a[22:23], v240 offset:12800\n"
    "v_fma_f32 v153, v89, v25, v89\n"
    "v_fma_f32 v154, v90, v26, v90\n"
    "v_fma_f32 v155, v91, v27, v91\n"
    "v_mfma_f32_16x16x32_bf16 v[36:39], a[0:3], a[112:115], v[36:39]\n"
    "ds_read_b64_tr_b16 a[24:25], v239 offset:12288\n"
    "ds_read_b128 v[20:23], v2 offset:640\n"
    "v_cvt_pk_bf16_f32 v208, v152, v153\n"
    "v_cvt_pk_bf16_f32 v209, v154, v155\n"
    "v_mfma_f32_16x16x32_bf16 v[40:43], a[0:3], a[116:119], v[40:43]\n"
    "ds_read_b64_tr_b16 a[26:27], v237 offset:12800\n"
    "v_mfma_f32_16x16x32_bf16 v[44:47], a[4:7], a[112:115], v[44:47]\n"
    "ds_read_b64_tr_b16 a[28:29], v240 offset:12288\n"
    "v_fma_f32 v160, v96, v32, v96\n"
    "v_fma_f32 v161, v97, v33, v97\n"
    "v_fma_f32 v162, v98, v34, v98\n"
    "v_mfma_f32_16x16x32_bf16 v[48:51], a[4:7], a[116:119], v[48:51]\n"
    "s_waitcnt vmcnt(20) lgkmcnt(8)\n"
    "ds_read_b64_tr_b16 a[30:31], v238 offset:12800\n"
    "v_fma_f32 v163, v99, v35, v99\n"
    "ds_read_b128 v[28:31], v2 offset:704\n"
    "v_cvt_pk_bf16_f32 v210, v160, v161\n"
    "v_mfma_f32_16x16x32_bf16 v[52:55], a[8:11], a[112:115], v[52:55]\n"
    "s_barrier\n"
    "ds_read_b64_tr_b16 a[0:1], v237 offset:16384\n"
    "v_cvt_pk_bf16_f32 v211, v162, v163\n"
    "buffer_store_dwordx4 v[208:211], v232, s[52:55], s68 offen offset:3072 sc1\n"
    "v_mfma_f32_16x16x32_bf16 v[56:59], a[8:11], a[116:119], v[56:59]\n"
    "ds_read_b64_tr_b16 a[2:3], v239 offset:16896\n"
    "buffer_load_dwordx4 v229, s[40:43], s69 offen offset:1024 lds\n"
    "v_mfma_f32_16x16x32_bf16 v[60:63], a[12:15], a[112:115], v[60:63]\n"
    "ds_read_b64_tr_b16 a[4:5], v238 offset:16384\n"
    "v_mfma_f32_16x16x32_bf16 v[64:67], a[12:15], a[116:119], v[64:67]\n"
    "s_waitcnt lgkmcnt(9)\n"
    "ds_read_b64_tr_b16 a[6:7], v240 offset:16896\n"
    "v_mfma_f32_16x16x32_bf16 v[36:39], a[16:19], a[120:123], v[36:39]\n"
    "ds_read_b64_tr_b16 a[8:9], v239 offset:16384\n"
    "v_mfma_f32_16x16x32_bf16 v[40:43], a[16:19], a[124:127], v[40:43]\n"
    "s_add_u32 m0, s87, 131072\n"
    "ds_read_b64_tr_b16 a[10:11], v237 offset:16896\n"
    "buffer_load_dwordx4 v230, s[40:43], s69 offen offset:0 lds\n"
    "v_mfma_f32_16x16x32_bf16 v[44:47], a[20:23], a[120:123], v[44:47]\n"
    "ds_read_b64_tr_b16 a[12:13], v240 offset:16384\n"
    "v_mfma_f32_16x16x32_bf16 v[48:51], a[20:23], a[124:127], v[48:51]\n"
    "s_waitcnt lgkmcnt(8)\n"
    "ds_read_b64_tr_b16 a[14:15], v238 offset:16896\n"
    "v_mfma_f32_16x16x32_bf16 v[52:55], a[24:27], a[120:123], v[52:55]\n"
    "ds_read_b64_tr_b16 a[16:17], v237 offset:20480\n"
    "v_mfma_f32_16x16x32_bf16 v[56:59], a[24:27], a[124:127], v[56:59]\n"
    "ds_read_b64_tr_b16 a[18:19], v239 offset:20992\n"
    "buffer_load_dwordx4 v231, s[40:43], s69 offen offset:1024 lds\n"
    "v_mfma_f32_16x16x32_bf16 v[60:63], a[28:31], a[120:123], v[60:63]\n"
    "ds_read_b64_tr_b16 a[20:21], v238 offset:20480\n"
    "v_mfma_f32_16x16x32_bf16 v[64:67], a[28:31], a[124:127], v[64:67]\n"
    "s_waitcnt lgkmcnt(7)\n"
    "ds_read_b64_tr_b16 a[22:23], v240 offset:20992\n"
    "v_mfma_f32_16x16x32_bf16 v[36:39], a[0:3], a[128:131], v[36:39]\n"
    "ds_read_b64_tr_b16 a[24:25], v239 offset:20480\n"
    "v_mfma_f32_16x16x32_bf16 v[40:43], a[0:3], a[132:135], v[40:43]\n"
    "s_add_u32 m0, s87, 139264\n"
    "ds_read_b64_tr_b16 a[26:27], v237 offset:20992\n"
    "buffer_load_dwordx4 v228, s[40:43], s70 offen offset:0 lds\n"
    "v_mfma_f32_16x16x32_bf16 v[44:47], a[4:7], a[128:131], v[44:47]\n"
    "ds_read_b64_tr_b16 a[28:29], v240 offset:20480\n"
    "v_mfma_f32_16x16x32_bf16 v[48:51], a[4:7], a[132:135], v[48:51]\n"
    "s_waitcnt lgkmcnt(7)\n"
    "ds_read_b64_tr_b16 a[30:31], v238 offset:20992\n"
    "v_mfma_f32_16x16x32_bf16 v[52:55], a[8:11], a[128:131], v[52:55]\n"
    "ds_read_b64_tr_b16 a[0:1], v237 offset:24576\n"
    "v_mfma_f32_16x16x32_bf16 v[56:59], a[8:11], a[132:135], v[56:59]\n"
    "ds_read_b64_tr_b16 a[2:3], v239 offset:25088\n"
    "v_mfma_f32_16x16x32_bf16 v[60:63], a[12:15], a[128:131], v[60:63]\n"
    "ds_read_b64_tr_b16 a[4:5], v238 offset:24576\n"
    "v_mfma_f32_16x16x32_bf16 v[64:67], a[12:15], a[132:135], v[64:67]\n"
    "s_waitcnt lgkmcnt(7)\n"
    "ds_read_b64_tr_b16 a[6:7], v240 offset:25088\n"
    "buffer_load_dwordx4 v229, s[40:43], s70 offen offset:1024 lds\n"
    "v_mfma_f32_16x16x32_bf16 v[36:39], a[16:19], a[136:139], v[36:39]\n"
    "ds_read_b64_tr_b16 a[8:9], v239 offset:24576\n"
    "v_mfma_f32_16x16x32_bf16 v[40:43], a[16:19], a[140:143], v[40:43]\n"
    "ds_read_b64_tr_b16 a[10:11], v237 offset:25088\n"
    "v_mfma_f32_16x16x32_bf16 v[44:47], a[20:23], a[136:139], v[44:47]\n"
    "s_add_u32 m0, s87, 147456\n"
    "ds_read_b64_tr_b16 a[12:13], v240 offset:24576\n"
    "v_mfma_f32_16x16x32_bf16 v[48:51], a[20:23], a[140:143], v[48:51]\n"
    "s_waitcnt lgkmcnt(7)\n"
    "ds_read_b64_tr_b16 a[14:15], v238 offset:25088\n"
    "buffer_load_dwordx4 v230, s[40:43], s70 offen offset:0 lds\n"
    "v_mfma_f32_16x16x32_bf16 v[52:55], a[24:27], a[136:139], v[52:55]\n"
    "ds_read_b64_tr_b16 a[16:17], v237 offset:28672\n"
    "v_mfma_f32_16x16x32_bf16 v[56:59], a[24:27], a[140:143], v[56:59]\n"
    "ds_read_b64_tr_b16 a[18:19], v239 offset:29184\n"
    "v_mfma_f32_16x16x32_bf16 v[60:63], a[28:31], a[136:139], v[60:63]\n"
    "ds_read_b64_tr_b16 a[20:21], v238 offset:28672\n"
    "v_mfma_f32_16x16x32_bf16 v[64:67], a[28:31], a[140:143], v[64:67]\n"
    "s_waitcnt lgkmcnt(7)\n"
    "ds_read_b64_tr_b16 a[22:23], v240 offset:29184\n"
    "buffer_load_dwordx4 v231, s[40:43], s70 offen offset:1024 lds\n"
    "v_mfma_f32_16x16x32_bf16 v[36:39], a[0:3], a[144:147], v[36:39]\n"
    "ds_read_b64_tr_b16 a[24:25], v239 offset:28672\n"
    "v_mfma_f32_16x16x32_bf16 v[40:43], a[0:3], a[148:151], v[40:43]\n"
    "ds_read_b64_tr_b16 a[26:27], v237 offset:29184\n"
    "v_mfma_f32_16x16x32_bf16 v[44:47], a[4:7], a[144:147], v[44:47]\n"
    "s_add_u32 m0, s87, 65536\n"
    "ds_read_b64_tr_b16 a[28:29], v240 offset:28672\n"
    "v_mfma_f32_16x16x32_bf16 v[48:51], a[4:7], a[148:151], v[48:51]\n"
    "s_waitcnt vmcnt(24) lgkmcnt(7)\n"
    "ds_read_b64_tr_b16 a[30:31], v238 offset:29184\n"
    "buffer_load_dwordx4 v228, s[40:43], s72 offen offset:0 lds\n"
    "v_mfma_f32_16x16x32_bf16 v[52:55], a[8:11], a[144:147], v[52:55]\n"
    "s_barrier\n"
    "ds_read_b64_tr_b16 a[0:1], v237 offset:32768\n"
    "v_mfma_f32_16x16x32_bf16 v[56:59], a[8:11], a[148:151], v[56:59]\n"
    "ds_read_b64_tr_b16 a[2:3], v239 offset:33280\n"
    "v_mfma_f32_16x16x32_bf16 v[60:63], a[12:15], a[144:147], v[60:63]\n"
    "L_ip3_%=:\n"
    "s_cbranch_execz L_ip7_%=\n"
    "ds_read_b64_tr_b16 a[4:5], v238 offset:32768\n"
    "v_mfma_f32_16x16x32_bf16 v[64:67], a[12:15], a[148:151], v[64:67]\n"
    "s_waitcnt lgkmcnt(7)\n"
    "ds_read_b64_tr_b16 a[6:7], v240 offset:33280\n"
    "v_mfma_f32_16x16x32_bf16 v[36:39], a[16:19], a[152:155], v[36:39]\n"
    "ds_read_b64_tr_b16 a[8:9], v239 offset:32768\n"
    "v_mfma_f32_16x16x32_bf16 v[40:43], a[16:19], a[156:159], v[40:43]\n"
    "ds_read_b64_tr_b16 a[10:11], v237 offset:33280\n"
    "buffer_load_dwordx4 v229, s[40:43], s72 offen offset:1024 lds\n"
    "v_mfma_f32_16x16x32_bf16 v[44:47], a[20:23], a[152:155], v[44:47]\n"
    "ds_read_b64_tr_b16 a[12:13], v240 offset:32768\n"
    "v_mfma_f32_16x16x32_bf16 v[48:51], a[20:23], a[156:159], v[48:51]\n"
    "s_waitcnt lgkmcnt(7)\n"
    "ds_read_b64_tr_b16 a[14:15], v238 offset:33280\n"
    "v_mfma_f32_16x16x32_bf16 v[52:55], a[24:27], a[152:155], v[52:55]\n"
    "ds_read_b64_tr_b16 a[16:17], v237 offset:36864\n"
    "v_mfma_f32_16x16x32_bf16 v[56:59], a[24:27], a[156:159], v[56:59]\n"
    "s_add_u32 m0, s87, 73728\n"
    "ds_read_b64_tr_b16 a[18:19], v239 offset:37376\n"
    "buffer_load_dwordx4 v230, s[40:43], s72 offen offset:0 lds\n"
    "v_mfma_f32_16x16x32_bf16 v[60:63], a[28:31], a[152:155], v[60:63]\n"
    "ds_read_b64_tr_b16 a[20:21], v238 offset:36864\n"
    "v_mfma_f32_16x16x32_bf16 v[64:67], a[28:31], a[156:159], v[64:67]\n"
    "s_waitcnt lgkmcnt(7)\n"
    "ds_read_b64_tr_b16 a[22:23], v240 offset:37376\n"
    "v_mfma_f32_16x16x32_bf16 v[36:39], a[0:3], a[160:163], v[36:39]\n"
    "ds_read_b64_tr_b16 a[24:25], v239 offset:36864\n"
    "v_mfma_f32_16x16x32_bf16 v[40:43], a[0:3], a[164:167], v[40:43]\n"
    "ds_read_b64_tr_b16 a[26:27], v237 offset:37376\n"
    "buffer_load_dwordx4 v231, s[40:43], s72 offen offset:1024 lds\n"
    "v_mfma_f32_16x16x32_bf16 v[44:47], a[4:7], a[160:163], v[44:47]\n"
    "ds_read_b64_tr_b16 a[28:29], v240 offset:36864\n"
    "v_mfma_f32_16x16x32_bf16 v[48:51], a[4:7], a[164:167], v[48:51]\n"
    "s_waitcnt lgkmcnt(7)\n"
    "ds_read_b64_tr_b16 a[30:31], v238 offset:37376\n"
    "v_mfma_f32_16x16x32_bf16 v[52:55], a[8:11], a[160:163], v[52:55]\n"
    "ds_read_b64_tr_b16 a[0:1], v237 offset:40960\n"
    "v_mfma_f32_16x16x32_bf16 v[56:59], a[8:11], a[164:167], v[56:59]\n"
    "s_add_u32 m0, s87, 0\n"
    "ds_read_b64_tr_b16 a[2:3], v239 offset:41472\n"
    "buffer_load_dwordx4 v228, s[40:43], s74 offen offset:0 lds\n"
    "v_mfma_f32_16x16x32_bf16 v[60:63], a[12:15], a[160:163], v[60:63]\n"
    "ds_read_b64_tr_b16 a[4:5], v238 offset:40960\n"
    "v_mfma_f32_16x16x32_bf16 v[64:67], a[12:15], a[164:167], v[64:67]\n"
    "s_waitcnt lgkmcnt(7)\n"
    "ds_read_b64_tr_b16 a[6:7], v240 offset:41472\n"
    "v_mfma_f32_16x16x32_bf16 v[36:39], a[16:19], a[168:171], v[36:39]\n"
    "ds_read_b64_tr_b16 a[8:9], v239 offset:40960\n"
    "v_mfma_f32_16x16x32_bf16 v[40:43], a[16:19], a[172:175], v[40:43]\n"
    "ds_read_b64_tr_b16 a[10:11], v237 offset:41472\n"
    "v_mfma_f32_16x16x32_bf16 v[44:47], a[20:23], a[168:171], v[44:47]\n"
    "ds_read_b64_tr_b16 a[12:13], v240 offset:40960\n"
    "v_mfma_f32_16x16x32_bf16 v[48:51], a[20:23], a[172:175], v[48:51]\n"
    "s_waitcnt vmcnt(12) lgkmcnt(7)\n"
    "ds_read_b64_tr_b16 a[14:15], v238 offset:41472\n"
    "buffer_store_byte v255, v253, s[64:67], s92 offen offset:0 sc1\n"
    "v_mfma_f32_16x16x32_bf16 v[52:55], a[24:27], a[168:171], v[52:55]\n"
    "ds_read_b64_tr_b16 a[16:17], v237 offset:45056\n"
    "v_mfma_f32_16x16x32_bf16 v[56:59], a[24:27], a[172:175], v[56:59]\n"
    "ds_read_b64_tr_b16 a[18:19], v239 offset:45568\n"
    "buffer_load_dwordx4 v229, s[40:43], s74 offen offset:1024 lds\n"
    "v_mfma_f32_16x16x32_bf16 v[60:63], a[28:31], a[168:171], v[60:63]\n"
    "ds_read_b64_tr_b16 a[20:21], v238 offset:45056\n"
    "v_mfma_f32_16x16x32_bf16 v[64:67], a[28:31], a[172:175], v[64:67]\n"
    "s_waitcnt lgkmcnt(7)\n"
    "ds_read_b64_tr_b16 a[22:23], v240 offset:45568\n"
    "v_mfma_f32_16x16x32_bf16 v[36:39], a[0:3], a[176:179], v[36:39]\n"
    "ds_read_b64_tr_b16 a[24:25], v239 offset:45056\n"
    "v_mfma_f32_16x16x32_bf16 v[40:43], a[0:3], a[180:183], v[40:43]\n"
    "s_add_u32 m0, s87, 81920\n"
    "ds_read_b64_tr_b16 a[26:27], v237 offset:45568\n"
    "buffer_load_dwordx4 v230, s[40:43], s74 offen offset:0 lds\n"
    "v_mfma_f32_16x16x32_bf16 v[44:47], a[4:7], a[176:179], v[44:47]\n"
    "ds_read_b64_tr_b16 a[28:29], v240 offset:45056\n"
    "v_mfma_f32_16x16x32_bf16 v[48:51], a[4:7], a[180:183], v[48:51]\n"
    "s_waitcnt lgkmcnt(7)\n"
    "ds_read_b64_tr_b16 a[30:31], v238 offset:45568\n"
    "v_mfma_f32_16x16x32_bf16 v[52:55], a[8:11], a[176:179], v[52:55]\n"
    "s_barrier\n"
    "ds_read_b64_tr_b16 a[0:1], v237 offset:49152\n"
    "v_mfma_f32_16x16x32_bf16 v[56:59], a[8:11], a[180:183], v[56:59]\n"
    "ds_read_b64_tr_b16 a[2:3], v239 offset:49664\n"
    "buffer_load_dwordx4 v231, s[40:43], s74 offen offset:1024 lds\n"
    "v_mfma_f32_16x16x32_bf16 v[60:63], a[12:15], a[176:179], v[60:63]\n"
    "ds_read_b64_tr_b16 a[4:5], v238 offset:49152\n"
    "v_mfma_f32_16x16x32_bf16 v[64:67], a[12:15], a[180:183], v[64:67]\n"
    "s_waitcnt lgkmcnt(7)\n"
    "ds_read_b64_tr_b16 a[6:7], v240 offset:49664\n"
    "v_mfma_f32_16x16x32_bf16 v[36:39], a[16:19], a[184:187], v[36:39]\n"
    "ds_read_b64_tr_b16 a[8:9], v239 offset:49152\n"
    "v_mfma_f32_16x16x32_bf16 v[40:43], a[16:19], a[188:191], v[40:43]\n"
    "s_add_u32 m0, s87, 8192\n"
    "ds_read_b64_tr_b16 a[10:11], v237 offset:49664\n"
    "buffer_load_dwordx4 v228, s[40:43], s76 offen offset:0 lds\n"
    "v_mfma_f32_16x16x32_bf16 v[44:47], a[20:23], a[184:187], v[44:47]\n"
    "ds_read_b64_tr_b16 a[12:13], v240 offset:49152\n"
    "v_mfma_f32_16x16x32_bf16 v[48:51], a[20:23], a[188:191], v[48:51]\n"
    "s_waitcnt lgkmcnt(7)\n"
    "ds_read_b64_tr_b16 a[14:15], v238 offset:49664\n"
    "v_mfma_f32_16x16x32_bf16 v[52:55], a[24:27], a[184:187], v[52:55]\n"
    "ds_read_b64_tr_b16 a[16:17], v237 offset:53248\n"
    "v_mfma_f32_16x16x32_bf16 v[56:59], a[24:27], a[188:191], v[56:59]\n"
    "ds_read_b64_tr_b16 a[18:19], v239 offset:53760\n"
    "v_mfma_f32_16x16x32_bf16 v[60:63], a[28:31], a[184:187], v[60:63]\n"
    "ds_read_b64_tr_b16 a[20:21], v238 offset:53248\n"
    "v_mfma_f32_16x16x32_bf16 v[64:67], a[28:31], a[188:191], v[64:67]\n"
    "s_waitcnt lgkmcnt(7)\n"
    "ds_read_b64_tr_b16 a[22:23], v240 offset:53760\n"
    "buffer_load_dwordx4 v229, s[40:43], s76 offen offset:1024 lds\n"
    "v_mfma_f32_16x16x32_bf16 v[36:39], a[0:3], a[192:195], v[36:39]\n"
    "ds_read_b64_tr_b16 a[24:25], v239 offset:53248\n"
    "v_mfma_f32_16x16x32_bf16 v[40:43], a[0:3], a[196:199], v[40:43]\n"
    "ds_read_b64_tr_b16 a[26:27], v237 offset:53760\n"
    "v_mfma_f32_16x16x32_bf16 v[44:47], a[4:7], a[192:195], v[44:47]\n"
    "s_add_u32 m0, s87, 16384\n"
    "ds_read_b64_tr_b16 a[28:29], v240 offset:53248\n"
    "v_mfma_f32_16x16x32_bf16 v[48:51], a[4:7], a[196:199], v[48:51]\n"
    "s_waitcnt lgkmcnt(7)\n"
    "ds_read_b64_tr_b16 a[30:31], v238 offset:53760\n"
    "buffer_load_dwordx4 v230, s[40:43], s76 offen offset:0 lds\n"
    "v_mfma_f32_16x16x32_bf16 v[52:55], a[8:11], a[192:195], v[52:55]\n"
    "ds_read_b64_tr_b16 a[0:1], v237 offset:57344\n"
    "v_mfma_f32_16x16x32_bf16 v[56:59], a[8:11], a[196:199], v[56:59]\n"
    "ds_read_b64_tr_b16 a[2:3], v239 offset:57856\n"
    "v_mfma_f32_16x16x32_bf16 v[60:63], a[12:15], a[192:195], v[60:63]\n"
    "ds_read_b64_tr_b16 a[4:5], v238 offset:57344\n"
    "v_mfma_f32_16x16x32_bf16 v[64:67], a[12:15], a[196:199], v[64:67]\n"
    "s_waitcnt lgkmcnt(7)\n"
    "ds_read_b64_tr_b16 a[6:7], v240 offset:57856\n"
    "buffer_load_dwordx4 v231, s[40:43], s76 offen offset:1024 lds\n"
    "v_mfma_f32_16x16x32_bf16 v[36:39], a[16:19], a[200:203], v[36:39]\n"
    "ds_read_b64_tr_b16 a[8:9], v239 offset:57344\n"
    "v_mfma_f32_16x16x32_bf16 v[40:43], a[16:19], a[204:207], v[40:43]\n"
    "ds_read_b64_tr_b16 a[10:11], v237 offset:57856\n"
    "v_mfma_f32_16x16x32_bf16 v[44:47], a[20:23], a[200:203], v[44:47]\n"
    "s_add_u32 m0, s87, 24576\n"
    "ds_read_b64_tr_b16 a[12:13], v240 offset:57344\n"
    "v_mfma_f32_16x16x32_bf16 v[48:51], a[20:23], a[204:207], v[48:51]\n"
    "s_waitcnt lgkmcnt(7)\n"
    "ds_read_b64_tr_b16 a[14:15], v238 offset:57856\n"
    "buffer_load_dwordx4 v228, s[40:43], s78 offen offset:0 lds\n"
    "v_mfma_f32_16x16x32_bf16 v[52:55], a[24:27], a[200:203], v[52:55]\n"
    "ds_read_b64_tr_b16 a[16:17], v237 offset:61440\n"
    "v_mfma_f32_16x16x32_bf16 v[56:59], a[24:27], a[204:207], v[56:59]\n"
    "ds_read_b64_tr_b16 a[18:19], v239 offset:61952\n"
    "v_mfma_f32_16x16x32_bf16 v[60:63], a[28:31], a[200:203], v[60:63]\n"
    "ds_read_b64_tr_b16 a[20:21], v238 offset:61440\n"
    "v_mfma_f32_16x16x32_bf16 v[64:67], a[28:31], a[204:207], v[64:67]\n"
    "s_waitcnt lgkmcnt(7)\n"
    "ds_read_b64_tr_b16 a[22:23], v240 offset:61952\n"
    "v_mfma_f32_16x16x32_bf16 v[36:39], a[0:3], a[208:211], v[36:39]\n"
    "ds_read_b64_tr_b16 a[24:25], v239 offset:61440\n"
    "v_mfma_f32_16x16x32_bf16 v[40:43], a[0:3], a[212:215], v[40:43]\n"
    "ds_read_b64_tr_b16 a[26:27], v237 offset:61952\n"
    "buffer_load_dwordx4 v229, s[40:43], s78 offen offset:1024 lds\n"
    "v_mfma_f32_16x16x32_bf16 v[44:47], a[4:7], a[208:211], v[44:47]\n"
    "ds_read_b64_tr_b16 a[28:29], v240 offset:61440\n"
    "v_mfma_f32_16x16x32_bf16 v[48:51], a[4:7], a[212:215], v[48:51]\n"
    "s_waitcnt lgkmcnt(7)\n"
    "ds_read_b64_tr_b16 a[30:31], v238 offset:61952\n"
    "v_mfma_f32_16x16x32_bf16 v[52:55], a[8:11], a[208:211], v[52:55]\n"
    "s_barrier\n"
    "ds_read_b64_tr_b16 a[0:1], v241 offset:24576\n"
    "v_mfma_f32_16x16x32_bf16 v[56:59], a[8:11], a[212:215], v[56:59]\n"
    "s_add_u32 m0, s87, 32768\n"
    "ds_read_b64_tr_b16 a[2:3], v243 offset:25088\n"
    "buffer_load_dwordx4 v228, s[40:43], s80 offen offset:0 lds\n"
    "v_mfma_f32_16x16x32_bf16 v[60:63], a[12:15], a[208:211], v[60:63]\n"
    "ds_read_b64_tr_b16 a[4:5], v242 offset:24576\n"
    "v_mfma_f32_16x16x32_bf16 v[64:67], a[12:15], a[212:215], v[64:67]\n"
    "s_waitcnt lgkmcnt(7)\n"
    "ds_read_b64_tr_b16 a[6:7], v244 offset:25088\n"
    "v_mfma_f32_16x16x32_bf16 v[36:39], a[16:19], a[216:219], v[36:39]\n"
    "s_buffer_load_dword s4, s[64:67], s91 offset:0 glc\n"
    "ds_read_b64_tr_b16 a[8:9], v243 offset:24576\n"
    "v_mfma_f32_16x16x32_bf16 v[40:43], a[16:19], a[220:223], v[40:43]\n"
    "ds_read_b64_tr_b16 a[10:11], v241 offset:25088\n"
    "buffer_load_dwordx4 v229, s[40:43], s80 offen offset:1024 lds\n"
    "v_mfma_f32_16x16x32_bf16 v[44:47], a[20:23], a[216:219], v[44:47]\n"
    "ds_read_b64_tr_b16 a[12:13], v244 offset:24576\n"
    "v_mfma_f32_16x16x32_bf16 v[48:51], a[20:23], a[220:223], v[48:51]\n"
    "s_waitcnt lgkmcnt(7)\n"
    "ds_read_b64_tr_b16 a[14:15], v242 offset:25088\n"
    "v_mfma_f32_16x16x32_bf16 v[52:55], a[24:27], a[216:219], v[52:55]\n"
    "ds_read_b64_tr_b16 a[16:17], v241 offset:28672\n"
    "v_mfma_f32_16x16x32_bf16 v[56:59], a[24:27], a[220:223], v[56:59]\n"
    "s_add_u32 m0, s87, 40960\n"
    "ds_read_b64_tr_b16 a[18:19], v243 offset:29184\n"
    "buffer_load_dwordx4 v228, s[40:43], s82 offen offset:0 lds\n"
    "v_mfma_f32_16x16x32_bf16 v[60:63], a[28:31], a[216:219], v[60:63]\n"
    "ds_read_b64_tr_b16 a[20:21], v242 offset:28672\n"
    "v_mfma_f32_16x16x32_bf16 v[64:67], a[28:31], a[220:223], v[64:67]\n"
    "s_waitcnt lgkmcnt(7)\n"
    "ds_read_b64_tr_b16 a[22:23], v244 offset:29184\n"
    "v_mfma_f32_16x16x32_bf16 v[36:39], a[0:3], a[224:227], v[36:39]\n"
    "ds_read_b64_tr_b16 a[24:25], v243 offset:28672\n"
    "v_mfma_f32_16x16x32_bf16 v[40:43], a[0:3], a[228:231], v[40:43]\n"
    "ds_read_b64_tr_b16 a[26:27], v241 offset:29184\n"
    "v_mfma_f32_16x16x32_bf16 v[44:47], a[4:7], a[224:227], v[44:47]\n"
    "ds_read_b64_tr_b16 a[28:29], v244 offset:28672\n"
    "v_mfma_f32_16x16x32_bf16 v[48:51], a[4:7], a[228:231], v[48:51]\n"
    "s_waitcnt lgkmcnt(7)\n"
    "ds_read_b64_tr_b16 a[30:31], v242 offset:29184\n"
    "buffer_load_dwordx4 v229, s[40:43], s82 offen offset:1024 lds\n"
    "v_mfma_f32_16x16x32_bf16 v[52:55], a[8:11], a[224:227], v[52:55]\n"
    "ds_read_b64_tr_b16 a[0:1], v241 offset:32768\n"
    "v_mfma_f32_16x16x32_bf16 v[56:59], a[8:11], a[228:231], v[56:59]\n"
    "ds_read_b64_tr_b16 a[2:3], v243 offset:33280\n"
    "v_mfma_f32_16x16x32_bf16 v[60:63], a[12:15], a[224:227], v[60:63]\n"
    "s_add_u32 m0, s87, 49152\n"
    "ds_read_b64_tr_b16 a[4:5], v242 offset:32768\n"
    "v_mfma_f32_16x16x32_bf16 v[64:67], a[12:15], a[228:231], v[64:67]\n"
    "s_waitcnt lgkmcnt(7)\n"
    "ds_read_b64_tr_b16 a[6:7], v244 offset:33280\n"
    "buffer_load_dwordx4 v228, s[40:43], s71 offen offset:0 lds\n"
    "v_mfma_f32_16x16x32_bf16 v[36:39], a[16:19], a[232:235], v[36:39]\n"
    "ds_read_b64_tr_b16 a[8:9], v243 offset:32768\n"
    "v_mfma_f32_16x16x32_bf16 v[40:43], a[16:19], a[236:239], v[40:43]\n"
    "ds_read_b64_tr_b16 a[10:11], v241 offset:33280\n"
    "v_mfma_f32_16x16x32_bf16 v[44:47], a[20:23], a[232:235], v[44:47]\n"
    "s_buffer_load_dword s5, s[64:67], s91 offset:0 glc\n"
    "ds_read_b64_tr_b16 a[12:13], v244 offset:32768\n"
    "v_mfma_f32_16x16x32_bf16 v[48:51], a[20:23], a[236:239], v[48:51]\n"
    "s_waitcnt lgkmcnt(7)\n"
    "ds_read_b64_tr_b16 a[14:15], v242 offset:33280\n"
    "buffer_load_dwordx4 v229, s[40:43], s71 offen offset:1024 lds\n"
    "v_mfma_f32_16x16x32_bf16 v[52:55], a[24:27], a[232:235], v[52:55]\n"
    "ds_read_b64_tr_b16 a[16:17], v241 offset:36864\n"
    "v_mfma_f32_16x16x32_bf16 v[56:59], a[24:27], a[236:239], v[56:59]\n"
    "ds_read_b64_tr_b16 a[18:19], v243 offset:37376\n"
    "v_mfma_f32_16x16x32_bf16 v[60:63], a[28:31], a[232:235], v[60:63]\n"
    "ds_read_b64_tr_b16 a[20:21], v242 offset:36864\n"
    "v_mfma_f32_16x16x32_bf16 v[64:67], a[28:31], a[236:239], v[64:67]\n"
    "s_waitcnt lgkmcnt(7)\n"
    "ds_read_b64_tr_b16 a[22:23], v244 offset:37376\n"
    "v_mfma_f32_16x16x32_bf16 v[36:39], a[0:3], a[240:243], v[36:39]\n"
    "ds_read_b64_tr_b16 a[24:25], v243 offset:36864\n"
    "v_mfma_f32_16x16x32_bf16 v[40:43], a[0:3], a[244:247], v[40:43]\n"
    "ds_read_b64_tr_b16 a[26:27], v241 offset:37376\n"
    "v_mfma_f32_16x16x32_bf16 v[44:47], a[4:7], a[240:243], v[44:47]\n"
    "s_buffer_load_dword s6, s[64:67], s91 offset:0 glc\n"
    "ds_read_b64_tr_b16 a[28:29], v244 offset:36864\n"
    "v_mfma_f32_16x16x32_bf16 v[48:51], a[4:7], a[244:247], v[48:51]\n"
    "s_waitcnt lgkmcnt(7)\n"
    "ds_read_b64_tr_b16 a[30:31], v242 offset:37376\n"
    "v_mfma_f32_16x16x32_bf16 v[52:55], a[8:11], a[240:243], v[52:55]\n"
    "s_barrier\n"
    "v_mov_b32 v237, v3\n"
    "v_xor_b32 v238, 32, v237\n"
    "v_xor_b32 v239, 64, v237\n"
    "v_xor_b32 v240, 96, v237\n"
    "v_add_u32 v241, 0x10000, v237\n"
    "v_add_u32 v242, 0x10000, v238\n"
    "v_add_u32 v243, 0x10000, v239\n"
    "v_add_u32 v244, 0x10000, v240\n"
    "v_add_u32 v245, 0x20000, v237\n"
    "v_add_u32 v246, 0x20000, v238\n"
    "v_add_u32 v247, 0x20000, v239\n"
    "v_add_u32 v248, 0x20000, v240\n"
    "ds_read_b64_tr_b16 a[0:1], v241 offset:40960\n"
    "v_mfma_f32_16x16x32_bf16 v[56:59], a[8:11], a[244:247], v[56:59]\n"
    "s_add_u32 m0, s87, 57344\n"
    "ds_read_b64_tr_b16 a[2:3], v241 offset:43008\n"
    "buffer_load_dwordx4 v228, s[40:43], s73 offen offset:0 lds\n"
    "v_mfma_f32_16x16x32_bf16 v[60:63], a[12:15], a[240:243], v[60:63]\n"
    "ds_read_b64_tr_b16 a[4:5], v242 offset:40960\n"
    "v_mfma_f32_16x16x32_bf16 v[64:67], a[12:15], a[244:247], v[64:67]\n"
    "s_waitcnt lgkmcnt(7)\n"
    "ds_read_b64_tr_b16 a[6:7], v242 offset:43008\n"
    "v_mfma_f32_16x16x32_bf16 v[36:39], a[16:19], a[248:251], v[36:39]\n"
    "ds_read_b64_tr_b16 a[8:9], v243 offset:40960\n"
    "v_mfma_f32_16x16x32_bf16 v[40:43], a[16:19], a[252:255], v[40:43]\n"
    "ds_read_b64_tr_b16 a[10:11], v243 offset:43008\n"
    "buffer_load_dwordx4 v229, s[40:43], s73 offen offset:1024 lds\n"
    "v_mfma_f32_16x16x32_bf16 v[44:47], a[20:23], a[248:251], v[44:47]\n"
    "ds_read_b64_tr_b16 a[12:13], v244 offset:40960\n"
    "v_mfma_f32_16x16x32_bf16 v[48:51], a[20:23], a[252:255], v[48:51]\n"
    "s_waitcnt lgkmcnt(7)\n"
    "ds_read_b64_tr_b16 a[14:15], v244 offset:43008\n"
    "v_mfma_f32_16x16x32_bf16 v[52:55], a[24:27], a[248:251], v[52:55]\n"
    "ds_read_b64_tr_b16 a[16:17], v241 offset:45056\n"
    "v_fma_f32 v164, v100, v36, v100\n"
    "v_fma_f32 v165, v101, v37, v101\n"
    "v_fma_f32 v166, v102, v38, v102\n"
    "v_mfma_f32_16x16x32_bf16 v[56:59], a[24:27], a[252:255], v[56:59]\n"
    "s_add_u32 m0, s87, 90112\n"
    "ds_read_b64_tr_b16 a[18:19], v241 offset:47104\n"
    "buffer_load_dwordx4 v228, s[40:43], s75 offen offset:0 lds\n"
    "v_mfma_f32_16x16x32_bf16 v[60:63], a[28:31], a[248:251], v[60:63]\n"
    "ds_read_b64_tr_b16 a[20:21], v242 offset:45056\n"
    "v_fma_f32 v167, v103, v39, v103\n"
    "v_fma_f32 v168, v104, v40, v104\n"
    "v_fma_f32 v169, v105, v41, v105\n"
    "v_mfma_f32_16x16x32_bf16 v[64:67], a[28:31], a[252:255], v[64:67]\n"
    "s_waitcnt lgkmcnt(7)\n"
    "ds_read_b64_tr_b16 a[22:23], v242 offset:47104\n"
    "v_fma_f32 v170, v106, v42, v106\n"
    "v_fma_f32 v171, v107, v43, v107\n"
    "ds_read_b128 v[36:39], v2 offset:768\n"
    "v_mfma_f32_16x16x32_bf16 v[8:11], a[0:3], v[200:203], v[4:7]\n"
    "ds_read_b64_tr_b16 a[24:25], v243 offset:45056\n"
    "v_fma_f32 v172, v108, v44, v108\n"
    "v_fma_f32 v173, v109, v45, v109\n"
    "v_fma_f32 v174, v110, v46, v110\n"
    "v_mfma_f32_16x16x32_bf16 v[4:7], a[0:3], v[196:199], v[4:7]\n"
    "ds_read_b64_tr_b16 a[26:27], v243 offset:47104\n"
    "buffer_load_dwordx4 v229, s[40:43], s75 offen offset:1024 lds\n"
    "v_mfma_f32_16x16x32_bf16 v[16:19], a[4:7], v[200:203], v[12:15]\n"
    "s_buffer_load_dword s8, s[64:67], s91 offset:4 glc\n"
    "ds_read_b64_tr_b16 a[28:29], v244 offset:45056\n"
    "v_fma_f32 v175, v111, v47, v111\n"
    "v_fma_f32 v176, v112, v48, v112\n"
    "v_fma_f32 v177, v113, v49, v113\n"
    "v_mfma_f32_16x16x32_bf16 v[12:15], a[4:7], v[196:199], v[12:15]\n"
    "s_waitcnt lgkmcnt(8)\n"
    "ds_read_b64_tr_b16 a[30:31], v244 offset:47104\n"
    "v_fma_f32 v178, v114, v50, v114\n"
    "v_fma_f32 v179, v115, v51, v115\n"
    "ds_read_b128 v[44:47], v2 offset:832\n"
    "v_mfma_f32_16x16x32_bf16 v[24:27], a[8:11], v[200:203], v[20:23]\n"
    "s_buffer_load_dword s7, s[64:67], s91 offset:0 glc\n"
    "ds_read_b64_tr_b16 a[0:1], v241 offset:49152\n"
    "v_fma_f32 v180, v116, v52, v116\n"
    "v_fma_f32 v181, v117, v53, v117\n"
    "v_fma_f32 v182, v118, v54, v118\n"
    "v_mfma_f32_16x16x32_bf16 v[20:23], a[8:11], v[196:199], v[20:23]\n"
    "s_xor_b32 s90, s4, s26\n"
    "ds_read_b64_tr_b16 a[2:3], v241 offset:51200\n"
    "v_mfma_f32_16x16x32_bf16 v[32:35], a[12:15], v[200:203], v[28:31]\n"
    "s_and_b32 s90, s90, 0x30300\n"
    "ds_read_b64_tr_b16 a[4:5], v242 offset:49152\n"
    "v_fma_f32 v183, v119, v55, v119\n"
    "v_fma_f32 v184, v120, v56, v120\n"
    "v_fma_f32 v185, v121, v57, v121\n"
    "v_mfma_f32_16x16x32_bf16 v[28:31], a[12:15], v[196:199], v[28:31]\n"
    "s_waitcnt lgkmcnt(9)\n"
    "ds_read_b64_tr_b16 a[6:7], v242 offset:51200\n"
    "v_fma_f32 v186, v122, v58, v122\n"
    "v_fma_f32 v187, v123, v59, v123\n"
    "ds_read_b128 v[52:55], v2 offset:896\n"
    "v_mfma_f32_16x16x32_bf16 v[4:7], a[16:19], v[204:207], v[4:7]\n"
    "s_xor_b32 s93, s5, s26\n"
    "s_and_b32 s93, s93, 0x30300\n"
    "s_cselect_b32 s93, s90, 0\n"
    "s_cmp_lg_u32 s93, 0\n"
    "s_cbranch_scc1 L_sp0_0_%=\n"
    "L_bp0_0_%=:\n"
    "ds_read_b64_tr_b16 a[8:9], v243 offset:49152\n"
    "v_fma_f32 v188, v124, v60, v124\n"
    "v_fma_f32 v189, v125, v61, v125\n"
    "v_fma_f32 v190, v126, v62, v126\n"
    "v_mfma_f32_16x16x32_bf16 v[8:11], a[16:19], v[208:211], v[8:11]\n"
    "ds_read_b64_tr_b16 a[10:11], v243 offset:51200\n"
    "buffer_load_dwordx4 a[32:35], v232, s[52:55], s70 offen offset:0 sc1 \n"
    "v_mfma_f32_16x16x32_bf16 v[12:15], a[20:23], v[204:207], v[12:15]\n"
    "ds_read_b64_tr_b16 a[12:13], v244 offset:49152\n"
    "v_fma_f32 v191, v127, v63, v127\n"
    "v_fma_f32 v192, v128, v64, v128\n"
    "v_fma_f32 v193, v129, v65, v129\n"
    "v_mfma_f32_16x16x32_bf16 v[16:19], a[20:23], v[208:211], v[16:19]\n"
    "s_waitcnt lgkmcnt(9)\n"
    "ds_read_b64_tr_b16 a[14:15], v244 offset:51200\n"
    "v_fma_f32 v194, v130, v66, v130\n"
    "v_fma_f32 v195, v131, v67, v131\n"
    "ds_read_b128 v[60:63], v2 offset:960\n"
    "buffer_load_dwordx4 a[36:39], v232, s[52:55], s70 offen offset:1024 sc1 \n"
    "v_mfma_f32_16x16x32_bf16 v[20:23], a[24:27], v[204:207], v[20:23]\n"
    "ds_read_b64_tr_b16 a[16:17], v241 offset:53248\n"
    "v_cvt_pk_bf16_f32 v212, v164, v165\n"
    "v_cvt_pk_bf16_f32 v213, v166, v167\n"
    "v_cvt_pk_bf16_f32 v214, v172, v173\n"
    "v_mfma_f32_16x16x32_bf16 v[24:27], a[24:27], v[208:211], v[24:27]\n"
    "ds_read_b64_tr_b16 a[18:19], v241 offset:55296\n"
    "buffer_load_dwordx4 a[40:43], v232, s[52:55], s70 offen offset:2048 sc1 \n"
    "v_mfma_f32_16x16x32_bf16 v[28:31], a[28:31], v[204:207], v[28:31]\n"
    "ds_read_b64_tr_b16 a[20:21], v242 offset:53248\n"
    "v_cvt_pk_bf16_f32 v215, v174, v175\n"
    "v_cvt_pk_bf16_f32 v216, v168, v169\n"
    "v_cvt_pk_bf16_f32 v217, v170, v171\n"
    "v_mfma_f32_16x16x32_bf16 v[32:35], a[28:31], v[208:211], v[32:35]\n"
    "s_waitcnt lgkmcnt(9)\n"
    "ds_read_b64_tr_b16 a[22:23], v242 offset:55296\n"
    "v_cvt_pk_bf16_f32 v218, v176, v177\n"
    "v_cvt_pk_bf16_f32 v219, v178, v179\n"
    "v_cvt_pk_bf16_f32 v220, v180, v181\n"
    "buffer_load_dwordx4 a[44:47], v232, s[52:55], s70 offen offset:3072 sc1 \n"
    "v_mfma_f32_16x16x32_bf16 v[40:43], a[0:3], v[200:203], v[36:39]\n"
    "ds_read_b64_tr_b16 a[24:25], v243 offset:53248\n"
    "v_cvt_pk_bf16_f32 v221, v182, v183\n"
    "v_cvt_pk_bf16_f32 v222, v188, v189\n"
    "v_cvt_pk_bf16_f32 v223, v190, v191\n"
    "v_mfma_f32_16x16x32_bf16 v[36:39], a[0:3], v[196:199], v[36:39]\n"
    "ds_read_b64_tr_b16 a[26:27], v243 offset:55296\n"
    "v_mfma_f32_16x16x32_bf16 v[48:51], a[4:7], v[200:203], v[44:47]\n"
    "ds_read_b64_tr_b16 a[28:29], v244 offset:53248\n"
    "v_cvt_pk_bf16_f32 v224, v184, v185\n"
    "v_cvt_pk_bf16_f32 v225, v186, v187\n"
    "v_cvt_pk_bf16_f32 v226, v192, v193\n"
    "v_mfma_f32_16x16x32_bf16 v[44:47], a[4:7], v[196:199], v[44:47]\n"
    "s_waitcnt vmcnt(33) lgkmcnt(7)\n"
    "ds_read_b64_tr_b16 a[30:31], v244 offset:55296\n"
    "v_cvt_pk_bf16_f32 v227, v194, v195\n"
    "v_mfma_f32_16x16x32_bf16 v[56:59], a[8:11], v[200:203], v[52:55]\n"
    "s_barrier\n"
    "ds_read_b64_tr_b16 a[0:1], v241 offset:57344\n"
    "v_mfma_f32_16x16x32_bf16 v[52:55], a[8:11], v[196:199], v[52:55]\n"
    "ds_read_b64_tr_b16 a[2:3], v241 offset:59392\n"
    "buffer_load_dwordx4 a[48:51], v232, s[52:55], s72 offen offset:0 sc1 \n"
    "v_mfma_f32_16x16x32_bf16 v[64:67], a[12:15], v[200:203], v[60:63]\n"
    "s_buffer_load_dword s9, s[64:67], s91 offset:4 glc\n"
    "ds_read_b64_tr_b16 a[4:5], v242 offset:57344\n"
    "v_mfma_f32_16x16x32_bf16 v[60:63], a[12:15], v[196:199], v[60:63]\n"
    "s_waitcnt lgkmcnt(7)\n"
    "ds_read_b64_tr_b16 a[6:7], v242 offset:59392\n"
    "v_mfma_f32_16x16x32_bf16 v[36:39], a[16:19], v[204:207], v[36:39]\n"
    "ds_read_b64_tr_b16 a[8:9], v243 offset:57344\n"
    "v_mfma_f32_16x16x32_bf16 v[40:43], a[16:19], v[208:211], v[40:43]\n"
    "ds_read_b64_tr_b16 a[10:11], v243 offset:59392\n"
    "v_mfma_f32_16x16x32_bf16 v[44:47], a[20:23], v[204:207], v[44:47]\n"
    "ds_read_b64_tr_b16 a[12:13], v244 offset:57344\n"
    "buffer_store_dwordx4 v[212:215], v232, s[52:55], s69 offen offset:0 sc1\n"
    "v_mfma_f32_16x16x32_bf16 v[48:51], a[20:23], v[208:211], v[48:51]\n"
    "s_waitcnt lgkmcnt(7)\n"
    "ds_read_b64_tr_b16 a[14:15], v244 offset:59392\n"
    "v_mfma_f32_16x16x32_bf16 v[52:55], a[24:27], v[204:207], v[52:55]\n"
    "s_add_u32 s99, s98, s78\n"
    "ds_read_b64_tr_b16 a[16:17], v241 offset:61440\n"
    "buffer_store_dwordx4 v[216:219], v232, s[52:55], s69 offen offset:1024 sc1\n"
    "v_mfma_f32_16x16x32_bf16 v[56:59], a[24:27], v[208:211], v[56:59]\n"
    "s_and_b32 s99, s99, 0x1fffff\n"
    "ds_read_b64_tr_b16 a[18:19], v241 offset:63488\n"
    "v_mfma_f32_16x16x32_bf16 v[60:63], a[28:31], v[204:207], v[60:63]\n"
    "s_buffer_load_dwordx2 s[30:31], s[40:43], s99 offset:60\n"
    "ds_read_b64_tr_b16 a[20:21], v242 offset:61440\n"
    "buffer_store_dwordx4 v[220:223], v232, s[52:55], s69 offen offset:2048 sc1\n"
    "v_mfma_f32_16x16x32_bf16 v[64:67], a[28:31], v[208:211], v[64:67]\n"
    "s_waitcnt lgkmcnt(7)\n"
    "ds_read_b64_tr_b16 a[22:23], v242 offset:63488\n"
    "v_mfma_f32_16x16x32_bf16 v[4:7], a[0:3], v[212:215], v[4:7]\n"
    "ds_read_b64_tr_b16 a[24:25], v243 offset:61440\n"
    "buffer_store_dwordx4 v[224:227], v232, s[52:55], s69 offen offset:3072 sc1\n"
    "v_mfma_f32_16x16x32_bf16 v[8:11], a[0:3], v[216:219], v[8:11]\n"
    "s_xor_b32 s90, s6, s26\n"
    "ds_read_b64_tr_b16 a[26:27], v243 offset:63488\n"
    "buffer_load_dwordx4 a[52:55], v232, s[52:55], s72 offen offset:1024 sc1 \n"
    "v_mfma_f32_16x16x32_bf16 v[12:15], a[4:7], v[212:215], v[12:15]\n"
    "s_and_b32 s90, s90, 0x3000000\n"
    "ds_read_b64_tr_b16 a[28:29], v244 offset:61440\n"
    "v_mfma_f32_16x16x32_bf16 v[16:19], a[4:7], v[216:219], v[16:19]\n"
    "s_waitcnt lgkmcnt(7)\n"
    "ds_read_b64_tr_b16 a[30:31], v244 offset:63488\n"
    "buffer_load_dwordx4 a[56:59], v232, s[52:55], s72 offen offset:2048 sc1 \n"
    "v_mfma_f32_16x16x32_bf16 v[20:23], a[8:11], v[212:215], v[20:23]\n"
    "s_xor_b32 s93, s7, s26\n"
    "s_and_b32 s93, s93, 0x3000000\n"
    "s_cselect_b32 s93, s90, 0\n"
    "s_cmp_lg_u32 s93, 0\n"
    "s_cbranch_scc1 L_sp0_1_%=\n"
    "L_bp0_1_%=:\n"
    "ds_read_b64_tr_b16 a[0:1], v245 offset:0\n"
    "v_mfma_f32_16x16x32_bf16 v[24:27], a[8:11], v[216:219], v[24:27]\n"
    "ds_read_b64_tr_b16 a[2:3], v245 offset:2048\n"
    "v_mfma_f32_16x16x32_bf16 v[28:31], a[12:15], v[212:215], v[28:31]\n"
    "ds_read_b64_tr_b16 a[4:5], v246 offset:0\n"
    "v_mfma_f32_16x16x32_bf16 v[32:35], a[12:15], v[216:219], v[32:35]\n"
    "s_waitcnt lgkmcnt(7)\n"
    "ds_read_b64_tr_b16 a[6:7], v246 offset:2048\n"
    "buffer_load_dwordx4 a[60:63], v232, s[52:55], s72 offen offset:3072 sc1 \n"
    "v_mfma_f32_16x16x32_bf16 v[4:7], a[16:19], v[220:223], v[4:7]\n"
    "ds_read_b64_tr_b16 a[8:9], v247 offset:0\n"
    "v_mfma_f32_16x16x32_bf16 v[8:11], a[16:19], v[224:227], v[8:11]\n"
    "ds_read_b64_tr_b16 a[10:11], v247 offset:2048\n"
    "v_mfma_f32_16x16x32_bf16 v[12:15], a[20:23], v[220:223], v[12:15]\n"
    "ds_read_b64_tr_b16 a[12:13], v248 offset:0\n"
    "v_mfma_f32_16x16x32_bf16 v[16:19], a[20:23], v[224:227], v[16:19]\n"
    "s_waitcnt lgkmcnt(7)\n"
    "ds_read_b64_tr_b16 a[14:15], v248 offset:2048\n"
    "buffer_load_dwordx4 a[64:67], v232, s[52:55], s74 offen offset:0 sc1 \n"
    "v_mfma_f32_16x16x32_bf16 v[20:23], a[24:27], v[220:223], v[20:23]\n"
    "ds_read_b64_tr_b16 a[16:17], v245 offset:4096\n"
    "v_mfma_f32_16x16x32_bf16 v[24:27], a[24:27], v[224:227], v[24:27]\n"
    "ds_read_b64_tr_b16 a[18:19], v245 offset:6144\n"
    "v_mfma_f32_16x16x32_bf16 v[28:31], a[28:31], v[220:223], v[28:31]\n"
    "ds_read_b64_tr_b16 a[20:21], v246 offset:4096\n"
    "v_mfma_f32_16x16x32_bf16 v[32:35], a[28:31], v[224:227], v[32:35]\n"
    "s_waitcnt lgkmcnt(7)\n"
    "ds_read_b64_tr_b16 a[22:23], v246 offset:6144\n"
    "buffer_load_dwordx4 a[68:71], v232, s[52:55], s74 offen offset:1024 sc1 \n"
    "v_mfma_f32_16x16x32_bf16 v[36:39], a[0:3], v[212:215], v[36:39]\n"
    "s_xor_b32 s90, s8, s27\n"
    "ds_read_b64_tr_b16 a[24:25], v247 offset:4096\n"
    "v_mfma_f32_16x16x32_bf16 v[40:43], a[0:3], v[216:219], v[40:43]\n"
    "s_and_b32 s90, s90, 0x3030303\n"
    "ds_read_b64_tr_b16 a[26:27], v247 offset:6144\n"
    "v_mfma_f32_16x16x32_bf16 v[44:47], a[4:7], v[212:215], v[44:47]\n"
    "s_xor_b32 s93, s9, s27\n"
    "s_and_b32 s93, s93, 0x3030303\n"
    "s_cselect_b32 s93, s90, 0\n"
    "s_cmp_lg_u32 s93, 0\n"
    "s_cbranch_scc1 L_sp0_2_%=\n"
    "L_bp0_2_%=:\n"
    "ds_read_b64_tr_b16 a[28:29], v248 offset:4096\n"
    "v_mfma_f32_16x16x32_bf16 v[48:51], a[4:7], v[216:219], v[48:51]\n"
    "s_waitcnt vmcnt(39) lgkmcnt(7)\n"
    "ds_read_b64_tr_b16 a[30:31], v248 offset:6144\n"
    "v_mov_b32 v255, 1\n"
    "s_mov_b64 s[24:25], 0\n"
    "v_mfma_f32_16x16x32_bf16 v[52:55], a[8:11], v[212:215], v[52:55]\n"
    "s_barrier\n"
    "ds_read_b64_tr_b16 a[0:1], v245 offset:8192\n"
    "s_mov_b64 s[26:27], 0\n"
    "s_mov_b64 s[16:17], 0\n"
    "v_mfma_f32_16x16x32_bf16 v[56:59], a[8:11], v[216:219], v[56:59]\n"
    "ds_read_b64_tr_b16 a[2:3], v245 offset:10240\n"
    "buffer_load_dwordx4 a[72:75], v232, s[52:55], s74 offen offset:2048 sc1 \n"
    "v_mfma_f32_16x16x32_bf16 v[60:63], a[12:15], v[212:215], v[60:63]\n"
    "ds_read_b64_tr_b16 a[4:5], v246 offset:8192\n"
    "v_mfma_f32_16x16x32_bf16 v[64:67], a[12:15], v[216:219], v[64:67]\n"
    "s_waitcnt lgkmcnt(7)\n"
    "ds_read_b64_tr_b16 a[6:7], v246 offset:10240\n"
    "v_mfma_f32_16x16x32_bf16 v[36:39], a[16:19], v[220:223], v[36:39]\n"
    "ds_read_b64_tr_b16 a[8:9], v247 offset:8192\n"
    "v_mfma_f32_16x16x32_bf16 v[40:43], a[16:19], v[224:227], v[40:43]\n"
    "ds_read_b64_tr_b16 a[10:11], v247 offset:10240\n"
    "buffer_load_dwordx4 a[76:79], v232, s[52:55], s74 offen offset:3072 sc1 \n"
    "v_mfma_f32_16x16x32_bf16 v[44:47], a[20:23], v[220:223], v[44:47]\n"
    "ds_read_b64_tr_b16 a[12:13], v248 offset:8192\n"
    "v_mfma_f32_16x16x32_bf16 v[48:51], a[20:23], v[224:227], v[48:51]\n"
    "s_waitcnt lgkmcnt(7)\n"
    "ds_read_b64_tr_b16 a[14:15], v248 offset:10240\n"
    "v_mfma_f32_16x16x32_bf16 v[52:55], a[24:27], v[220:223], v[52:55]\n"
    "ds_read_b64_tr_b16 a[16:17], v245 offset:12288\n"
    "v_mfma_f32_16x16x32_bf16 v[56:59], a[24:27], v[224:227], v[56:59]\n"
    "ds_read_b64_tr_b16 a[18:19], v245 offset:14336\n"
    "buffer_load_dwordx4 a[80:83], v232, s[52:55], s76 offen offset:0 sc1 \n"
    "v_mfma_f32_16x16x32_bf16 v[60:63], a[28:31], v[220:223], v[60:63]\n"
    "ds_read_b64_tr_b16 a[20:21], v246 offset:12288\n"
    "v_mfma_f32_16x16x32_bf16 v[64:67], a[28:31], v[224:227], v[64:67]\n"
    "s_waitcnt vmcnt(13) lgkmcnt(7)\n"
    "ds_read_b64_tr_b16 a[22:23], v246 offset:14336\n"
    "v_mfma_f32_16x16x32_bf16 v[4:7], a[0:3], a[32:35], v[4:7]\n"
    "ds_read_b64_tr_b16 a[24:25], v247 offset:12288\n"
    "v_mfma_f32_16x16x32_bf16 v[8:11], a[0:3], a[36:39], v[8:11]\n"
    "ds_read_b64_tr_b16 a[26:27], v247 offset:14336\n"
    "buffer_load_dwordx4 a[84:87], v232, s[52:55], s76 offen offset:1024 sc1 \n"
    "v_mfma_f32_16x16x32_bf16 v[12:15], a[4:7], a[32:35], v[12:15]\n"
    "ds_read_b64_tr_b16 a[28:29], v248 offset:12288\n"
    "v_mfma_f32_16x16x32_bf16 v[16:19], a[4:7], a[36:39], v[16:19]\n"
    "s_waitcnt lgkmcnt(7)\n"
    "ds_read_b64_tr_b16 a[30:31], v248 offset:14336\n"
    "v_mfma_f32_16x16x32_bf16 v[20:23], a[8:11], a[32:35], v[20:23]\n"
    "L_ip4_%=:\n"
    "s_cbranch_execz L_ip8_%=\n"
    "ds_read_b64_tr_b16 a[0:1], v245 offset:16384\n"
    "v_mfma_f32_16x16x32_bf16 v[24:27], a[8:11], a[36:39], v[24:27]\n"
    "ds_read_b64_tr_b16 a[2:3], v245 offset:18432\n"
    "buffer_load_dwordx4 a[88:91], v232, s[52:55], s76 offen offset:2048 sc1 \n"
    "v_mfma_f32_16x16x32_bf16 v[28:31], a[12:15], a[32:35], v[28:31]\n"
    "ds_read_b64_tr_b16 a[4:5], v246 offset:16384\n"
    "v_mfma_f32_16x16x32_bf16 v[32:35], a[12:15], a[36:39], v[32:35]\n"
    "s_waitcnt lgkmcnt(7)\n"
    "ds_read_b64_tr_b16 a[6:7], v246 offset:18432\n"
    "v_mfma_f32_16x16x32_bf16 v[4:7], a[16:19], a[40:43], v[4:7]\n"
    "ds_read_b64_tr_b16 a[8:9], v247 offset:16384\n"
    "v_mfma_f32_16x16x32_bf16 v[8:11], a[16:19], a[44:47], v[8:11]\n"
    "ds_read_b64_tr_b16 a[10:11], v247 offset:18432\n"
    "buffer_load_dwordx4 a[92:95], v232, s[52:55], s76 offen offset:3072 sc1 \n"
    "v_mfma_f32_16x16x32_bf16 v[12:15], a[20:23], a[40:43], v[12:15]\n"
    "ds_read_b64_tr_b16 a[12:13], v248 offset:16384\n"
    "v_mfma_f32_16x16x32_bf16 v[16:19], a[20:23], a[44:47], v[16:19]\n"
    "s_waitcnt lgkmcnt(7)\n"
    "ds_read_b64_tr_b16 a[14:15], v248 offset:18432\n"
    "v_mfma_f32_16x16x32_bf16 v[20:23], a[24:27], a[40:43], v[20:23]\n"
    "ds_read_b64_tr_b16 a[16:17], v245 offset:20480\n"
    "v_mfma_f32_16x16x32_bf16 v[24:27], a[24:27], a[44:47], v[24:27]\n"
    "ds_read_b64_tr_b16 a[18:19], v245 offset:22528\n"
    "buffer_load_dwordx4 a[96:99], v232, s[52:55], s78 offen offset:0 sc1 \n"
    "v_mfma_f32_16x16x32_bf16 v[28:31], a[28:31], a[40:43], v[28:31]\n"
    "ds_read_b64_tr_b16 a[20:21], v246 offset:20480\n"
    "v_mfma_f32_16x16x32_bf16 v[32:35], a[28:31], a[44:47], v[32:35]\n"
    "s_waitcnt lgkmcnt(7)\n"
    "ds_read_b64_tr_b16 a[22:23], v246 offset:22528\n"
    "v_mfma_f32_16x16x32_bf16 v[36:39], a[0:3], a[32:35], v[36:39]\n"
    "ds_read_b64_tr_b16 a[24:25], v247 offset:20480\n"
    "v_mfma_f32_16x16x32_bf16 v[40:43], a[0:3], a[36:39], v[40:43]\n"
    "ds_read_b64_tr_b16 a[26:27], v247 offset:22528\n"
    "buffer_load_dwordx4 a[100:103], v232, s[52:55], s78 offen offset:1024 sc1 \n"
    "v_mfma_f32_16x16x32_bf16 v[44:47], a[4:7], a[32:35], v[44:47]\n"
    "ds_read_b64_tr_b16 a[28:29], v248 offset:20480\n"
    "v_mfma_f32_16x16x32_bf16 v[48:51], a[4:7], a[36:39], v[48:51]\n"
    "s_waitcnt lgkmcnt(7)\n"
    "ds_read_b64_tr_b16 a[30:31], v248 offset:22528\n"
    "v_mfma_f32_16x16x32_bf16 v[52:55], a[8:11], a[32:35], v[52:55]\n"
    "s_barrier\n"
    "ds_read_b64_tr_b16 a[0:1], v241 offset:0\n"
    "v_mfma_f32_16x16x32_bf16 v[56:59], a[8:11], a[36:39], v[56:59]\n"
    "ds_read_b64_tr_b16 a[2:3], v241 offset:2048\n"
    "buffer_load_dwordx4 a[104:107], v232, s[52:55], s78 offen offset:2048 sc1 \n"
    "v_mfma_f32_16x16x32_bf16 v[60:63], a[12:15], a[32:35], v[60:63]\n"
    "ds_read_b64_tr_b16 a[4:5], v242 offset:0\n"
    "v_mfma_f32_16x16x32_bf16 v[64:67], a[12:15], a[36:39], v[64:67]\n"
    "s_waitcnt vmcnt(14) lgkmcnt(7)\n"
    "ds_read_b64_tr_b16 a[6:7], v242 offset:2048\n"
    "buffer_store_byte v255, v253, s[64:67], s92 offen offset:8 sc1\n"
    "v_mfma_f32_16x16x32_bf16 v[36:39], a[16:19], a[40:43], v[36:39]\n"
    "ds_read_b64_tr_b16 a[8:9], v243 offset:0\n"
    "v_mfma_f32_16x16x32_bf16 v[40:43], a[16:19], a[44:47], v[40:43]\n"
    "ds_read_b64_tr_b16 a[10:11], v243 offset:2048\n"
    "buffer_load_dwordx4 a[108:111], v232, s[52:55], s78 offen offset:3072 sc1 \n"
    "v_mfma_f32_16x16x32_bf16 v[44:47], a[20:23], a[40:43], v[44:47]\n"
    "ds_read_b64_tr_b16 a[12:13], v244 offset:0\n"
    "v_mfma_f32_16x16x32_bf16 v[48:51], a[20:23], a[44:47], v[48:51]\n"
    "s_waitcnt lgkmcnt(7)\n"
    "ds_read_b64_tr_b16 a[14:15], v244 offset:2048\n"
    "v_mfma_f32_16x16x32_bf16 v[52:55], a[24:27], a[40:43], v[52:55]\n"
    "ds_read_b64_tr_b16 a[16:17], v241 offset:4096\n"
    "v_mfma_f32_16x16x32_bf16 v[56:59], a[24:27], a[44:47], v[56:59]\n"
    "ds_read_b64_tr_b16 a[18:19], v241 offset:6144\n"
    "buffer_load_dwordx4 a[112:115], v232, s[52:55], s80 offen offset:0 sc1 \n"
    "v_mfma_f32_16x16x32_bf16 v[60:63], a[28:31], a[40:43], v[60:63]\n"
    "ds_read_b64_tr_b16 a[20:21], v242 offset:4096\n"
    "v_mfma_f32_16x16x32_bf16 v[64:67], a[28:31], a[44:47], v[64:67]\n"
    "s_waitcnt vmcnt(14) lgkmcnt(7)\n"
    "ds_read_b64_tr_b16 a[22:23], v242 offset:6144\n"
    "v_mfma_f32_16x16x32_bf16 v[4:7], a[0:3], a[48:51], v[4:7]\n"
    "ds_read_b64_tr_b16 a[24:25], v243 offset:4096\n"
    "v_mfma_f32_16x16x32_bf16 v[8:11], a[0:3], a[52:55], v[8:11]\n"
    "ds_read_b64_tr_b16 a[26:27], v243 offset:6144\n"
    "buffer_load_dwordx4 a[116:119], v232, s[52:55], s80 offen offset:1024 sc1 \n"
    "v_mfma_f32_16x16x32_bf16 v[12:15], a[4:7], a[48:51], v[12:15]\n"
    "ds_read_b64_tr_b16 a[28:29], v244 offset:4096\n"
    "v_mfma_f32_16x16x32_bf16 v[16:19], a[4:7], a[52:55], v[16:19]\n"
    "s_waitcnt lgkmcnt(7)\n"
    "ds_read_b64_tr_b16 a[30:31], v244 offset:6144\n"
    "v_mfma_f32_16x16x32_bf16 v[20:23], a[8:11], a[48:51], v[20:23]\n"
    "ds_read_b64_tr_b16 a[0:1], v241 offset:8192\n"
    "v_mfma_f32_16x16x32_bf16 v[24:27], a[8:11], a[52:55], v[24:27]\n"
    "ds_read_b64_tr_b16 a[2:3], v241 offset:10240\n"
    "buffer_load_dwordx4 a[120:123], v232, s[52:55], s80 offen offset:2048 sc1 \n"
    "v_mfma_f32_16x16x32_bf16 v[28:31], a[12:15], a[48:51], v[28:31]\n"
    "ds_read_b64_tr_b16 a[4:5], v242 offset:8192\n"
    "v_mfma_f32_16x16x32_bf16 v[32:35], a[12:15], a[52:55], v[32:35]\n"
    "s_waitcnt lgkmcnt(7)\n"
    "ds_read_b64_tr_b16 a[6:7], v242 offset:10240\n"
    "v_mfma_f32_16x16x32_bf16 v[4:7], a[16:19], a[56:59], v[4:7]\n"
    "ds_read_b64_tr_b16 a[8:9], v243 offset:8192\n"
    "v_mfma_f32_16x16x32_bf16 v[8:11], a[16:19], a[60:63], v[8:11]\n"
    "ds_read_b64_tr_b16 a[10:11], v243 offset:10240\n"
    "buffer_load_dwordx4 a[124:127], v232, s[52:55], s80 offen offset:3072 sc1 \n"
    "v_mfma_f32_16x16x32_bf16 v[12:15], a[20:23], a[56:59], v[12:15]\n"
    "ds_read_b64_tr_b16 a[12:13], v244 offset:8192\n"
    "v_mfma_f32_16x16x32_bf16 v[16:19], a[20:23], a[60:63], v[16:19]\n"
    "s_waitcnt lgkmcnt(7)\n"
    "ds_read_b64_tr_b16 a[14:15], v244 offset:10240\n"
    "v_mfma_f32_16x16x32_bf16 v[20:23], a[24:27], a[56:59], v[20:23]\n"
    "ds_read_b64_tr_b16 a[16:17], v241 offset:12288\n"
    "v_mfma_f32_16x16x32_bf16 v[24:27], a[24:27], a[60:63], v[24:27]\n"
    "ds_read_b64_tr_b16 a[18:19], v241 offset:14336\n"
    "buffer_load_dwordx4 a[128:131], v232, s[52:55], s82 offen offset:0 sc1 \n"
    "v_mfma_f32_16x16x32_bf16 v[28:31], a[28:31], a[56:59], v[28:31]\n"
    "ds_read_b64_tr_b16 a[20:21], v242 offset:12288\n"
    "v_mfma_f32_16x16x32_bf16 v[32:35], a[28:31], a[60:63], v[32:35]\n"
    "s_waitcnt lgkmcnt(7)\n"
    "ds_read_b64_tr_b16 a[22:23], v242 offset:14336\n"
    "v_mfma_f32_16x16x32_bf16 v[36:39], a[0:3], a[48:51], v[36:39]\n"
    "s_buffer_load_dword s10, s[64:67], s91 offset:8 glc\n"
    "ds_read_b64_tr_b16 a[24:25], v243 offset:12288\n"
    "v_mfma_f32_16x16x32_bf16 v[40:43], a[0:3], a[52:55], v[40:43]\n"
    "ds_read_b64_tr_b16 a[26:27], v243 offset:14336\n"
    "buffer_load_dwordx4 a[132:135], v232, s[52:55], s82 offen offset:1024 sc1 \n"
    "v_mfma_f32_16x16x32_bf16 v[44:47], a[4:7], a[48:51], v[44:47]\n"
    "ds_read_b64_tr_b16 a[28:29], v244 offset:12288\n"
    "v_mfma_f32_16x16x32_bf16 v[48:51], a[4:7], a[52:55], v[48:51]\n"
    "s_waitcnt lgkmcnt(7)\n"
    "ds_read_b64_tr_b16 a[30:31], v244 offset:14336\n"
    "v_mfma_f32_16x16x32_bf16 v[52:55], a[8:11], a[48:51], v[52:55]\n"
    "s_barrier\n"
    "ds_read_b64_tr_b16 a[0:1], v237 offset:0\n"
    "v_mfma_f32_16x16x32_bf16 v[56:59], a[8:11], a[52:55], v[56:59]\n"
    "s_add_u32 s99, s98, s80\n"
    "ds_read_b64_tr_b16 a[2:3], v237 offset:2048\n"
    "buffer_load_dwordx4 a[136:139], v232, s[52:55], s82 offen offset:2048 sc1 \n"
    "v_mfma_f32_16x16x32_bf16 v[60:63], a[12:15], a[48:51], v[60:63]\n"
    "s_and_b32 s99, s99, 0x1fffff\n"
    "ds_read_b64_tr_b16 a[4:5], v238 offset:0\n"
    "v_mfma_f32_16x16x32_bf16 v[64:67], a[12:15], a[52:55], v[64:67]\n"
    "s_waitcnt lgkmcnt(7)\n"
    "ds_read_b64_tr_b16 a[6:7], v238 offset:2048\n"
    "v_mfma_f32_16x16x32_bf16 v[36:39], a[16:19], a[56:59], v[36:39]\n"
    "s_buffer_load_dwordx2 s[30:31], s[40:43], s99 offset:60\n"
    "ds_read_b64_tr_b16 a[8:9], v239 offset:0\n"
    "v_mfma_f32_16x16x32_bf16 v[40:43], a[16:19], a[60:63], v[40:43]\n"
    "ds_read_b64_tr_b16 a[10:11], v239 offset:2048\n"
    "buffer_load_dwordx4 a[140:143], v232, s[52:55], s82 offen offset:3072 sc1 \n"
    "v_mfma_f32_16x16x32_bf16 v[44:47], a[20:23], a[56:59], v[44:47]\n"
    "ds_read_b64_tr_b16 a[12:13], v240 offset:0\n"
    "v_mfma_f32_16x16x32_bf16 v[48:51], a[20:23], a[60:63], v[48:51]\n"
    "s_waitcnt lgkmcnt(7)\n"
    "ds_read_b64_tr_b16 a[14:15], v240 offset:2048\n"
    "v_mfma_f32_16x16x32_bf16 v[52:55], a[24:27], a[56:59], v[52:55]\n"
    "s_buffer_load_dword s12, s[64:67], s91 offset:8 glc\n"
    "ds_read_b64_tr_b16 a[16:17], v237 offset:4096\n"
    "v_mfma_f32_16x16x32_bf16 v[56:59], a[24:27], a[60:63], v[56:59]\n"
    "s_add_u32 m0, s87, 98304\n"
    "ds_read_b64_tr_b16 a[18:19], v237 offset:6144\n"
    "buffer_load_dwordx4 v228, s[40:43], s77 offen offset:0 lds\n"
    "v_mfma_f32_16x16x32_bf16 v[60:63], a[28:31], a[56:59], v[60:63]\n"
    "ds_read_b64_tr_b16 a[20:21], v238 offset:4096\n"
    "v_mfma_f32_16x16x32_bf16 v[64:67], a[28:31], a[60:63], v[64:67]\n"
    "s_waitcnt vmcnt(18) lgkmcnt(7)\n"
    "ds_read_b64_tr_b16 a[22:23], v238 offset:6144\n"
    "v_mfma_f32_16x16x32_bf16 v[4:7], a[0:3], a[64:67], v[4:7]\n"
    "ds_read_b64_tr_b16 a[24:25], v239 offset:4096\n"
    "v_mfma_f32_16x16x32_bf16 v[8:11], a[0:3], a[68:71], v[8:11]\n"
    "ds_read_b64_tr_b16 a[26:27], v239 offset:6144\n"
    "buffer_load_dwordx4 v229, s[40:43], s77 offen offset:1024 lds\n"
    "v_mfma_f32_16x16x32_bf16 v[12:15], a[4:7], a[64:67], v[12:15]\n"
    "s_buffer_load_dword s11, s[64:67], s91 offset:8 glc\n"
    "ds_read_b64_tr_b16 a[28:29], v240 offset:4096\n"
    "v_mfma_f32_16x16x32_bf16 v[16:19], a[4:7], a[68:71], v[16:19]\n"
    "s_waitcnt lgkmcnt(7)\n"
    "ds_read_b64_tr_b16 a[30:31], v240 offset:6144\n"
    "v_mfma_f32_16x16x32_bf16 v[20:23], a[8:11], a[64:67], v[20:23]\n"
    "ds_read_b64_tr_b16 a[0:1], v241 offset:16384\n"
    "v_mfma_f32_16x16x32_bf16 v[24:27], a[8:11], a[68:71], v[24:27]\n"
    "s_add_u32 m0, s87, 106496\n"
    "ds_read_b64_tr_b16 a[2:3], v241 offset:18432\n"
    "buffer_load_dwordx4 v228, s[40:43], s79 offen offset:0 lds\n"
    "v_mfma_f32_16x16x32_bf16 v[28:31], a[12:15], a[64:67], v[28:31]\n"
    "ds_read_b64_tr_b16 a[4:5], v242 offset:16384\n"
    "v_mfma_f32_16x16x32_bf16 v[32:35], a[12:15], a[68:71], v[32:35]\n"
    "s_waitcnt lgkmcnt(7)\n"
    "ds_read_b64_tr_b16 a[6:7], v242 offset:18432\n"
    "v_mfma_f32_16x16x32_bf16 v[4:7], a[16:19], a[72:75], v[4:7]\n"
    "ds_read_b64_tr_b16 a[8:9], v243 offset:16384\n"
    "v_mfma_f32_16x16x32_bf16 v[8:11], a[16:19], a[76:79], v[8:11]\n"
    "ds_read_b64_tr_b16 a[10:11], v243 offset:18432\n"
    "buffer_load_dwordx4 v229, s[40:43], s79 offen offset:1024 lds\n"
    "v_mfma_f32_16x16x32_bf16 v[12:15], a[20:23], a[72:75], v[12:15]\n"
    "ds_read_b64_tr_b16 a[12:13], v244 offset:16384\n"
    "v_mfma_f32_16x16x32_bf16 v[16:19], a[20:23], a[76:79], v[16:19]\n"
    "s_waitcnt lgkmcnt(7)\n"
    "ds_read_b64_tr_b16 a[14:15], v244 offset:18432\n"
    "v_mfma_f32_16x16x32_bf16 v[20:23], a[24:27], a[72:75], v[20:23]\n"
    "s_add_u32 m0, s87, 114688\n"
    "ds_read_b64_tr_b16 a[16:17], v241 offset:20480\n"
    "v_mfma_f32_16x16x32_bf16 v[24:27], a[24:27], a[76:79], v[24:27]\n"
    "s_buffer_load_dword s14, s[64:67], s91 offset:12 glc\n"
    "ds_read_b64_tr_b16 a[18:19], v241 offset:22528\n"
    "buffer_load_dwordx4 v228, s[40:43], s81 offen offset:0 lds\n"
    "v_mfma_f32_16x16x32_bf16 v[28:31], a[28:31], a[72:75], v[28:31]\n"
    "s_buffer_load_dword s13, s[64:67], s91 offset:8 glc\n"
    "ds_read_b64_tr_b16 a[20:21], v242 offset:20480\n"
    "v_mfma_f32_16x16x32_bf16 v[32:35], a[28:31], a[76:79], v[32:35]\n"
    "s_waitcnt lgkmcnt(7)\n"
    "ds_read_b64_tr_b16 a[22:23], v242 offset:22528\n"
    "v_mfma_f32_16x16x32_bf16 v[36:39], a[0:3], a[64:67], v[36:39]\n"
    "ds_read_b64_tr_b16 a[24:25], v243 offset:20480\n"
    "v_mfma_f32_16x16x32_bf16 v[40:43], a[0:3], a[68:71], v[40:43]\n"
    "ds_read_b64_tr_b16 a[26:27], v243 offset:22528\n"
    "buffer_load_dwordx4 v229, s[40:43], s81 offen offset:1024 lds\n"
    "v_mfma_f32_16x16x32_bf16 v[44:47], a[4:7], a[64:67], v[44:47]\n"
    "ds_read_b64_tr_b16 a[28:29], v244 offset:20480\n"
    "v_mfma_f32_16x16x32_bf16 v[48:51], a[4:7], a[68:71], v[48:51]\n"
    "s_waitcnt lgkmcnt(7)\n"
    "ds_read_b64_tr_b16 a[30:31], v244 offset:22528\n"
    "v_mfma_f32_16x16x32_bf16 v[52:55], a[8:11], a[64:67], v[52:55]\n"
    "s_barrier\n"
    "ds_read_b64_tr_b16 a[0:1], v237 offset:8192\n"
    "v_mfma_f32_16x16x32_bf16 v[56:59], a[8:11], a[68:71], v[56:59]\n"
    "s_add_u32 m0, s87, 122880\n"
    "ds_read_b64_tr_b16 a[2:3], v237 offset:10240\n"
    "buffer_load_dwordx4 v228, s[40:43], s83 offen offset:0 lds\n"
    "v_mfma_f32_16x16x32_bf16 v[60:63], a[12:15], a[64:67], v[60:63]\n"
    "ds_read_b64_tr_b16 a[4:5], v238 offset:8192\n"
    "v_mfma_f32_16x16x32_bf16 v[64:67], a[12:15], a[68:71], v[64:67]\n"
    "s_waitcnt lgkmcnt(7)\n"
    "ds_read_b64_tr_b16 a[6:7], v238 offset:10240\n"
    "v_mfma_f32_16x16x32_bf16 v[36:39], a[16:19], a[72:75], v[36:39]\n"
    "ds_read_b64_tr_b16 a[8:9], v239 offset:8192\n"
    "v_mfma_f32_16x16x32_bf16 v[40:43], a[16:19], a[76:79], v[40:43]\n"
    "ds_read_b64_tr_b16 a[10:11], v239 offset:10240\n"
    "buffer_load_dwordx4 v229, s[40:43], s83 offen offset:1024 lds\n"
    "v_mfma_f32_16x16x32_bf16 v[44:47], a[20:23], a[72:75], v[44:47]\n"
    "ds_read_b64_tr_b16 a[12:13], v240 offset:8192\n"
    "v_mfma_f32_16x16x32_bf16 v[48:51], a[20:23], a[76:79], v[48:51]\n"
    "s_waitcnt lgkmcnt(7)\n"
    "ds_read_b64_tr_b16 a[14:15], v240 offset:10240\n"
    "v_mfma_f32_16x16x32_bf16 v[52:55], a[24:27], a[72:75], v[52:55]\n"
    "s_add_u32 s99, s98, s82\n"
    "ds_read_b64_tr_b16 a[16:17], v237 offset:12288\n"
    "v_mfma_f32_16x16x32_bf16 v[56:59], a[24:27], a[76:79], v[56:59]\n"
    "s_add_u32 m0, s87, 131072\n"
    "ds_read_b64_tr_b16 a[18:19], v237 offset:14336\n"
    "buffer_load_dwordx4 v230, s[40:43], s78 offen offset:0 lds\n"
    "v_mfma_f32_16x16x32_bf16 v[60:63], a[28:31], a[72:75], v[60:63]\n"
    "s_buffer_load_dword s15, s[64:67], s91 offset:12 glc\n"
    "ds_read_b64_tr_b16 a[20:21], v238 offset:12288\n"
    "v_mfma_f32_16x16x32_bf16 v[64:67], a[28:31], a[76:79], v[64:67]\n"
    "s_waitcnt vmcnt(22) lgkmcnt(7)\n"
    "ds_read_b64_tr_b16 a[22:23], v238 offset:14336\n"
    "v_mfma_f32_16x16x32_bf16 v[4:7], a[0:3], a[80:83], v[4:7]\n"
    "s_or_b32 s93, s10, s11\n"
    "s_andn2_b32 s93, 0x100, s93\n"
    "s_cbranch_scc1 L_sp1_0_%=\n"
    "L_bp1_0_%=:\n"
    "ds_read_b64_tr_b16 a[24:25], v239 offset:12288\n"
    "v_mfma_f32_16x16x32_bf16 v[8:11], a[0:3], a[84:87], v[8:11]\n"
    "s_and_b32 s99, s99, 0x1fffff\n"
    "ds_read_b64_tr_b16 a[26:27], v239 offset:14336\n"
    "buffer_load_dwordx4 v231, s[40:43], s78 offen offset:1024 lds\n"
    "v_mfma_f32_16x16x32_bf16 v[12:15], a[4:7], a[80:83], v[12:15]\n"
    "s_buffer_load_dwordx2 s[30:31], s[40:43], s99 offset:60\n"
    "ds_read_b64_tr_b16 a[28:29], v240 offset:12288\n"
    "v_mfma_f32_16x16x32_bf16 v[16:19], a[4:7], a[84:87], v[16:19]\n"
    "s_waitcnt lgkmcnt(7)\n"
    "ds_read_b64_tr_b16 a[30:31], v240 offset:14336\n"
    "v_mfma_f32_16x16x32_bf16 v[20:23], a[8:11], a[80:83], v[20:23]\n"
    "s_add_u32 s99, s98, s71\n"
    "ds_read_b64_tr_b16 a[0:1], v237 offset:16384\n"
    "v_mfma_f32_16x16x32_bf16 v[24:27], a[8:11], a[84:87], v[24:27]\n"
    "s_and_b32 s99, s99, 0x1fffff\n"
    "ds_read_b64_tr_b16 a[2:3], v237 offset:18432\n"
    "buffer_load_dwordx4 a[144:147], v232, s[52:55], s71 offen offset:0 sc1 \n"
    "v_mfma_f32_16x16x32_bf16 v[28:31], a[12:15], a[80:83], v[28:31]\n"
    "s_buffer_load_dwordx2 s[30:31], s[40:43], s99 offset:60\n"
    "ds_read_b64_tr_b16 a[4:5], v238 offset:16384\n"
    "v_mfma_f32_16x16x32_bf16 v[32:35], a[12:15], a[84:87], v[32:35]\n"
    "s_waitcnt lgkmcnt(7)\n"
    "ds_read_b64_tr_b16 a[6:7], v238 offset:18432\n"
    "v_mfma_f32_16x16x32_bf16 v[4:7], a[16:19], a[88:91], v[4:7]\n"
    "ds_read_b64_tr_b16 a[8:9], v239 offset:16384\n"
    "v_mfma_f32_16x16x32_bf16 v[8:11], a[16:19], a[92:95], v[8:11]\n"
    "ds_read_b64_tr_b16 a[10:11], v239 offset:18432\n"
    "buffer_load_dwordx4 a[148:151], v232, s[52:55], s71 offen offset:1024 sc1 \n"
    "v_mfma_f32_16x16x32_bf16 v[12:15], a[20:23], a[88:91], v[12:15]\n"
    "ds_read_b64_tr_b16 a[12:13], v240 offset:16384\n"
    "v_mfma_f32_16x16x32_bf16 v[16:19], a[20:23], a[92:95], v[16:19]\n"
    "s_waitcnt lgkmcnt(7)\n"
    "ds_read_b64_tr_b16 a[14:15], v240 offset:18432\n"
    "v_mfma_f32_16x16x32_bf16 v[20:23], a[24:27], a[88:91], v[20:23]\n"
    "s_or_b32 s93, s12, s13\n"
    "s_andn2_b32 s93, 0x1010000, s93\n"
    "s_cbranch_scc1 L_sp1_1_%=\n"
    "L_bp1_1_%=:\n"
    "ds_read_b64_tr_b16 a[16:17], v237 offset:20480\n"
    "v_mfma_f32_16x16x32_bf16 v[24:27], a[24:27], a[92:95], v[24:27]\n"
    "s_add_u32 s99, s98, s73\n"
    "ds_read_b64_tr_b16 a[18:19], v237 offset:22528\n"
    "buffer_load_dwordx4 a[152:155], v232, s[52:55], s71 offen offset:2048 sc1 \n"
    "v_mfma_f32_16x16x32_bf16 v[28:31], a[28:31], a[88:91], v[28:31]\n"
    "s_and_b32 s99, s99, 0x1fffff\n"
    "ds_read_b64_tr_b16 a[20:21], v238 offset:20480\n"
    "v_mfma_f32_16x16x32_bf16 v[32:35], a[28:31], a[92:95], v[32:35]\n"
    "s_waitcnt lgkmcnt(7)\n"
    "ds_read_b64_tr_b16 a[22:23], v238 offset:22528\n"
    "v_mfma_f32_16x16x32_bf16 v[36:39], a[0:3], a[80:83], v[36:39]\n"
    "s_buffer_load_dwordx2 s[30:31], s[40:43], s99 offset:60\n"
    "ds_read_b64_tr_b16 a[24:25], v239 offset:20480\n"
    "v_mfma_f32_16x16x32_bf16 v[40:43], a[0:3], a[84:87], v[40:43]\n"
    "ds_read_b64_tr_b16 a[26:27], v239 offset:22528\n"
    "buffer_load_dwordx4 a[156:159], v232, s[52:55], s71 offen offset:3072 sc1 \n"
    "v_mfma_f32_16x16x32_bf16 v[44:47], a[4:7], a[80:83], v[44:47]\n"
    "ds_read_b64_tr_b16 a[28:29], v240 offset:20480\n"
    "v_mfma_f32_16x16x32_bf16 v[48:51], a[4:7], a[84:87], v[48:51]\n"
    "s_waitcnt lgkmcnt(7)\n"
    "ds_read_b64_tr_b16 a[30:31], v240 offset:22528\n"
    "v_mfma_f32_16x16x32_bf16 v[52:55], a[8:11], a[80:83], v[52:55]\n"
    "s_barrier\n"
    "ds_read_b64_tr_b16 a[0:1], v237 offset:24576\n"
    "v_mfma_f32_16x16x32_bf16 v[56:59], a[8:11], a[84:87], v[56:59]\n"
    "s_add_u32 s99, s98, s75\n"
    "ds_read_b64_tr_b16 a[2:3], v237 offset:26624\n"
    "buffer_load_dwordx4 a[160:163], v232, s[52:55], s73 offen offset:0 sc1 \n"
    "v_mfma_f32_16x16x32_bf16 v[60:63], a[12:15], a[80:83], v[60:63]\n"
    "s_and_b32 s99, s99, 0x1fffff\n"
    "ds_read_b64_tr_b16 a[4:5], v238 offset:24576\n"
    "v_mfma_f32_16x16x32_bf16 v[64:67], a[12:15], a[84:87], v[64:67]\n"
    "s_waitcnt lgkmcnt(7)\n"
    "ds_read_b64_tr_b16 a[6:7], v238 offset:26624\n"
    "v_mfma_f32_16x16x32_bf16 v[36:39], a[16:19], a[88:91], v[36:39]\n"
    "s_buffer_load_dwordx2 s[30:31], s[40:43], s99 offset:60\n"
    "ds_read_b64_tr_b16 a[8:9], v239 offset:24576\n"
    "v_mfma_f32_16x16x32_bf16 v[40:43], a[16:19], a[92:95], v[40:43]\n"
    "ds_read_b64_tr_b16 a[10:11], v239 offset:26624\n"
    "buffer_load_dwordx4 a[164:167], v232, s[52:55], s73 offen offset:1024 sc1 \n"
    "v_mfma_f32_16x16x32_bf16 v[44:47], a[20:23], a[88:91], v[44:47]\n"
    "ds_read_b64_tr_b16 a[12:13], v240 offset:24576\n"
    "v_mfma_f32_16x16x32_bf16 v[48:51], a[20:23], a[92:95], v[48:51]\n"
    "s_waitcnt lgkmcnt(7)\n"
    "ds_read_b64_tr_b16 a[14:15], v240 offset:26624\n"
    "v_mfma_f32_16x16x32_bf16 v[52:55], a[24:27], a[88:91], v[52:55]\n"
    "ds_read_b64_tr_b16 a[16:17], v237 offset:28672\n"
    "v_mfma_f32_16x16x32_bf16 v[56:59], a[24:27], a[92:95], v[56:59]\n"
    "ds_read_b64_tr_b16 a[18:19], v237 offset:30720\n"
    "buffer_load_dwordx4 a[168:171], v232, s[52:55], s73 offen offset:2048 sc1 \n"
    "v_mfma_f32_16x16x32_bf16 v[60:63], a[28:31], a[88:91], v[60:63]\n"
    "s_or_b32 s93, s14, s15\n"
    "s_andn2_b32 s93, 0x1010101, s93\n"
    "s_cbranch_scc1 L_sp1_2_%=\n"
    "L_bp1_2_%=:\n"
    "ds_read_b64_tr_b16 a[20:21], v238 offset:28672\n"
    "v_mfma_f32_16x16x32_bf16 v[64:67], a[28:31], a[92:95], v[64:67]\n"
    "s_waitcnt vmcnt(25) lgkmcnt(7)\n"
    "ds_read_b64_tr_b16 a[22:23], v238 offset:30720\n"
    "v_mfma_f32_16x16x32_bf16 v[4:7], a[0:3], a[96:99], v[4:7]\n"
    "s_add_u32 s99, s98, s77\n"
    "ds_read_b64_tr_b16 a[24:25], v239 offset:28672\n"
    "v_mfma_f32_16x16x32_bf16 v[8:11], a[0:3], a[100:103], v[8:11]\n"
    "s_and_b32 s99, s99, 0x1fffff\n"
    "ds_read_b64_tr_b16 a[26:27], v239 offset:30720\n"
    "buffer_load_dwordx4 a[172:175], v232, s[52:55], s73 offen offset:3072 sc1 \n"
    "v_mfma_f32_16x16x32_bf16 v[12:15], a[4:7], a[96:99], v[12:15]\n"
    "s_buffer_load_dwordx2 s[30:31], s[40:43], s99 offset:60\n"
    "ds_read_b64_tr_b16 a[28:29], v240 offset:28672\n"
    "v_mfma_f32_16x16x32_bf16 v[16:19], a[4:7], a[100:103], v[16:19]\n"
    "s_waitcnt lgkmcnt(7)\n"
    "ds_read_b64_tr_b16 a[30:31], v240 offset:30720\n"
    "v_mfma_f32_16x16x32_bf16 v[20:23], a[8:11], a[96:99], v[20:23]\n"
    "ds_read_b64_tr_b16 a[0:1], v237 offset:32768\n"
    "v_mfma_f32_16x16x32_bf16 v[24:27], a[8:11], a[100:103], v[24:27]\n"
    "ds_read_b64_tr_b16 a[2:3], v237 offset:34816\n"
    "buffer_load_dwordx4 a[176:179], v232, s[52:55], s75 offen offset:0 sc1 \n"
    "v_mfma_f32_16x16x32_bf16 v[28:31], a[12:15], a[96:99], v[28:31]\n"
    "ds_read_b64_tr_b16 a[4:5], v238 offset:32768\n"
    "v_mfma_f32_16x16x32_bf16 v[32:35], a[12:15], a[100:103], v[32:35]\n"
    "s_waitcnt lgkmcnt(7)\n"
    "ds_read_b64_tr_b16 a[6:7], v238 offset:34816\n"
    "v_mfma_f32_16x16x32_bf16 v[4:7], a[16:19], a[104:107], v[4:7]\n"
    "s_add_u32 s99, s98, s79\n"
    "ds_read_b64_tr_b16 a[8:9], v239 offset:32768\n"
    "v_mfma_f32_16x16x32_bf16 v[8:11], a[16:19], a[108:111], v[8:11]\n"
    "s_and_b32 s99, s99, 0x1fffff\n"
    "ds_read_b64_tr_b16 a[10:11], v239 offset:34816\n"
    "buffer_load_dwordx4 a[180:183], v232, s[52:55], s75 offen offset:1024 sc1 \n"
    "v_mfma_f32_16x16x32_bf16 v[12:15], a[20:23], a[104:107], v[12:15]\n"
    "s_buffer_load_dwordx2 s[30:31], s[40:43], s99 offset:60\n"
    "ds_read_b64_tr_b16 a[12:13], v240 offset:32768\n"
    "v_mfma_f32_16x16x32_bf16 v[16:19], a[20:23], a[108:111], v[16:19]\n"
    "s_waitcnt lgkmcnt(7)\n"
    "ds_read_b64_tr_b16 a[14:15], v240 offset:34816\n"
    "v_mfma_f32_16x16x32_bf16 v[20:23], a[24:27], a[104:107], v[20:23]\n"
    "ds_read_b64_tr_b16 a[16:17], v237 offset:36864\n"
    "v_mfma_f32_16x16x32_bf16 v[24:27], a[24:27], a[108:111], v[24:27]\n"
    "ds_read_b64_tr_b16 a[18:19], v237 offset:38912\n"
    "buffer_load_dwordx4 a[184:187], v232, s[52:55], s75 offen offset:2048 sc1 \n"
    "v_mfma_f32_16x16x32_bf16 v[28:31], a[28:31], a[104:107], v[28:31]\n"
    "ds_read_b64_tr_b16 a[20:21], v238 offset:36864\n"
    "v_mfma_f32_16x16x32_bf16 v[32:35], a[28:31], a[108:111], v[32:35]\n"
    "s_waitcnt vmcnt(25) lgkmcnt(7)\n"
    "ds_read_b64_tr_b16 a[22:23], v238 offset:38912\n"
    "v_mfma_f32_16x16x32_bf16 v[4:7], a[0:3], a[112:115], v[4:7]\n"
    "ds_read_b64_tr_b16 a[24:25], v239 offset:36864\n"
    "v_mfma_f32_16x16x32_bf16 v[8:11], a[0:3], a[116:119], v[8:11]\n"
    "s_add_u32 s99, s98, s81\n"
    "ds_read_b64_tr_b16 a[26:27], v239 offset:38912\n"
    "buffer_load_dwordx4 a[188:191], v232, s[52:55], s75 offen offset:3072 sc1 \n"
    "v_mfma_f32_16x16x32_bf16 v[12:15], a[4:7], a[112:115], v[12:15]\n"
    "s_and_b32 s99, s99, 0x1fffff\n"
    "ds_read_b64_tr_b16 a[28:29], v240 offset:36864\n"
    "v_mfma_f32_16x16x32_bf16 v[16:19], a[4:7], a[116:119], v[16:19]\n"
    "s_waitcnt lgkmcnt(7)\n"
    "ds_read_b64_tr_b16 a[30:31], v240 offset:38912\n"
    "v_mfma_f32_16x16x32_bf16 v[20:23], a[8:11], a[112:115], v[20:23]\n"
    "s_barrier\n"
    "ds_read_b64_tr_b16 a[0:1], v237 offset:40960\n"
    "v_mfma_f32_16x16x32_bf16 v[24:27], a[8:11], a[116:119], v[24:27]\n"
    "s_buffer_load_dwordx2 s[30:31], s[40:43], s99 offset:60\n"
    "ds_read_b64_tr_b16 a[2:3], v237 offset:43008\n"
    "buffer_load_dwordx4 a[192:195], v232, s[52:55], s77 offen offset:0 sc1 \n"
    "v_mfma_f32_16x16x32_bf16 v[28:31], a[12:15], a[112:115], v[28:31]\n"
    "ds_read_b64_tr_b16 a[4:5], v238 offset:40960\n"
    "v_mfma_f32_16x16x32_bf16 v[32:35], a[12:15], a[116:119], v[32:35]\n"
    "s_waitcnt lgkmcnt(7)\n"
    "ds_read_b64_tr_b16 a[6:7], v238 offset:43008\n"
    "v_mfma_f32_16x16x32_bf16 v[4:7], a[16:19], a[120:123], v[4:7]\n"
    "ds_read_b64_tr_b16 a[8:9], v239 offset:40960\n"
    "v_mfma_f32_16x16x32_bf16 v[8:11], a[16:19], a[124:127], v[8:11]\n"
    "s_add_u32 s99, s98, s83\n"
    "ds_read_b64_tr_b16 a[10:11], v239 offset:43008\n"
    "buffer_load_dwordx4 a[196:199], v232, s[52:55], s77 offen offset:1024 sc1 \n"
    "v_mfma_f32_16x16x32_bf16 v[12:15], a[20:23], a[120:123], v[12:15]\n"
    "s_and_b32 s99, s99, 0x1fffff\n"
    "ds_read_b64_tr_b16 a[12:13], v240 offset:40960\n"
    "v_mfma_f32_16x16x32_bf16 v[16:19], a[20:23], a[124:127], v[16:19]\n"
    "s_waitcnt lgkmcnt(7)\n"
    "ds_read_b64_tr_b16 a[14:15], v240 offset:43008\n"
    "v_mfma_f32_16x16x32_bf16 v[20:23], a[24:27], a[120:123], v[20:23]\n"
    "s_buffer_load_dwordx2 s[30:31], s[40:43], s99 offset:60\n"
    "ds_read_b64_tr_b16 a[16:17], v237 offset:45056\n"
    "v_mfma_f32_16x16x32_bf16 v[24:27], a[24:27], a[124:127], v[24:27]\n"
    "ds_read_b64_tr_b16 a[18:19], v237 offset:47104\n"
    "buffer_load_dwordx4 a[200:203], v232, s[52:55], s77 offen offset:2048 sc1 \n"
    "v_mfma_f32_16x16x32_bf16 v[28:31], a[28:31], a[120:123], v[28:31]\n"
    "ds_read_b64_tr_b16 a[20:21], v238 offset:45056\n"
    "v_mfma_f32_16x16x32_bf16 v[32:35], a[28:31], a[124:127], v[32:35]\n"
    "s_waitcnt vmcnt(25) lgkmcnt(7)\n"
    "ds_read_b64_tr_b16 a[22:23], v238 offset:47104\n"
    "v_mfma_f32_16x16x32_bf16 v[4:7], a[0:3], a[128:131], v[4:7]\n"
    "ds_read_b64_tr_b16 a[24:25], v239 offset:45056\n"
    "v_mfma_f32_16x16x32_bf16 v[8:11], a[0:3], a[132:135], v[8:11]\n"
    "ds_read_b64_tr_b16 a[26:27], v239 offset:47104\n"
    "buffer_load_dwordx4 a[204:207], v232, s[52:55], s77 offen offset:3072 sc1 \n"
    "v_mfma_f32_16x16x32_bf16 v[12:15], a[4:7], a[128:131], v[12:15]\n"
    "ds_read_b64_tr_b16 a[28:29], v240 offset:45056\n"
    "v_mfma_f32_16x16x32_bf16 v[16:19], a[4:7], a[132:135], v[16:19]\n"
    "s_waitcnt lgkmcnt(7)\n"
    "ds_read_b64_tr_b16 a[30:31], v240 offset:47104\n"
    "v_mfma_f32_16x16x32_bf16 v[20:23], a[8:11], a[128:131], v[20:23]\n"
    "ds_read_b64_tr_b16 a[0:1], v237 offset:49152\n"
    "v_mfma_f32_16x16x32_bf16 v[24:27], a[8:11], a[132:135], v[24:27]\n"
    "ds_read_b64_tr_b16 a[2:3], v237 offset:51200\n"
    "buffer_load_dwordx4 a[208:211], v232, s[52:55], s79 offen offset:0 sc1 \n"
    "v_mfma_f32_16x16x32_bf16 v[28:31], a[12:15], a[128:131], v[28:31]\n"
    "ds_read_b64_tr_b16 a[4:5], v238 offset:49152\n"
    "v_mfma_f32_16x16x32_bf16 v[32:35], a[12:15], a[132:135], v[32:35]\n"
    "s_waitcnt lgkmcnt(7)\n"
    "ds_read_b64_tr_b16 a[6:7], v238 offset:51200\n"
    "v_mfma_f32_16x16x32_bf16 v[4:7], a[16:19], a[136:139], v[4:7]\n"
    "ds_read_b64_tr_b16 a[8:9], v239 offset:49152\n"
    "v_mfma_f32_16x16x32_bf16 v[8:11], a[16:19], a[140:143], v[8:11]\n"
    "ds_read_b64_tr_b16 a[10:11], v239 offset:51200\n"
    "buffer_load_dwordx4 a[212:215], v232, s[52:55], s79 offen offset:1024 sc1 \n"
    "v_mfma_f32_16x16x32_bf16 v[12:15], a[20:23], a[136:139], v[12:15]\n"
    "ds_read_b64_tr_b16 a[12:13], v240 offset:49152\n"
    "v_mfma_f32_16x16x32_bf16 v[16:19], a[20:23], a[140:143], v[16:19]\n"
    "s_waitcnt lgkmcnt(7)\n"
    "ds_read_b64_tr_b16 a[14:15], v240 offset:51200\n"
    "v_mfma_f32_16x16x32_bf16 v[20:23], a[24:27], a[136:139], v[20:23]\n"
    "s_add_u32 s99, s98, s68\n"
    "ds_read_b64_tr_b16 a[16:17], v237 offset:53248\n"
    "v_mfma_f32_16x16x32_bf16 v[24:27], a[24:27], a[140:143], v[24:27]\n"
    "s_and_b32 s99, s99, 0x1fffff\n"
    "ds_read_b64_tr_b16 a[18:19], v237 offset:55296\n"
    "buffer_load_dwordx4 a[216:219], v232, s[52:55], s79 offen offset:2048 sc1 \n"
    "v_mfma_f32_16x16x32_bf16 v[28:31], a[28:31], a[136:139], v[28:31]\n"
    "s_buffer_load_dwordx2 s[30:31], s[44:47], s99 offset:60\n"
    "ds_read_b64_tr_b16 a[20:21], v238 offset:53248\n"
    "v_mfma_f32_16x16x32_bf16 v[32:35], a[28:31], a[140:143], v[32:35]\n"
    "s_waitcnt vmcnt(15) lgkmcnt(7)\n"
    "ds_read_b64_tr_b16 a[22:23], v238 offset:55296\n"
    "v_mfma_f32_16x16x32_bf16 v[4:7], a[0:3], a[144:147], v[4:7]\n"
    "ds_read_b64_tr_b16 a[24:25], v239 offset:53248\n"
    "v_mfma_f32_16x16x32_bf16 v[8:11], a[0:3], a[148:151], v[8:11]\n"
    "ds_read_b64_tr_b16 a[26:27], v239 offset:55296\n"
    "buffer_load_dwordx4 a[220:223], v232, s[52:55], s79 offen offset:3072 sc1 \n"
    "v_mfma_f32_16x16x32_bf16 v[12:15], a[4:7], a[144:147], v[12:15]\n"
    "ds_read_b64_tr_b16 a[28:29], v240 offset:53248\n"
    "v_mfma_f32_16x16x32_bf16 v[16:19], a[4:7], a[148:151], v[16:19]\n"
    "s_waitcnt lgkmcnt(7)\n"
    "ds_read_b64_tr_b16 a[30:31], v240 offset:55296\n"
    "v_mfma_f32_16x16x32_bf16 v[20:23], a[8:11], a[144:147], v[20:23]\n"
    "s_barrier\n"
    "ds_read_b64_tr_b16 a[0:1], v237 offset:57344\n"
    "v_mfma_f32_16x16x32_bf16 v[24:27], a[8:11], a[148:151], v[24:27]\n"
    "s_add_u32 m0, s87, 139264\n"
    "ds_read_b64_tr_b16 a[2:3], v237 offset:59392\n"
    "buffer_load_dwordx4 v230, s[40:43], s80 offen offset:0 lds\n"
    "v_mfma_f32_16x16x32_bf16 v[28:31], a[12:15], a[144:147], v[28:31]\n"
    "ds_read_b64_tr_b16 a[4:5], v238 offset:57344\n"
    "v_mfma_f32_16x16x32_bf16 v[32:35], a[12:15], a[148:151], v[32:35]\n"
    "s_waitcnt lgkmcnt(7)\n"
    "ds_read_b64_tr_b16 a[6:7], v238 offset:59392\n"
    "v_mfma_f32_16x16x32_bf16 v[4:7], a[16:19], a[152:155], v[4:7]\n"
    "ds_read_b64_tr_b16 a[8:9], v239 offset:57344\n"
    "v_mfma_f32_16x16x32_bf16 v[8:11], a[16:19], a[156:159], v[8:11]\n"
    "ds_read_b64_tr_b16 a[10:11], v239 offset:59392\n"
    "buffer_load_dwordx4 v231, s[40:43], s80 offen offset:1024 lds\n"
    "v_mfma_f32_16x16x32_bf16 v[12:15], a[20:23], a[152:155], v[12:15]\n"
    "ds_read_b64_tr_b16 a[12:13], v240 offset:57344\n"
    "v_mfma_f32_16x16x32_bf16 v[16:19], a[20:23], a[156:159], v[16:19]\n"
    "s_waitcnt lgkmcnt(7)\n"
    "ds_read_b64_tr_b16 a[14:15], v240 offset:59392\n"
    "v_mfma_f32_16x16x32_bf16 v[20:23], a[24:27], a[152:155], v[20:23]\n"
    "ds_read_b64_tr_b16 a[16:17], v237 offset:61440\n"
    "v_mfma_f32_16x16x32_bf16 v[24:27], a[24:27], a[156:159], v[24:27]\n"
    "ds_read_b64_tr_b16 a[18:19], v237 offset:63488\n"
    "buffer_load_dwordx4 a[224:227], v232, s[52:55], s81 offen offset:0 sc1 \n"
    "v_mfma_f32_16x16x32_bf16 v[28:31], a[28:31], a[152:155], v[28:31]\n"
    "ds_read_b64_tr_b16 a[20:21], v238 offset:61440\n"
    "v_mfma_f32_16x16x32_bf16 v[32:35], a[28:31], a[156:159], v[32:35]\n"
    "s_waitcnt vmcnt(15) lgkmcnt(7)\n"
    "ds_read_b64_tr_b16 a[22:23], v238 offset:63488\n"
    "v_mfma_f32_16x16x32_bf16 v[4:7], a[0:3], a[160:163], v[4:7]\n"
    "s_add_u32 s99, s98, s69\n"
    "ds_read_b64_tr_b16 a[24:25], v239 offset:61440\n"
    "v_mfma_f32_16x16x32_bf16 v[8:11], a[0:3], a[164:167], v[8:11]\n"
    "s_and_b32 s99, s99, 0x1fffff\n"
    "ds_read_b64_tr_b16 a[26:27], v239 offset:63488\n"
    "buffer_load_dwordx4 a[228:231], v232, s[52:55], s81 offen offset:1024 sc1 \n"
    "v_mfma_f32_16x16x32_bf16 v[12:15], a[4:7], a[160:163], v[12:15]\n"
    "s_buffer_load_dwordx2 s[30:31], s[44:47], s99 offset:60\n"
    "ds_read_b64_tr_b16 a[28:29], v240 offset:61440\n"
    "v_mfma_f32_16x16x32_bf16 v[16:19], a[4:7], a[164:167], v[16:19]\n"
    "s_waitcnt lgkmcnt(7)\n"
    "ds_read_b64_tr_b16 a[30:31], v240 offset:63488\n"
    "v_mfma_f32_16x16x32_bf16 v[20:23], a[8:11], a[160:163], v[20:23]\n"
    "ds_read_b64_tr_b16 a[0:1], v241 offset:24576\n"
    "v_mfma_f32_16x16x32_bf16 v[24:27], a[8:11], a[164:167], v[24:27]\n"
    "ds_read_b64_tr_b16 a[2:3], v241 offset:26624\n"
    "buffer_load_dwordx4 a[232:235], v232, s[52:55], s81 offen offset:2048 sc1 \n"
    "v_mfma_f32_16x16x32_bf16 v[28:31], a[12:15], a[160:163], v[28:31]\n"
    "L_ip5_%=:\n"
    "s_cbranch_execz L_ip9_%=\n"
    "ds_read_b64_tr_b16 a[4:5], v242 offset:24576\n"
    "v_mfma_f32_16x16x32_bf16 v[32:35], a[12:15], a[164:167], v[32:35]\n"
    "s_waitcnt lgkmcnt(7)\n"
    "ds_read_b64_tr_b16 a[6:7], v242 offset:26624\n"
    "v_mfma_f32_16x16x32_bf16 v[4:7], a[16:19], a[168:171], v[4:7]\n"
    "ds_read_b64_tr_b16 a[8:9], v243 offset:24576\n"
    "v_mfma_f32_16x16x32_bf16 v[8:11], a[16:19], a[172:175], v[8:11]\n"
    "ds_read_b64_tr_b16 a[10:11], v243 offset:26624\n"
    "buffer_load_dwordx4 a[236:239], v232, s[52:55], s81 offen offset:3072 sc1 \n"
    "v_mfma_f32_16x16x32_bf16 v[12:15], a[20:23], a[168:171], v[12:15]\n"
    "ds_read_b64_tr_b16 a[12:13], v244 offset:24576\n"
    "v_mfma_f32_16x16x32_bf16 v[16:19], a[20:23], a[172:175], v[16:19]\n"
    "s_waitcnt lgkmcnt(7)\n"
    "ds_read_b64_tr_b16 a[14:15], v244 offset:26624\n"
    "v_mfma_f32_16x16x32_bf16 v[20:23], a[24:27], a[168:171], v[20:23]\n"
    "ds_read_b64_tr_b16 a[16:17], v241 offset:28672\n"
    "v_mfma_f32_16x16x32_bf16 v[24:27], a[24:27], a[172:175], v[24:27]\n"
    "ds_read_b64_tr_b16 a[18:19], v241 offset:30720\n"
    "buffer_load_dwordx4 a[240:243], v232, s[52:55], s83 offen offset:0 sc1 \n"
    "v_mfma_f32_16x16x32_bf16 v[28:31], a[28:31], a[168:171], v[28:31]\n"
    "ds_read_b64_tr_b16 a[20:21], v242 offset:28672\n"
    "v_mfma_f32_16x16x32_bf16 v[32:35], a[28:31], a[172:175], v[32:35]\n"
    "s_waitcnt vmcnt(15) lgkmcnt(7)\n"
    "ds_read_b64_tr_b16 a[22:23], v242 offset:30720\n"
    "v_mfma_f32_16x16x32_bf16 v[4:7], a[0:3], a[176:179], v[4:7]\n"
    "ds_read_b64_tr_b16 a[24:25], v243 offset:28672\n"
    "v_mfma_f32_16x16x32_bf16 v[8:11], a[0:3], a[180:183], v[8:11]\n"
    "s_add_u32 s99, s98, s70\n"
    "ds_read_b64_tr_b16 a[26:27], v243 offset:30720\n"
    "buffer_load_dwordx4 a[244:247], v232, s[52:55], s83 offen offset:1024 sc1 \n"
    "v_mfma_f32_16x16x32_bf16 v[12:15], a[4:7], a[176:179], v[12:15]\n"
    "s_and_b32 s99, s99, 0x1fffff\n"
    "ds_read_b64_tr_b16 a[28:29], v244 offset:28672\n"
    "v_mfma_f32_16x16x32_bf16 v[16:19], a[4:7], a[180:183], v[16:19]\n"
    "s_waitcnt lgkmcnt(7)\n"
    "ds_read_b64_tr_b16 a[30:31], v244 offset:30720\n"
    "v_mfma_f32_16x16x32_bf16 v[20:23], a[8:11], a[176:179], v[20:23]\n"
    "s_barrier\n"
    "ds_read_b64_tr_b16 a[0:1], v241 offset:32768\n"
    "v_mfma_f32_16x16x32_bf16 v[24:27], a[8:11], a[180:183], v[24:27]\n"
    "s_buffer_load_dwordx2 s[30:31], s[44:47], s99 offset:60\n"
    "ds_read_b64_tr_b16 a[2:3], v241 offset:34816\n"
    "buffer_load_dwordx4 a[248:251], v232, s[52:55], s83 offen offset:2048 sc1 \n"
    "v_mfma_f32_16x16x32_bf16 v[28:31], a[12:15], a[176:179], v[28:31]\n"
    "ds_read_b64_tr_b16 a[4:5], v242 offset:32768\n"
    "v_mfma_f32_16x16x32_bf16 v[32:35], a[12:15], a[180:183], v[32:35]\n"
    "s_waitcnt lgkmcnt(7)\n"
    "ds_read_b64_tr_b16 a[6:7], v242 offset:34816\n"
    "v_mfma_f32_16x16x32_bf16 v[4:7], a[16:19], a[184:187], v[4:7]\n"
    "ds_read_b64_tr_b16 a[8:9], v243 offset:32768\n"
    "v_mfma_f32_16x16x32_bf16 v[8:11], a[16:19], a[188:191], v[8:11]\n"
    "ds_read_b64_tr_b16 a[10:11], v243 offset:34816\n"
    "buffer_load_dwordx4 a[252:255], v232, s[52:55], s83 offen offset:3072 sc1 \n"
    "v_mfma_f32_16x16x32_bf16 v[12:15], a[20:23], a[184:187], v[12:15]\n"
    "ds_read_b64_tr_b16 a[12:13], v244 offset:32768\n"
    "v_mfma_f32_16x16x32_bf16 v[16:19], a[20:23], a[188:191], v[16:19]\n"
    "s_waitcnt lgkmcnt(7)\n"
    "ds_read_b64_tr_b16 a[14:15], v244 offset:34816\n"
    "v_mfma_f32_16x16x32_bf16 v[20:23], a[24:27], a[184:187], v[20:23]\n"
    "ds_read_b64_tr_b16 a[16:17], v241 offset:36864\n"
    "v_mfma_f32_16x16x32_bf16 v[24:27], a[24:27], a[188:191], v[24:27]\n"
    "s_add_u32 m0, s87, 65536\n"
    "ds_read_b64_tr_b16 a[18:19], v241 offset:38912\n"
    "buffer_load_dwordx4 v230, s[40:43], s82 offen offset:0 lds\n"
    "v_mfma_f32_16x16x32_bf16 v[28:31], a[28:31], a[184:187], v[28:31]\n"
    "ds_read_b64_tr_b16 a[20:21], v242 offset:36864\n"
    "v_mfma_f32_16x16x32_bf16 v[32:35], a[28:31], a[188:191], v[32:35]\n"
    "s_waitcnt vmcnt(15) lgkmcnt(7)\n"
    "ds_read_b64_tr_b16 a[22:23], v242 offset:38912\n"
    "v_mfma_f32_16x16x32_bf16 v[4:7], a[0:3], a[192:195], v[4:7]\n"
    "ds_read_b64_tr_b16 a[24:25], v243 offset:36864\n"
    "v_mfma_f32_16x16x32_bf16 v[8:11], a[0:3], a[196:199], v[8:11]\n"
    "ds_read_b64_tr_b16 a[26:27], v243 offset:38912\n"
    "buffer_load_dwordx4 v231, s[40:43], s82 offen offset:1024 lds\n"
    "v_mfma_f32_16x16x32_bf16 v[12:15], a[4:7], a[192:195], v[12:15]\n"
    "ds_read_b64_tr_b16 a[28:29], v244 offset:36864\n"
    "v_mfma_f32_16x16x32_bf16 v[16:19], a[4:7], a[196:199], v[16:19]\n"
    "s_waitcnt lgkmcnt(7)\n"
    "ds_read_b64_tr_b16 a[30:31], v244 offset:38912\n"
    "v_mfma_f32_16x16x32_bf16 v[20:23], a[8:11], a[192:195], v[20:23]\n"
    "s_add_u32 s99, s98, s72\n"
    "ds_read_b64_tr_b16 a[0:1], v241 offset:40960\n"
    "v_mfma_f32_16x16x32_bf16 v[24:27], a[8:11], a[196:199], v[24:27]\n"
    "s_add_u32 m0, s87, 147456\n"
    "ds_read_b64_tr_b16 a[2:3], v241 offset:43008\n"
    "buffer_load_dwordx4 v230, s[40:43], s71 offen offset:0 lds\n"
    "v_mfma_f32_16x16x32_bf16 v[28:31], a[12:15], a[192:195], v[28:31]\n"
    "s_and_b32 s99, s99, 0x1fffff\n"
    "ds_read_b64_tr_b16 a[4:5], v242 offset:40960\n"
    "v_mfma_f32_16x16x32_bf16 v[32:35], a[12:15], a[196:199], v[32:35]\n"
    "s_waitcnt lgkmcnt(7)\n"
    "ds_read_b64_tr_b16 a[6:7], v242 offset:43008\n"
    "v_mfma_f32_16x16x32_bf16 v[4:7], a[16:19], a[200:203], v[4:7]\n"
    "s_buffer_load_dwordx2 s[30:31], s[44:47], s99 offset:60\n"
    "ds_read_b64_tr_b16 a[8:9], v243 offset:40960\n"
    "v_mfma_f32_16x16x32_bf16 v[8:11], a[16:19], a[204:207], v[8:11]\n"
    "ds_read_b64_tr_b16 a[10:11], v243 offset:43008\n"
    "buffer_load_dwordx4 v231, s[40:43], s71 offen offset:1024 lds\n"
    "v_mfma_f32_16x16x32_bf16 v[12:15], a[20:23], a[200:203], v[12:15]\n"
    "ds_read_b64_tr_b16 a[12:13], v244 offset:40960\n"
    "v_mfma_f32_16x16x32_bf16 v[16:19], a[20:23], a[204:207], v[16:19]\n"
    "s_waitcnt lgkmcnt(7)\n"
    "ds_read_b64_tr_b16 a[14:15], v244 offset:43008\n"
    "v_mfma_f32_16x16x32_bf16 v[20:23], a[24:27], a[200:203], v[20:23]\n"
    "ds_read_b64_tr_b16 a[16:17], v241 offset:45056\n"
    "v_mfma_f32_16x16x32_bf16 v[24:27], a[24:27], a[204:207], v[24:27]\n"
    "ds_read_b64_tr_b16 a[18:19], v241 offset:47104\n"
    "v_mfma_f32_16x16x32_bf16 v[28:31], a[28:31], a[200:203], v[28:31]\n"
    "s_add_u32 m0, s87, 0\n"
    "ds_read_b64_tr_b16 a[20:21], v242 offset:45056\n"
    "v_mfma_f32_16x16x32_bf16 v[32:35], a[28:31], a[204:207], v[32:35]\n"
    "s_waitcnt vmcnt(14) lgkmcnt(7)\n"
    "ds_read_b64_tr_b16 a[22:23], v242 offset:47104\n"
    "buffer_load_dwordx4 v230, s[40:43], s73 offen offset:0 lds\n"
    "v_mfma_f32_16x16x32_bf16 v[4:7], a[0:3], a[208:211], v[4:7]\n"
    "ds_read_b64_tr_b16 a[24:25], v243 offset:45056\n"
    "v_mfma_f32_16x16x32_bf16 v[8:11], a[0:3], a[212:215], v[8:11]\n"
    "ds_read_b64_tr_b16 a[26:27], v243 offset:47104\n"
    "v_mfma_f32_16x16x32_bf16 v[12:15], a[4:7], a[208:211], v[12:15]\n"
    "ds_read_b64_tr_b16 a[28:29], v244 offset:45056\n"
    "v_mfma_f32_16x16x32_bf16 v[16:19], a[4:7], a[212:215], v[16:19]\n"
    "s_waitcnt lgkmcnt(7)\n"
    "ds_read_b64_tr_b16 a[30:31], v244 offset:47104\n"
    "buffer_load_dwordx4 v231, s[40:43], s73 offen offset:1024 lds\n"
    "v_mfma_f32_16x16x32_bf16 v[20:23], a[8:11], a[208:211], v[20:23]\n"
    "s_barrier\n"
    "ds_read_b64_tr_b16 a[0:1], v241 offset:49152\n"
    "v_mfma_f32_16x16x32_bf16 v[24:27], a[8:11], a[212:215], v[24:27]\n"
    "ds_read_b64_tr_b16 a[2:3], v241 offset:51200\n"
    "v_mfma_f32_16x16x32_bf16 v[28:31], a[12:15], a[208:211], v[28:31]\n"
    "s_add_u32 m0, s87, 73728\n"
    "ds_read_b64_tr_b16 a[4:5], v242 offset:49152\n"
    "v_mfma_f32_16x16x32_bf16 v[32:35], a[12:15], a[212:215], v[32:35]\n"
    "s_waitcnt lgkmcnt(7)\n"
    "ds_read_b64_tr_b16 a[6:7], v242 offset:51200\n"
    "buffer_load_dwordx4 v230, s[40:43], s75 offen offset:0 lds\n"
    "v_mfma_f32_16x16x32_bf16 v[4:7], a[16:19], a[216:219], v[4:7]\n"
    "s_add_u32 s99, s98, s74\n"
    "ds_read_b64_tr_b16 a[8:9], v243 offset:49152\n"
    "v_mfma_f32_16x16x32_bf16 v[8:11], a[16:19], a[220:223], v[8:11]\n"
    "s_and_b32 s99, s99, 0x1fffff\n"
    "ds_read_b64_tr_b16 a[10:11], v243 offset:51200\n"
    "v_mfma_f32_16x16x32_bf16 v[12:15], a[20:23], a[216:219], v[12:15]\n"
    "s_buffer_load_dwordx2 s[30:31], s[44:47], s99 offset:60\n"
    "ds_read_b64_tr_b16 a[12:13], v244 offset:49152\n"
    "v_mfma_f32_16x16x32_bf16 v[16:19], a[20:23], a[220:223], v[16:19]\n"
    "s_waitcnt lgkmcnt(7)\n"
    "ds_read_b64_tr_b16 a[14:15], v244 offset:51200\n"
    "buffer_load_dwordx4 v231, s[40:43], s75 offen offset:1024 lds\n"
    "v_mfma_f32_16x16x32_bf16 v[20:23], a[24:27], a[216:219], v[20:23]\n"
    "ds_read_b64_tr_b16 a[16:17], v241 offset:53248\n"
    "v_mfma_f32_16x16x32_bf16 v[24:27], a[24:27], a[220:223], v[24:27]\n"
    "ds_read_b64_tr_b16 a[18:19], v241 offset:55296\n"
    "v_mfma_f32_16x16x32_bf16 v[28:31], a[28:31], a[216:219], v[28:31]\n"
    "ds_read_b64_tr_b16 a[20:21], v242 offset:53248\n"
    "v_mfma_f32_16x16x32_bf16 v[32:35], a[28:31], a[220:223], v[32:35]\n"
    "s_waitcnt vmcnt(12) lgkmcnt(7)\n"
    "ds_read_b64_tr_b16 a[22:23], v242 offset:55296\n"
    "v_mfma_f32_16x16x32_bf16 v[4:7], a[0:3], a[224:227], v[4:7]\n"
    "ds_read_b64_tr_b16 a[24:25], v243 offset:53248\n"
    "v_mfma_f32_16x16x32_bf16 v[8:11], a[0:3], a[228:231], v[8:11]\n"
    "s_add_u32 m0, s87, 8192\n"
    "ds_read_b64_tr_b16 a[26:27], v243 offset:55296\n"
    "buffer_load_dwordx4 v230, s[40:43], s77 offen offset:0 lds\n"
    "v_mfma_f32_16x16x32_bf16 v[12:15], a[4:7], a[224:227], v[12:15]\n"
    "ds_read_b64_tr_b16 a[28:29], v244 offset:53248\n"
    "v_mfma_f32_16x16x32_bf16 v[16:19], a[4:7], a[228:231], v[16:19]\n"
    "s_waitcnt lgkmcnt(7)\n"
    "ds_read_b64_tr_b16 a[30:31], v244 offset:55296\n"
    "v_mfma_f32_16x16x32_bf16 v[20:23], a[8:11], a[224:227], v[20:23]\n"
    "ds_read_b64_tr_b16 a[0:1], v241 offset:57344\n"
    "v_mfma_f32_16x16x32_bf16 v[24:27], a[8:11], a[228:231], v[24:27]\n"
    "ds_read_b64_tr_b16 a[2:3], v241 offset:59392\n"
    "buffer_load_dwordx4 v231, s[40:43], s77 offen offset:1024 lds\n"
    "v_mfma_f32_16x16x32_bf16 v[28:31], a[12:15], a[224:227], v[28:31]\n"
    "ds_read_b64_tr_b16 a[4:5], v242 offset:57344\n"
    "v_mfma_f32_16x16x32_bf16 v[32:35], a[12:15], a[228:231], v[32:35]\n"
    "s_waitcnt lgkmcnt(7)\n"
    "ds_read_b64_tr_b16 a[6:7], v242 offset:59392\n"
    "v_mfma_f32_16x16x32_bf16 v[4:7], a[16:19], a[232:235], v[4:7]\n"
    "ds_read_b64_tr_b16 a[8:9], v243 offset:57344\n"
    "v_mfma_f32_16x16x32_bf16 v[8:11], a[16:19], a[236:239], v[8:11]\n"
    "s_add_u32 m0, s87, 81920\n"
    "ds_read_b64_tr_b16 a[10:11], v243 offset:59392\n"
    "buffer_load_dwordx4 v230, s[40:43], s79 offen offset:0 lds\n"
    "v_mfma_f32_16x16x32_bf16 v[12:15], a[20:23], a[232:235], v[12:15]\n"
    "s_add_u32 s99, s98, s76\n"
    "ds_read_b64_tr_b16 a[12:13], v244 offset:57344\n"
    "v_mfma_f32_16x16x32_bf16 v[16:19], a[20:23], a[236:239], v[16:19]\n"
    "s_waitcnt lgkmcnt(7)\n"
    "ds_read_b64_tr_b16 a[14:15], v244 offset:59392\n"
    "v_mfma_f32_16x16x32_bf16 v[20:23], a[24:27], a[232:235], v[20:23]\n"
    "s_and_b32 s99, s99, 0x1fffff\n"
    "ds_read_b64_tr_b16 a[16:17], v241 offset:61440\n"
    "v_mfma_f32_16x16x32_bf16 v[24:27], a[24:27], a[236:239], v[24:27]\n"
    "s_buffer_load_dwordx2 s[30:31], s[44:47], s99 offset:60\n"
    "ds_read_b64_tr_b16 a[18:19], v241 offset:63488\n"
    "buffer_load_dwordx4 v231, s[40:43], s79 offen offset:1024 lds\n"
    "v_mfma_f32_16x16x32_bf16 v[28:31], a[28:31], a[232:235], v[28:31]\n"
    "ds_read_b64_tr_b16 a[20:21], v242 offset:61440\n"
    "v_mfma_f32_16x16x32_bf16 v[32:35], a[28:31], a[236:239], v[32:35]\n"
    "s_waitcnt vmcnt(12) lgkmcnt(7)\n"
    "ds_read_b64_tr_b16 a[22:23], v242 offset:63488\n"
    "v_mfma_f32_16x16x32_bf16 v[4:7], a[0:3], a[240:243], v[4:7]\n"
    "ds_read_b64_tr_b16 a[24:25], v243 offset:61440\n"
    "v_mfma_f32_16x16x32_bf16 v[8:11], a[0:3], a[244:247], v[8:11]\n"
    "ds_read_b64_tr_b16 a[26:27], v243 offset:63488\n"
    "v_mfma_f32_16x16x32_bf16 v[12:15], a[4:7], a[240:243], v[12:15]\n"
    "s_add_u32 m0, s87, 16384\n"
    "ds_read_b64_tr_b16 a[28:29], v244 offset:61440\n"
    "v_mfma_f32_16x16x32_bf16 v[16:19], a[4:7], a[244:247], v[16:19]\n"
    "s_waitcnt lgkmcnt(7)\n"
    "ds_read_b64_tr_b16 a[30:31], v244 offset:63488\n"
    "buffer_load_dwordx4 v230, s[40:43], s81 offen offset:0 lds\n"
    "v_mfma_f32_16x16x32_bf16 v[20:23], a[8:11], a[240:243], v[20:23]\n"
    "s_barrier\n"
    "ds_read_b64_tr_b16 a[0:1], v245 offset:0\n"
    "v_mfma_f32_16x16x32_bf16 v[24:27], a[8:11], a[244:247], v[24:27]\n"
    "ds_read_b64_tr_b16 a[2:3], v245 offset:2048\n"
    "v_mfma_f32_16x16x32_bf16 v[28:31], a[12:15], a[240:243], v[28:31]\n"
    "ds_read_b64_tr_b16 a[4:5], v246 offset:0\n"
    "v_mfma_f32_16x16x32_bf16 v[32:35], a[12:15], a[244:247], v[32:35]\n"
    "s_waitcnt lgkmcnt(7)\n"
    "ds_read_b64_tr_b16 a[6:7], v246 offset:2048\n"
    "buffer_load_dwordx4 v231, s[40:43], s81 offen offset:1024 lds\n"
    "v_mfma_f32_16x16x32_bf16 v[4:7], a[16:19], a[248:251], v[4:7]\n"
    "ds_read_b64_tr_b16 a[8:9], v247 offset:0\n"
    "v_mfma_f32_16x16x32_bf16 v[8:11], a[16:19], a[252:255], v[8:11]\n"
    "ds_read_b64_tr_b16 a[10:11], v247 offset:2048\n"
    "v_mfma_f32_16x16x32_bf16 v[12:15], a[20:23], a[248:251], v[12:15]\n"
    "s_add_u32 m0, s87, 24576\n"
    "ds_read_b64_tr_b16 a[12:13], v248 offset:0\n"
    "v_mfma_f32_16x16x32_bf16 v[16:19], a[20:23], a[252:255], v[16:19]\n"
    "s_waitcnt lgkmcnt(7)\n"
    "ds_read_b64_tr_b16 a[14:15], v248 offset:2048\n"
    "buffer_load_dwordx4 v230, s[40:43], s83 offen offset:0 lds\n"
    "v_mfma_f32_16x16x32_bf16 v[20:23], a[24:27], a[248:251], v[20:23]\n"
    "s_add_u32 s99, s98, s78\n"
    "ds_read_b64_tr_b16 a[16:17], v245 offset:4096\n"
    "v_fma_f32 v132, v68, v4, v132\n"
    "v_fma_f32 v133, v69, v5, v133\n"
    "v_fma_f32 v134, v70, v6, v134\n"
    "v_mfma_f32_16x16x32_bf16 v[24:27], a[24:27], a[252:255], v[24:27]\n"
    "s_and_b32 s99, s99, 0x1fffff\n"
    "ds_read_b64_tr_b16 a[18:19], v245 offset:6144\n"
    "v_mfma_f32_16x16x32_bf16 v[28:31], a[28:31], a[248:251], v[28:31]\n"
    "s_buffer_load_dwordx2 s[30:31], s[44:47], s99 offset:60\n"
    "ds_read_b64_tr_b16 a[20:21], v246 offset:4096\n"
    "v_fma_f32 v135, v71, v7, v135\n"
    "v_cvt_pk_bf16_f32 v196, v132, v133\n"
    "v_cvt_pk_bf16_f32 v197, v134, v135\n"
    "v_mfma_f32_16x16x32_bf16 v[32:35], a[28:31], a[252:255], v[32:35]\n"
    "s_waitcnt lgkmcnt(7)\n"
    "ds_read_b64_tr_b16 a[22:23], v246 offset:6144\n"
    "v_fma_f32 v140, v76, v12, v140\n"
    "v_fma_f32 v141, v77, v13, v141\n"
    "v_fma_f32 v142, v78, v14, v142\n"
    "v_mfma_f32_16x16x32_bf16 v[36:39], a[0:3], a[96:99], v[36:39]\n"
    "ds_read_b64_tr_b16 a[24:25], v247 offset:4096\n"
    "v_fma_f32 v143, v79, v15, v143\n"
    "v_cvt_pk_bf16_f32 v198, v140, v141\n"
    "v_cvt_pk_bf16_f32 v199, v142, v143\n"
    "v_mfma_f32_16x16x32_bf16 v[40:43], a[0:3], a[100:103], v[40:43]\n"
    "ds_read_b64_tr_b16 a[26:27], v247 offset:6144\n"
    "buffer_load_dwordx4 v231, s[40:43], s83 offen offset:1024 lds\n"
    "v_mfma_f32_16x16x32_bf16 v[44:47], a[4:7], a[96:99], v[44:47]\n"
    "ds_read_b64_tr_b16 a[28:29], v248 offset:4096\n"
    "buffer_store_dwordx4 v[196:199], v232, s[56:59], s68 offen offset:0 sc1\n"
    "v_fma_f32 v136, v72, v8, v136\n"
    "v_fma_f32 v137, v73, v9, v137\n"
    "v_mfma_f32_16x16x32_bf16 v[48:51], a[4:7], a[100:103], v[48:51]\n"
    "s_waitcnt lgkmcnt(7)\n"
    "ds_read_b64_tr_b16 a[30:31], v248 offset:6144\n"
    "v_fma_f32 v138, v74, v10, v138\n"
    "v_fma_f32 v139, v75, v11, v139\n"
    "ds_read_b128 v[4:7], v2 offset:1024\n"
    "v_mfma_f32_16x16x32_bf16 v[52:55], a[8:11], a[96:99], v[52:55]\n"
    "ds_read_b64_tr_b16 a[0:1], v245 offset:8192\n"
    "v_cvt_pk_bf16_f32 v200, v136, v137\n"
    "v_cvt_pk_bf16_f32 v201, v138, v139\n"
    "v_fma_f32 v144, v80, v16, v144\n"
    "v_mfma_f32_16x16x32_bf16 v[56:59], a[8:11], a[100:103], v[56:59]\n"
    "s_add_u32 m0, s87, 32768\n"
    "ds_read_b64_tr_b16 a[2:3], v245 offset:10240\n"
    "buffer_load_dwordx4 v228, s[44:47], s68 offen offset:0 lds\n"
    "v_mfma_f32_16x16x32_bf16 v[60:63], a[12:15], a[96:99], v[60:63]\n"
    "ds_read_b64_tr_b16 a[4:5], v246 offset:8192\n"
    "v_fma_f32 v145, v81, v17, v145\n"
    "v_fma_f32 v146, v82, v18, v146\n"
    "v_fma_f32 v147, v83, v19, v147\n"
    "v_mfma_f32_16x16x32_bf16 v[64:67], a[12:15], a[100:103], v[64:67]\n"
    "s_waitcnt lgkmcnt(8)\n"
    "ds_read_b64_tr_b16 a[6:7], v246 offset:10240\n"
    "ds_read_b128 v[12:15], v2 offset:1088\n"
    "v_cvt_pk_bf16_f32 v202, v144, v145\n"
    "v_cvt_pk_bf16_f32 v203, v146, v147\n"
    "v_mfma_f32_16x16x32_bf16 v[36:39], a[16:19], a[104:107], v[36:39]\n"
    "ds_read_b64_tr_b16 a[8:9], v247 offset:8192\n"
    "buffer_store_dwordx4 v[200:203], v232, s[56:59], s68 offen offset:1024 sc1\n"
    "v_fma_f32 v148, v84, v20, v148\n"
    "v_fma_f32 v149, v85, v21, v149\n"
    "v_mfma_f32_16x16x32_bf16 v[40:43], a[16:19], a[108:111], v[40:43]\n"
    "ds_read_b64_tr_b16 a[10:11], v247 offset:10240\n"
    "buffer_load_dwordx4 v229, s[44:47], s68 offen offset:1024 lds\n"
    "v_mfma_f32_16x16x32_bf16 v[44:47], a[20:23], a[104:107], v[44:47]\n"
    "ds_read_b64_tr_b16 a[12:13], v248 offset:8192\n"
    "v_fma_f32 v150, v86, v22, v150\n"
    "v_fma_f32 v151, v87, v23, v151\n"
    "v_cvt_pk_bf16_f32 v204, v148, v149\n"
    "v_mfma_f32_16x16x32_bf16 v[48:51], a[20:23], a[108:111], v[48:51]\n"
    "s_waitcnt lgkmcnt(9)\n"
    "ds_read_b64_tr_b16 a[14:15], v248 offset:10240\n"
    "v_cvt_pk_bf16_f32 v205, v150, v151\n"
    "v_fma_f32 v156, v92, v28, v156\n"
    "v_fma_f32 v157, v93, v29, v157\n"
    "v_mfma_f32_16x16x32_bf16 v[52:55], a[24:27], a[104:107], v[52:55]\n"
    "ds_read_b64_tr_b16 a[16:17], v245 offset:12288\n"
    "v_fma_f32 v158, v94, v30, v158\n"
    "v_fma_f32 v159, v95, v31, v159\n"
    "v_cvt_pk_bf16_f32 v206, v156, v157\n"
    "v_mfma_f32_16x16x32_bf16 v[56:59], a[24:27], a[108:111], v[56:59]\n"
    "s_add_u32 m0, s87, 40960\n"
    "ds_read_b64_tr_b16 a[18:19], v245 offset:14336\n"
    "buffer_load_dwordx4 v230, s[44:47], s68 offen offset:0 lds\n"
    "v_mfma_f32_16x16x32_bf16 v[60:63], a[28:31], a[104:107], v[60:63]\n"
    "s_add_u32 s99, s98, s80\n"
    "ds_read_b64_tr_b16 a[20:21], v246 offset:12288\n"
    "v_cvt_pk_bf16_f32 v207, v158, v159\n"
    "buffer_store_dwordx4 v[204:207], v232, s[56:59], s68 offen offset:2048 sc1\n"
    "v_fma_f32 v152, v88, v24, v152\n"
    "v_mfma_f32_16x16x32_bf16 v[64:67], a[28:31], a[108:111], v[64:67]\n"
    "s_waitcnt lgkmcnt(8)\n"
    "ds_read_b64_tr_b16 a[22:23], v246 offset:14336\n"
    "v_fma_f32 v153, v89, v25, v153\n"
    "v_fma_f32 v154, v90, v26, v154\n"
    "v_fma_f32 v155, v91, v27, v155\n"
    "v_mfma_f32_16x16x32_bf16 v[36:39], a[0:3], a[112:115], v[36:39]\n"
    "s_and_b32 s99, s99, 0x1fffff\n"
    "ds_read_b64_tr_b16 a[24:25], v247 offset:12288\n"
    "ds_read_b128 v[20:23], v2 offset:1152\n"
    "v_cvt_pk_bf16_f32 v208, v152, v153\n"
    "v_cvt_pk_bf16_f32 v209, v154, v155\n"
    "v_mfma_f32_16x16x32_bf16 v[40:43], a[0:3], a[116:119], v[40:43]\n"
    "s_buffer_load_dwordx2 s[30:31], s[44:47], s99 offset:60\n"
    "ds_read_b64_tr_b16 a[26:27], v247 offset:14336\n"
    "v_mfma_f32_16x16x32_bf16 v[44:47], a[4:7], a[112:115], v[44:47]\n"
    "ds_read_b64_tr_b16 a[28:29], v248 offset:12288\n"
    "v_fma_f32 v160, v96, v32, v160\n"
    "v_fma_f32 v161, v97, v33, v161\n"
    "v_fma_f32 v162, v98, v34, v162\n"
    "v_mfma_f32_16x16x32_bf16 v[48:51], a[4:7], a[116:119], v[48:51]\n"
    "s_waitcnt vmcnt(18) lgkmcnt(8)\n"
    "ds_read_b64_tr_b16 a[30:31], v248 offset:14336\n"
    "v_fma_f32 v163, v99, v35, v163\n"
    "ds_read_b128 v[28:31], v2 offset:1216\n"
    "v_cvt_pk_bf16_f32 v210, v160, v161\n"
    "v_mfma_f32_16x16x32_bf16 v[52:55], a[8:11], a[112:115], v[52:55]\n"
    "s_barrier\n"
    "ds_read_b64_tr_b16 a[0:1], v241 offset:0\n"
    "v_cvt_pk_bf16_f32 v211, v162, v163\n"
    "buffer_store_dwordx4 v[208:211], v232, s[56:59], s68 offen offset:3072 sc1\n"
    "v_mfma_f32_16x16x32_bf16 v[56:59], a[8:11], a[116:119], v[56:59]\n"
    "ds_read_b64_tr_b16 a[2:3], v241 offset:2048\n"
    "buffer_load_dwordx4 v231, s[44:47], s68 offen offset:1024 lds\n"
    "v_mfma_f32_16x16x32_bf16 v[60:63], a[12:15], a[112:115], v[60:63]\n"
    "ds_read_b64_tr_b16 a[4:5], v242 offset:0\n"
    "v_mfma_f32_16x16x32_bf16 v[64:67], a[12:15], a[116:119], v[64:67]\n"
    "s_waitcnt lgkmcnt(9)\n"
    "ds_read_b64_tr_b16 a[6:7], v242 offset:2048\n"
    "v_mfma_f32_16x16x32_bf16 v[36:39], a[16:19], a[120:123], v[36:39]\n"
    "ds_read_b64_tr_b16 a[8:9], v243 offset:0\n"
    "v_mfma_f32_16x16x32_bf16 v[40:43], a[16:19], a[124:127], v[40:43]\n"
    "s_add_u32 m0, s87, 49152\n"
    "ds_read_b64_tr_b16 a[10:11], v243 offset:2048\n"
    "buffer_load_dwordx4 v228, s[44:47], s69 offen offset:0 lds\n"
    "v_mfma_f32_16x16x32_bf16 v[44:47], a[20:23], a[120:123], v[44:47]\n"
    "ds_read_b64_tr_b16 a[12:13], v244 offset:0\n"
    "v_mfma_f32_16x16x32_bf16 v[48:51], a[20:23], a[124:127], v[48:51]\n"
    "s_waitcnt lgkmcnt(8)\n"
    "ds_read_b64_tr_b16 a[14:15], v244 offset:2048\n"
    "v_mfma_f32_16x16x32_bf16 v[52:55], a[24:27], a[120:123], v[52:55]\n"
    "ds_read_b64_tr_b16 a[16:17], v241 offset:4096\n"
    "v_mfma_f32_16x16x32_bf16 v[56:59], a[24:27], a[124:127], v[56:59]\n"
    "ds_read_b64_tr_b16 a[18:19], v241 offset:6144\n"
    "buffer_load_dwordx4 v229, s[44:47], s69 offen offset:1024 lds\n"
    "v_mfma_f32_16x16x32_bf16 v[60:63], a[28:31], a[120:123], v[60:63]\n"
    "ds_read_b64_tr_b16 a[20:21], v242 offset:4096\n"
    "v_mfma_f32_16x16x32_bf16 v[64:67], a[28:31], a[124:127], v[64:67]\n"
    "s_waitcnt lgkmcnt(7)\n"
    "ds_read_b64_tr_b16 a[22:23], v242 offset:6144\n"
    "v_mfma_f32_16x16x32_bf16 v[36:39], a[0:3], a[128:131], v[36:39]\n"
    "s_add_u32 s99, s98, s82\n"
    "ds_read_b64_tr_b16 a[24:25], v243 offset:4096\n"
    "v_mfma_f32_16x16x32_bf16 v[40:43], a[0:3], a[132:135], v[40:43]\n"
    "s_add_u32 m0, s87, 57344\n"
    "ds_read_b64_tr_b16 a[26:27], v243 offset:6144\n"
    "buffer_load_dwordx4 v230, s[44:47], s69 offen offset:0 lds\n"
    "v_mfma_f32_16x16x32_bf16 v[44:47], a[4:7], a[128:131], v[44:47]\n"
    "s_and_b32 s99, s99, 0x1fffff\n"
    "ds_read_b64_tr_b16 a[28:29], v244 offset:4096\n"
    "v_mfma_f32_16x16x32_bf16 v[48:51], a[4:7], a[132:135], v[48:51]\n"
    "s_waitcnt lgkmcnt(7)\n"
    "ds_read_b64_tr_b16 a[30:31], v244 offset:6144\n"
    "v_mfma_f32_16x16x32_bf16 v[52:55], a[8:11], a[128:131], v[52:55]\n"
    "s_buffer_load_dwordx2 s[30:31], s[44:47], s99 offset:60\n"
    "ds_read_b64_tr_b16 a[0:1], v245 offset:16384\n"
    "v_mfma_f32_16x16x32_bf16 v[56:59], a[8:11], a[132:135], v[56:59]\n"
    "ds_read_b64_tr_b16 a[2:3], v245 offset:18432\n"
    "v_mfma_f32_16x16x32_bf16 v[60:63], a[12:15], a[128:131], v[60:63]\n"
    "ds_read_b64_tr_b16 a[4:5], v246 offset:16384\n"
    "v_mfma_f32_16x16x32_bf16 v[64:67], a[12:15], a[132:135], v[64:67]\n"
    "s_waitcnt lgkmcnt(7)\n"
    "ds_read_b64_tr_b16 a[6:7], v246 offset:18432\n"
    "buffer_load_dwordx4 v231, s[44:47], s69 offen offset:1024 lds\n"
    "v_mfma_f32_16x16x32_bf16 v[36:39], a[16:19], a[136:139], v[36:39]\n"
    "ds_read_b64_tr_b16 a[8:9], v247 offset:16384\n"
    "v_mfma_f32_16x16x32_bf16 v[40:43], a[16:19], a[140:143], v[40:43]\n"
    "ds_read_b64_tr_b16 a[10:11], v247 offset:18432\n"
    "v_mfma_f32_16x16x32_bf16 v[44:47], a[20:23], a[136:139], v[44:47]\n"
    "s_add_u32 m0, s87, 90112\n"
    "ds_read_b64_tr_b16 a[12:13], v248 offset:16384\n"
    "v_mfma_f32_16x16x32_bf16 v[48:51], a[20:23], a[140:143], v[48:51]\n"
    "s_waitcnt lgkmcnt(7)\n"
    "ds_read_b64_tr_b16 a[14:15], v248 offset:18432\n"
    "buffer_load_dwordx4 v228, s[44:47], s70 offen offset:0 lds\n"
    "v_mfma_f32_16x16x32_bf16 v[52:55], a[24:27], a[136:139], v[52:55]\n"
    "ds_read_b64_tr_b16 a[16:17], v245 offset:20480\n"
    "v_mfma_f32_16x16x32_bf16 v[56:59], a[24:27], a[140:143], v[56:59]\n"
    "ds_read_b64_tr_b16 a[18:19], v245 offset:22528\n"
    "v_mfma_f32_16x16x32_bf16 v[60:63], a[28:31], a[136:139], v[60:63]\n"
    "ds_read_b64_tr_b16 a[20:21], v246 offset:20480\n"
    "v_mfma_f32_16x16x32_bf16 v[64:67], a[28:31], a[140:143], v[64:67]\n"
    "s_waitcnt lgkmcnt(7)\n"
    "ds_read_b64_tr_b16 a[22:23], v246 offset:22528\n"
    "buffer_load_dwordx4 v229, s[44:47], s70 offen offset:1024 lds\n"
    "v_mfma_f32_16x16x32_bf16 v[36:39], a[0:3], a[144:147], v[36:39]\n"
    "ds_read_b64_tr_b16 a[24:25], v247 offset:20480\n"
    "v_mfma_f32_16x16x32_bf16 v[40:43], a[0:3], a[148:151], v[40:43]\n"
    "ds_read_b64_tr_b16 a[26:27], v247 offset:22528\n"
    "v_mfma_f32_16x16x32_bf16 v[44:47], a[4:7], a[144:147], v[44:47]\n"
    "s_add_u32 m0, s87, 98304\n"
    "ds_read_b64_tr_b16 a[28:29], v248 offset:20480\n"
    "v_mfma_f32_16x16x32_bf16 v[48:51], a[4:7], a[148:151], v[48:51]\n"
    "s_waitcnt vmcnt(22) lgkmcnt(7)\n"
    "ds_read_b64_tr_b16 a[30:31], v248 offset:22528\n"
    "buffer_load_dwordx4 v230, s[44:47], s70 offen offset:0 lds\n"
    "v_mfma_f32_16x16x32_bf16 v[52:55], a[8:11], a[144:147], v[52:55]\n"
    "s_barrier\n"
    "ds_read_b64_tr_b16 a[0:1], v237 offset:0\n"
    "v_mfma_f32_16x16x32_bf16 v[56:59], a[8:11], a[148:151], v[56:59]\n"
    "ds_read_b64_tr_b16 a[2:3], v237 offset:2048\n"
    "v_mfma_f32_16x16x32_bf16 v[60:63], a[12:15], a[144:147], v[60:63]\n"
    "ds_read_b64_tr_b16 a[4:5], v238 offset:0\n"
    "v_mfma_f32_16x16x32_bf16 v[64:67], a[12:15], a[148:151], v[64:67]\n"
    "s_waitcnt lgkmcnt(7)\n"
    "ds_read_b64_tr_b16 a[6:7], v238 offset:2048\n"
    "v_mfma_f32_16x16x32_bf16 v[36:39], a[16:19], a[152:155], v[36:39]\n"
    "ds_read_b64_tr_b16 a[8:9], v239 offset:0\n"
    "v_mfma_f32_16x16x32_bf16 v[40:43], a[16:19], a[156:159], v[40:43]\n"
    "ds_read_b64_tr_b16 a[10:11], v239 offset:2048\n"
    "buffer_load_dwordx4 v231, s[44:47], s70 offen offset:1024 lds\n"
    "v_mfma_f32_16x16x32_bf16 v[44:47], a[20:23], a[152:155], v[44:47]\n"
    "ds_read_b64_tr_b16 a[12:13], v240 offset:0\n"
    "v_mfma_f32_16x16x32_bf16 v[48:51], a[20:23], a[156:159], v[48:51]\n"
    "s_waitcnt lgkmcnt(7)\n"
    "ds_read_b64_tr_b16 a[14:15], v240 offset:2048\n"
    "v_mfma_f32_16x16x32_bf16 v[52:55], a[24:27], a[152:155], v[52:55]\n"
    "ds_read_b64_tr_b16 a[16:17], v237 offset:4096\n"
    "v_mfma_f32_16x16x32_bf16 v[56:59], a[24:27], a[156:159], v[56:59]\n"
    "s_add_u32 m0, s87, 106496\n"
    "ds_read_b64_tr_b16 a[18:19], v237 offset:6144\n"
    "buffer_load_dwordx4 v228, s[44:47], s72 offen offset:0 lds\n"
    "v_mfma_f32_16x16x32_bf16 v[60:63], a[28:31], a[152:155], v[60:63]\n"
    "ds_read_b64_tr_b16 a[20:21], v238 offset:4096\n"
    "v_mfma_f32_16x16x32_bf16 v[64:67], a[28:31], a[156:159], v[64:67]\n"
    "s_waitcnt lgkmcnt(7)\n"
    "ds_read_b64_tr_b16 a[22:23], v238 offset:6144\n"
    "v_mfma_f32_16x16x32_bf16 v[36:39], a[0:3], a[160:163], v[36:39]\n"
    "ds_read_b64_tr_b16 a[24:25], v239 offset:4096\n"
    "v_mfma_f32_16x16x32_bf16 v[40:43], a[0:3], a[164:167], v[40:43]\n"
    "ds_read_b64_tr_b16 a[26:27], v239 offset:6144\n"
    "buffer_load_dwordx4 v229, s[44:47], s72 offen offset:1024 lds\n"
    "v_mfma_f32_16x16x32_bf16 v[44:47], a[4:7], a[160:163], v[44:47]\n"
    "ds_read_b64_tr_b16 a[28:29], v240 offset:4096\n"
    "v_mfma_f32_16x16x32_bf16 v[48:51], a[4:7], a[164:167], v[48:51]\n"
    "s_waitcnt lgkmcnt(7)\n"
    "ds_read_b64_tr_b16 a[30:31], v240 offset:6144\n"
    "v_mfma_f32_16x16x32_bf16 v[52:55], a[8:11], a[160:163], v[52:55]\n"
    "ds_read_b64_tr_b16 a[0:1], v241 offset:8192\n"
    "v_mfma_f32_16x16x32_bf16 v[56:59], a[8:11], a[164:167], v[56:59]\n"
    "s_add_u32 m0, s87, 114688\n"
    "ds_read_b64_tr_b16 a[2:3], v241 offset:10240\n"
    "buffer_load_dwordx4 v230, s[44:47], s72 offen offset:0 lds\n"
    "v_mfma_f32_16x16x32_bf16 v[60:63], a[12:15], a[160:163], v[60:63]\n"
    "ds_read_b64_tr_b16 a[4:5], v242 offset:8192\n"
    "v_mfma_f32_16x16x32_bf16 v[64:67], a[12:15], a[164:167], v[64:67]\n"
    "s_waitcnt lgkmcnt(7)\n"
    "ds_read_b64_tr_b16 a[6:7], v242 offset:10240\n"
    "v_mfma_f32_16x16x32_bf16 v[36:39], a[16:19], a[168:171], v[36:39]\n"
    "ds_read_b64_tr_b16 a[8:9], v243 offset:8192\n"
    "v_mfma_f32_16x16x32_bf16 v[40:43], a[16:19], a[172:175], v[40:43]\n"
    "ds_read_b64_tr_b16 a[10:11], v243 offset:10240\n"
    "v_mfma_f32_16x16x32_bf16 v[44:47], a[20:23], a[168:171], v[44:47]\n"
    "ds_read_b64_tr_b16 a[12:13], v244 offset:8192\n"
    "v_mfma_f32_16x16x32_bf16 v[48:51], a[20:23], a[172:175], v[48:51]\n"
    "s_waitcnt vmcnt(12) lgkmcnt(7)\n"
    "ds_read_b64_tr_b16 a[14:15], v244 offset:10240\n"
    "buffer_store_byte v255, v253, s[64:67], s92 offen offset:16 sc1\n"
    "v_mfma_f32_16x16x32_bf16 v[52:55], a[24:27], a[168:171], v[52:55]\n"
    "ds_read_b64_tr_b16 a[16:17], v241 offset:12288\n"
    "v_mfma_f32_16x16x32_bf16 v[56:59], a[24:27], a[172:175], v[56:59]\n"
    "ds_read_b64_tr_b16 a[18:19], v241 offset:14336\n"
    "buffer_load_dwordx4 v231, s[44:47], s72 offen offset:1024 lds\n"
    "v_mfma_f32_16x16x32_bf16 v[60:63], a[28:31], a[168:171], v[60:63]\n"
    "ds_read_b64_tr_b16 a[20:21], v242 offset:12288\n"
    "v_mfma_f32_16x16x32_bf16 v[64:67], a[28:31], a[172:175], v[64:67]\n"
    "s_waitcnt lgkmcnt(7)\n"
    "ds_read_b64_tr_b16 a[22:23], v242 offset:14336\n"
    "v_mfma_f32_16x16x32_bf16 v[36:39], a[0:3], a[176:179], v[36:39]\n"
    "ds_read_b64_tr_b16 a[24:25], v243 offset:12288\n"
    "v_mfma_f32_16x16x32_bf16 v[40:43], a[0:3], a[180:183], v[40:43]\n"
    "s_add_u32 m0, s87, 122880\n"
    "ds_read_b64_tr_b16 a[26:27], v243 offset:14336\n"
    "buffer_load_dwordx4 v228, s[44:47], s74 offen offset:0 lds\n"
    "v_mfma_f32_16x16x32_bf16 v[44:47], a[4:7], a[176:179], v[44:47]\n"
    "ds_read_b64_tr_b16 a[28:29], v244 offset:12288\n"
    "v_mfma_f32_16x16x32_bf16 v[48:51], a[4:7], a[180:183], v[48:51]\n"
    "s_waitcnt lgkmcnt(7)\n"
    "ds_read_b64_tr_b16 a[30:31], v244 offset:14336\n"
    "v_mfma_f32_16x16x32_bf16 v[52:55], a[8:11], a[176:179], v[52:55]\n"
    "s_barrier\n"
    "ds_read_b64_tr_b16 a[0:1], v237 offset:8192\n"
    "v_mfma_f32_16x16x32_bf16 v[56:59], a[8:11], a[180:183], v[56:59]\n"
    "ds_read_b64_tr_b16 a[2:3], v237 offset:10240\n"
    "buffer_load_dwordx4 v229, s[44:47], s74 offen offset:1024 lds\n"
    "v_mfma_f32_16x16x32_bf16 v[60:63], a[12:15], a[176:179], v[60:63]\n"
    "ds_read_b64_tr_b16 a[4:5], v238 offset:8192\n"
    "v_mfma_f32_16x16x32_bf16 v[64:67], a[12:15], a[180:183], v[64:67]\n"
    "s_waitcnt lgkmcnt(7)\n"
    "ds_read_b64_tr_b16 a[6:7], v238 offset:10240\n"
    "v_mfma_f32_16x16x32_bf16 v[36:39], a[16:19], a[184:187], v[36:39]\n"
    "L_ip6_%=:\n"
    "s_cbranch_execz L_ipback_%=\n"
    "ds_read_b64_tr_b16 a[8:9], v239 offset:8192\n"
    "v_mfma_f32_16x16x32_bf16 v[40:43], a[16:19], a[188:191], v[40:43]\n"
    "s_add_u32 m0, s87, 131072\n"
    "ds_read_b64_tr_b16 a[10:11], v239 offset:10240\n"
    "buffer_load_dwordx4 v230, s[44:47], s74 offen offset:0 lds\n"
    "v_mfma_f32_16x16x32_bf16 v[44:47], a[20:23], a[184:187], v[44:47]\n"
    "ds_read_b64_tr_b16 a[12:13], v240 offset:8192\n"
    "v_mfma_f32_16x16x32_bf16 v[48:51], a[20:23], a[188:191], v[48:51]\n"
    "s_waitcnt lgkmcnt(7)\n"
    "ds_read_b64_tr_b16 a[14:15], v240 offset:10240\n"
    "v_mfma_f32_16x16x32_bf16 v[52:55], a[24:27], a[184:187], v[52:55]\n"
    "ds_read_b64_tr_b16 a[16:17], v237 offset:12288\n"
    "v_mfma_f32_16x16x32_bf16 v[56:59], a[24:27], a[188:191], v[56:59]\n"
    "ds_read_b64_tr_b16 a[18:19], v237 offset:14336\n"
    "v_mfma_f32_16x16x32_bf16 v[60:63], a[28:31], a[184:187], v[60:63]\n"
    "ds_read_b64_tr_b16 a[20:21], v238 offset:12288\n"
    "v_mfma_f32_16x16x32_bf16 v[64:67], a[28:31], a[188:191], v[64:67]\n"
    "s_waitcnt lgkmcnt(7)\n"
    "ds_read_b64_tr_b16 a[22:23], v238 offset:14336\n"
    "buffer_load_dwordx4 v231, s[44:47], s74 offen offset:1024 lds\n"
    "v_mfma_f32_16x16x32_bf16 v[36:39], a[0:3], a[192:195], v[36:39]\n"
    "ds_read_b64_tr_b16 a[24:25], v239 offset:12288\n"
    "v_mfma_f32_16x16x32_bf16 v[40:43], a[0:3], a[196:199], v[40:43]\n"
    "ds_read_b64_tr_b16 a[26:27], v239 offset:14336\n"
    "v_mfma_f32_16x16x32_bf16 v[44:47], a[4:7], a[192:195], v[44:47]\n"
    "s_add_u32 m0, s87, 65536\n"
    "ds_read_b64_tr_b16 a[28:29], v240 offset:12288\n"
    "v_mfma_f32_16x16x32_bf16 v[48:51], a[4:7], a[196:199], v[48:51]\n"
    "s_waitcnt lgkmcnt(7)\n"
    "ds_read_b64_tr_b16 a[30:31], v240 offset:14336\n"
    "buffer_load_dwordx4 v228, s[44:47], s76 offen offset:0 lds\n"
    "v_mfma_f32_16x16x32_bf16 v[52:55], a[8:11], a[192:195], v[52:55]\n"
    "ds_read_b64_tr_b16 a[0:1], v241 offset:16384\n"
    "v_mfma_f32_16x16x32_bf16 v[56:59], a[8:11], a[196:199], v[56:59]\n"
    "ds_read_b64_tr_b16 a[2:3], v241 offset:18432\n"
    "v_mfma_f32_16x16x32_bf16 v[60:63], a[12:15], a[192:195], v[60:63]\n"
    "ds_read_b64_tr_b16 a[4:5], v242 offset:16384\n"
    "v_mfma_f32_16x16x32_bf16 v[64:67], a[12:15], a[196:199], v[64:67]\n"
    "s_waitcnt lgkmcnt(7)\n"
    "ds_read_b64_tr_b16 a[6:7], v242 offset:18432\n"
    "buffer_load_dwordx4 v229, s[44:47], s76 offen offset:1024 lds\n"
    "v_mfma_f32_16x16x32_bf16 v[36:39], a[16:19], a[200:203], v[36:39]\n"
    "ds_read_b64_tr_b16 a[8:9], v243 offset:16384\n"
    "v_mfma_f32_16x16x32_bf16 v[40:43], a[16:19], a[204:207], v[40:43]\n"
    "ds_read_b64_tr_b16 a[10:11], v243 offset:18432\n"
    "v_mfma_f32_16x16x32_bf16 v[44:47], a[20:23], a[200:203], v[44:47]\n"
    "s_add_u32 m0, s87, 139264\n"
    "ds_read_b64_tr_b16 a[12:13], v244 offset:16384\n"
    "v_mfma_f32_16x16x32_bf16 v[48:51], a[20:23], a[204:207], v[48:51]\n"
    "s_waitcnt lgkmcnt(7)\n"
    "ds_read_b64_tr_b16 a[14:15], v244 offset:18432\n"
    "buffer_load_dwordx4 v230, s[44:47], s76 offen offset:0 lds\n"
    "v_mfma_f32_16x16x32_bf16 v[52:55], a[24:27], a[200:203], v[52:55]\n"
    "ds_read_b64_tr_b16 a[16:17], v241 offset:20480\n"
    "v_mfma_f32_16x16x32_bf16 v[56:59], a[24:27], a[204:207], v[56:59]\n"
    "ds_read_b64_tr_b16 a[18:19], v241 offset:22528\n"
    "v_mfma_f32_16x16x32_bf16 v[60:63], a[28:31], a[200:203], v[60:63]\n"
    "ds_read_b64_tr_b16 a[20:21], v242 offset:20480\n"
    "v_mfma_f32_16x16x32_bf16 v[64:67], a[28:31], a[204:207], v[64:67]\n"
    "s_waitcnt lgkmcnt(7)\n"
    "ds_read_b64_tr_b16 a[22:23], v242 offset:22528\n"
    "v_mfma_f32_16x16x32_bf16 v[36:39], a[0:3], a[208:211], v[36:39]\n"
    "ds_read_b64_tr_b16 a[24:25], v243 offset:20480\n"
    "v_mfma_f32_16x16x32_bf16 v[40:43], a[0:3], a[212:215], v[40:43]\n"
    "ds_read_b64_tr_b16 a[26:27], v243 offset:22528\n"
    "buffer_load_dwordx4 v231, s[44:47], s76 offen offset:1024 lds\n"
    "v_mfma_f32_16x16x32_bf16 v[44:47], a[4:7], a[208:211], v[44:47]\n"
    "ds_read_b64_tr_b16 a[28:29], v244 offset:20480\n"
    "v_mfma_f32_16x16x32_bf16 v[48:51], a[4:7], a[212:215], v[48:51]\n"
    "s_waitcnt lgkmcnt(7)\n"
    "ds_read_b64_tr_b16 a[30:31], v244 offset:22528\n"
    "v_mfma_f32_16x16x32_bf16 v[52:55], a[8:11], a[208:211], v[52:55]\n"
    "s_barrier\n"
    "ds_read_b64_tr_b16 a[0:1], v237 offset:16384\n"
    "v_mfma_f32_16x16x32_bf16 v[56:59], a[8:11], a[212:215], v[56:59]\n"
    "s_add_u32 m0, s87, 0\n"
    "ds_read_b64_tr_b16 a[2:3], v237 offset:18432\n"
    "buffer_load_dwordx4 v228, s[44:47], s78 offen offset:0 lds\n"
    "v_mfma_f32_16x16x32_bf16 v[60:63], a[12:15], a[208:211], v[60:63]\n"
    "ds_read_b64_tr_b16 a[4:5], v238 offset:16384\n"
    "v_mfma_f32_16x16x32_bf16 v[64:67], a[12:15], a[212:215], v[64:67]\n"
    "s_waitcnt lgkmcnt(7)\n"
    "ds_read_b64_tr_b16 a[6:7], v238 offset:18432\n"
    "v_mfma_f32_16x16x32_bf16 v[36:39], a[16:19], a[216:219], v[36:39]\n"
    "s_buffer_load_dword s16, s[64:67], s91 offset:16 glc\n"
    "ds_read_b64_tr_b16 a[8:9], v239 offset:16384\n"
    "v_mfma_f32_16x16x32_bf16 v[40:43], a[16:19], a[220:223], v[40:43]\n"
    "ds_read_b64_tr_b16 a[10:11], v239 offset:18432\n"
    "buffer_load_dwordx4 v229, s[44:47], s78 offen offset:1024 lds\n"
    "v_mfma_f32_16x16x32_bf16 v[44:47], a[20:23], a[216:219], v[44:47]\n"
    "ds_read_b64_tr_b16 a[12:13], v240 offset:16384\n"
    "v_mfma_f32_16x16x32_bf16 v[48:51], a[20:23], a[220:223], v[48:51]\n"
    "s_waitcnt lgkmcnt(7)\n"
    "ds_read_b64_tr_b16 a[14:15], v240 offset:18432\n"
    "v_mfma_f32_16x16x32_bf16 v[52:55], a[24:27], a[216:219], v[52:55]\n"
    "ds_read_b64_tr_b16 a[16:17], v237 offset:20480\n"
    "v_mfma_f32_16x16x32_bf16 v[56:59], a[24:27], a[220:223], v[56:59]\n"
    "s_add_u32 m0, s87, 147456\n"
    "ds_read_b64_tr_b16 a[18:19], v237 offset:22528\n"
    "buffer_load_dwordx4 v230, s[44:47], s78 offen offset:0 lds\n"
    "v_mfma_f32_16x16x32_bf16 v[60:63], a[28:31], a[216:219], v[60:63]\n"
    "ds_read_b64_tr_b16 a[20:21], v238 offset:20480\n"
    "v_mfma_f32_16x16x32_bf16 v[64:67], a[28:31], a[220:223], v[64:67]\n"
    "s_waitcnt lgkmcnt(7)\n"
    "ds_read_b64_tr_b16 a[22:23], v238 offset:22528\n"
    "v_mfma_f32_16x16x32_bf16 v[36:39], a[0:3], a[224:227], v[36:39]\n"
    "ds_read_b64_tr_b16 a[24:25], v239 offset:20480\n"
    "v_mfma_f32_16x16x32_bf16 v[40:43], a[0:3], a[228:231], v[40:43]\n"
    "ds_read_b64_tr_b16 a[26:27], v239 offset:22528\n"
    "v_mfma_f32_16x16x32_bf16 v[44:47], a[4:7], a[224:227], v[44:47]\n"
    "ds_read_b64_tr_b16 a[28:29], v240 offset:20480\n"
    "v_mfma_f32_16x16x32_bf16 v[48:51], a[4:7], a[228:231], v[48:51]\n"
    "s_waitcnt lgkmcnt(7)\n"
    "ds_read_b64_tr_b16 a[30:31], v240 offset:22528\n"
    "buffer_load_dwordx4 v231, s[44:47], s78 offen offset:1024 lds\n"
    "v_mfma_f32_16x16x32_bf16 v[52:55], a[8:11], a[224:227], v[52:55]\n"
    "ds_read_b64_tr_b16 a[0:1], v237 offset:24576\n"
    "v_mfma_f32_16x16x32_bf16 v[56:59], a[8:11], a[228:231], v[56:59]\n"
    "ds_read_b64_tr_b16 a[2:3], v237 offset:26624\n"
    "v_mfma_f32_16x16x32_bf16 v[60:63], a[12:15], a[224:227], v[60:63]\n"
    "s_add_u32 m0, s87, 8192\n"
    "ds_read_b64_tr_b16 a[4:5], v238 offset:24576\n"
    "v_mfma_f32_16x16x32_bf16 v[64:67], a[12:15], a[228:231], v[64:67]\n"
    "s_waitcnt lgkmcnt(7)\n"
    "ds_read_b64_tr_b16 a[6:7], v238 offset:26624\n"
    "buffer_load_dwordx4 v228, s[44:47], s80 offen offset:0 lds\n"
    "v_mfma_f32_16x16x32_bf16 v[36:39], a[16:19], a[232:235], v[36:39]\n"
    "ds_read_b64_tr_b16 a[8:9], v239 offset:24576\n"
    "v_mfma_f32_16x16x32_bf16 v[40:43], a[16:19], a[236:239], v[40:43]\n"
    "ds_read_b64_tr_b16 a[10:11], v239 offset:26624\n"
    "v_mfma_f32_16x16x32_bf16 v[44:47], a[20:23], a[232:235], v[44:47]\n"
    "s_buffer_load_dword s17, s[64:67], s91 offset:16 glc\n"
    "ds_read_b64_tr_b16 a[12:13], v240 offset:24576\n"
    "v_mfma_f32_16x16x32_bf16 v[48:51], a[20:23], a[236:239], v[48:51]\n"
    "s_waitcnt lgkmcnt(7)\n"
    "ds_read_b64_tr_b16 a[14:15], v240 offset:26624\n"
    "buffer_load_dwordx4 v229, s[44:47], s80 offen offset:1024 lds\n"
    "v_mfma_f32_16x16x32_bf16 v[52:55], a[24:27], a[232:235], v[52:55]\n"
    "ds_read_b64_tr_b16 a[16:17], v237 offset:28672\n"
    "v_mfma_f32_16x16x32_bf16 v[56:59], a[24:27], a[236:239], v[56:59]\n"
    "ds_read_b64_tr_b16 a[18:19], v237 offset:30720\n"
    "v_mfma_f32_16x16x32_bf16 v[60:63], a[28:31], a[232:235], v[60:63]\n"
    "s_add_u32 m0, s87, 73728\n"
    "ds_read_b64_tr_b16 a[20:21], v238 offset:28672\n"
    "v_mfma_f32_16x16x32_bf16 v[64:67], a[28:31], a[236:239], v[64:67]\n"
    "s_waitcnt lgkmcnt(7)\n"
    "ds_read_b64_tr_b16 a[22:23], v238 offset:30720\n"
    "buffer_load_dwordx4 v230, s[44:47], s80 offen offset:0 lds\n"
    "v_mfma_f32_16x16x32_bf16 v[36:39], a[0:3], a[240:243], v[36:39]\n"
    "ds_read_b64_tr_b16 a[24:25], v239 offset:28672\n"
    "v_mfma_f32_16x16x32_bf16 v[40:43], a[0:3], a[244:247], v[40:43]\n"
    "ds_read_b64_tr_b16 a[26:27], v239 offset:30720\n"
    "v_mfma_f32_16x16x32_bf16 v[44:47], a[4:7], a[240:243], v[44:47]\n"
    "s_buffer_load_dword s18, s[64:67], s91 offset:16 glc\n"
    "ds_read_b64_tr_b16 a[28:29], v240 offset:28672\n"
    "v_mfma_f32_16x16x32_bf16 v[48:51], a[4:7], a[244:247], v[48:51]\n"
    "s_waitcnt vmcnt(28) lgkmcnt(7)\n"
    "ds_read_b64_tr_b16 a[30:31], v240 offset:30720\n"
    "v_mfma_f32_16x16x32_bf16 v[52:55], a[8:11], a[240:243], v[52:55]\n"
    "s_barrier\n"
    "ds_read_b64_tr_b16 a[0:1], v237 offset:32768\n"
    "v_mfma_f32_16x16x32_bf16 v[56:59], a[8:11], a[244:247], v[56:59]\n"
    "ds_read_b64_tr_b16 a[2:3], v237 offset:34816\n"
    "buffer_load_dwordx4 v231, s[44:47], s80 offen offset:1024 lds\n"
    "v_mfma_f32_16x16x32_bf16 v[60:63], a[12:15], a[240:243], v[60:63]\n"
    "ds_read_b64_tr_b16 a[4:5], v238 offset:32768\n"
    "v_mfma_f32_16x16x32_bf16 v[64:67], a[12:15], a[244:247], v[64:67]\n"
    "s_waitcnt lgkmcnt(7)\n"
    "ds_read_b64_tr_b16 a[6:7], v238 offset:34816\n"
    "v_mfma_f32_16x16x32_bf16 v[36:39], a[16:19], a[248:251], v[36:39]\n"
    "ds_read_b64_tr_b16 a[8:9], v239 offset:32768\n"
    "v_mfma_f32_16x16x32_bf16 v[40:43], a[16:19], a[252:255], v[40:43]\n"
    "s_add_u32 m0, s87, 16384\n"
    "ds_read_b64_tr_b16 a[10:11], v239 offset:34816\n"
    "buffer_load_dwordx4 v228, s[44:47], s82 offen offset:0 lds\n"
    "v_mfma_f32_16x16x32_bf16 v[44:47], a[20:23], a[248:251], v[44:47]\n"
    "ds_read_b64_tr_b16 a[12:13], v240 offset:32768\n"
    "v_mfma_f32_16x16x32_bf16 v[48:51], a[20:23], a[252:255], v[48:51]\n"
    "s_waitcnt lgkmcnt(7)\n"
    "ds_read_b64_tr_b16 a[14:15], v240 offset:34816\n"
    "v_mfma_f32_16x16x32_bf16 v[52:55], a[24:27], a[248:251], v[52:55]\n"
    "ds_read_b64_tr_b16 a[16:17], v237 offset:36864\n"
    "v_fma_f32 v164, v100, v36, v164\n"
    "v_fma_f32 v165, v101, v37, v165\n"
    "v_fma_f32 v166, v102, v38, v166\n"
    "v_mfma_f32_16x16x32_bf16 v[56:59], a[24:27], a[252:255], v[56:59]\n"
    "ds_read_b64_tr_b16 a[18:19], v237 offset:38912\n"
    "buffer_load_dwordx4 v229, s[44:47], s82 offen offset:1024 lds\n"
    "v_mfma_f32_16x16x32_bf16 v[60:63], a[28:31], a[248:251], v[60:63]\n"
    "ds_read_b64_tr_b16 a[20:21], v238 offset:36864\n"
    "v_fma_f32 v167, v103, v39, v167\n"
    "v_fma_f32 v168, v104, v40, v168\n"
    "v_fma_f32 v169, v105, v41, v169\n"
    "v_mfma_f32_16x16x32_bf16 v[64:67], a[28:31], a[252:255], v[64:67]\n"
    "s_waitcnt lgkmcnt(7)\n"
    "ds_read_b64_tr_b16 a[22:23], v238 offset:38912\n"
    "v_fma_f32 v170, v106, v42, v170\n"
    "v_fma_f32 v171, v107, v43, v171\n"
    "ds_read_b128 v[36:39], v2 offset:1280\n"
    "v_mfma_f32_16x16x32_bf16 v[8:11], a[0:3], v[200:203], v[4:7]\n"
    "ds_read_b64_tr_b16 a[24:25], v239 offset:36864\n"
    "v_fma_f32 v172, v108, v44, v172\n"
    "v_fma_f32 v173, v109, v45, v173\n"
    "v_fma_f32 v174, v110, v46, v174\n"
    "v_mfma_f32_16x16x32_bf16 v[4:7], a[0:3], v[196:199], v[4:7]\n"
    "s_add_u32 m0, s87, 81920\n"
    "ds_read_b64_tr_b16 a[26:27], v239 offset:38912\n"
    "buffer_load_dwordx4 v230, s[44:47], s82 offen offset:0 lds\n"
    "v_mfma_f32_16x16x32_bf16 v[16:19], a[4:7], v[200:203], v[12:15]\n"
    "ds_read_b64_tr_b16 a[28:29], v240 offset:36864\n"
    "v_fma_f32 v175, v111, v47, v175\n"
    "v_fma_f32 v176, v112, v48, v176\n"
    "v_fma_f32 v177, v113, v49, v177\n"
    "v_mfma_f32_16x16x32_bf16 v[12:15], a[4:7], v[196:199], v[12:15]\n"
    "s_waitcnt lgkmcnt(8)\n"
    "ds_read_b64_tr_b16 a[30:31], v240 offset:38912\n"
    "v_fma_f32 v178, v114, v50, v178\n"
    "v_fma_f32 v179, v115, v51, v179\n"
    "ds_read_b128 v[44:47], v2 offset:1344\n"
    "v_mfma_f32_16x16x32_bf16 v[24:27], a[8:11], v[200:203], v[20:23]\n"
    "s_buffer_load_dword s20, s[64:67], s91 offset:20 glc\n"
    "ds_read_b64_tr_b16 a[0:1], v237 offset:40960\n"
    "v_fma_f32 v180, v116, v52, v180\n"
    "v_fma_f32 v181, v117, v53, v181\n"
    "v_fma_f32 v182, v118, v54, v182\n"
    "v_mfma_f32_16x16x32_bf16 v[20:23], a[8:11], v[196:199], v[20:23]\n"
    "s_add_u32 s99, s98, s71\n"
    "ds_read_b64_tr_b16 a[2:3], v237 offset:43008\n"
    "buffer_load_dwordx4 v231, s[44:47], s82 offen offset:1024 lds\n"
    "v_mfma_f32_16x16x32_bf16 v[32:35], a[12:15], v[200:203], v[28:31]\n"
    "s_buffer_load_dword s19, s[64:67], s91 offset:16 glc\n"
    "ds_read_b64_tr_b16 a[4:5], v238 offset:40960\n"
    "v_fma_f32 v183, v119, v55, v183\n"
    "v_fma_f32 v184, v120, v56, v184\n"
    "v_fma_f32 v185, v121, v57, v185\n"
    "v_mfma_f32_16x16x32_bf16 v[28:31], a[12:15], v[196:199], v[28:31]\n"
    "s_waitcnt lgkmcnt(9)\n"
    "ds_read_b64_tr_b16 a[6:7], v238 offset:43008\n"
    "v_fma_f32 v186, v122, v58, v186\n"
    "v_fma_f32 v187, v123, v59, v187\n"
    "ds_read_b128 v[52:55], v2 offset:1408\n"
    "v_mfma_f32_16x16x32_bf16 v[4:7], a[16:19], v[204:207], v[4:7]\n"
    "s_or_b32 s93, s16, s17\n"
    "s_andn2_b32 s93, 0x10100, s93\n"
    "s_cbranch_scc1 L_sp2_0_%=\n"
    "L_bp2_0_%=:\n"
    "ds_read_b64_tr_b16 a[8:9], v239 offset:40960\n"
    "v_fma_f32 v188, v124, v60, v188\n"
    "v_fma_f32 v189, v125, v61, v189\n"
    "v_fma_f32 v190, v126, v62, v190\n"
    "v_mfma_f32_16x16x32_bf16 v[8:11], a[16:19], v[208:211], v[8:11]\n"
    "s_and_b32 s99, s99, 0x1fffff\n"
    "ds_read_b64_tr_b16 a[10:11], v239 offset:43008\n"
    "buffer_load_dwordx4 a[32:35], v232, s[56:59], s70 offen offset:0 sc1 \n"
    "v_mfma_f32_16x16x32_bf16 v[12:15], a[20:23], v[204:207], v[12:15]\n"
    "s_buffer_load_dwordx2 s[30:31], s[44:47], s99 offset:60\n"
    "ds_read_b64_tr_b16 a[12:13], v240 offset:40960\n"
    "v_fma_f32 v191, v127, v63, v191\n"
    "v_fma_f32 v192, v128, v64, v192\n"
    "v_fma_f32 v193, v129, v65, v193\n"
    "v_mfma_f32_16x16x32_bf16 v[16:19], a[20:23], v[208:211], v[16:19]\n"
    "s_waitcnt lgkmcnt(9)\n"
    "ds_read_b64_tr_b16 a[14:15], v240 offset:43008\n"
    "v_fma_f32 v194, v130, v66, v194\n"
    "v_fma_f32 v195, v131, v67, v195\n"
    "ds_read_b128 v[60:63], v2 offset:1472\n"
    "buffer_load_dwordx4 a[36:39], v232, s[56:59], s70 offen offset:1024 sc1 \n"
    "v_mfma_f32_16x16x32_bf16 v[20:23], a[24:27], v[204:207], v[20:23]\n"
    "ds_read_b64_tr_b16 a[16:17], v237 offset:45056\n"
    "v_cvt_pk_bf16_f32 v212, v164, v165\n"
    "v_cvt_pk_bf16_f32 v213, v166, v167\n"
    "v_cvt_pk_bf16_f32 v214, v172, v173\n"
    "v_mfma_f32_16x16x32_bf16 v[24:27], a[24:27], v[208:211], v[24:27]\n"
    "ds_read_b64_tr_b16 a[18:19], v237 offset:47104\n"
    "buffer_load_dwordx4 a[40:43], v232, s[56:59], s70 offen offset:2048 sc1 \n"
    "v_mfma_f32_16x16x32_bf16 v[28:31], a[28:31], v[204:207], v[28:31]\n"
    "ds_read_b64_tr_b16 a[20:21], v238 offset:45056\n"
    "v_cvt_pk_bf16_f32 v215, v174, v175\n"
    "v_cvt_pk_bf16_f32 v216, v168, v169\n"
    "v_cvt_pk_bf16_f32 v217, v170, v171\n"
    "v_mfma_f32_16x16x32_bf16 v[32:35], a[28:31], v[208:211], v[32:35]\n"
    "s_waitcnt lgkmcnt(9)\n"
    "ds_read_b64_tr_b16 a[22:23], v238 offset:47104\n"
    "v_cvt_pk_bf16_f32 v218, v176, v177\n"
    "v_cvt_pk_bf16_f32 v219, v178, v179\n"
    "v_cvt_pk_bf16_f32 v220, v180, v181\n"
    "buffer_load_dwordx4 a[44:47], v232, s[56:59], s70 offen offset:3072 sc1 \n"
    "v_mfma_f32_16x16x32_bf16 v[40:43], a[0:3], v[200:203], v[36:39]\n"
    "ds_read_b64_tr_b16 a[24:25], v239 offset:45056\n"
    "v_cvt_pk_bf16_f32 v221, v182, v183\n"
    "v_cvt_pk_bf16_f32 v222, v188, v189\n"
    "v_cvt_pk_bf16_f32 v223, v190, v191\n"
    "v_mfma_f32_16x16x32_bf16 v[36:39], a[0:3], v[196:199], v[36:39]\n"
    "ds_read_b64_tr_b16 a[26:27], v239 offset:47104\n"
    "v_mfma_f32_16x16x32_bf16 v[48:51], a[4:7], v[200:203], v[44:47]\n"
    "ds_read_b64_tr_b16 a[28:29], v240 offset:45056\n"
    "v_cvt_pk_bf16_f32 v224, v184, v185\n"
    "v_cvt_pk_bf16_f32 v225, v186, v187\n"
    "v_cvt_pk_bf16_f32 v226, v192, v193\n"
    "v_mfma_f32_16x16x32_bf16 v[44:47], a[4:7], v[196:199], v[44:47]\n"
    "s_waitcnt vmcnt(33) lgkmcnt(7)\n"
    "ds_read_b64_tr_b16 a[30:31], v240 offset:47104\n"
    "v_cvt_pk_bf16_f32 v227, v194, v195\n"
    "v_mfma_f32_16x16x32_bf16 v[56:59], a[8:11], v[200:203], v[52:55]\n"
    "s_barrier\n"
    "ds_read_b64_tr_b16 a[0:1], v237 offset:49152\n"
    "v_mfma_f32_16x16x32_bf16 v[52:55], a[8:11], v[196:199], v[52:55]\n"
    "ds_read_b64_tr_b16 a[2:3], v237 offset:51200\n"
    "buffer_load_dwordx4 a[48:51], v232, s[56:59], s72 offen offset:0 sc1 \n"
    "v_mfma_f32_16x16x32_bf16 v[64:67], a[12:15], v[200:203], v[60:63]\n"
    "s_buffer_load_dword s21, s[64:67], s91 offset:20 glc\n"
    "ds_read_b64_tr_b16 a[4:5], v238 offset:49152\n"
    "v_mfma_f32_16x16x32_bf16 v[60:63], a[12:15], v[196:199], v[60:63]\n"
    "s_waitcnt lgkmcnt(7)\n"
    "ds_read_b64_tr_b16 a[6:7], v238 offset:51200\n"
    "v_mfma_f32_16x16x32_bf16 v[36:39], a[16:19], v[204:207], v[36:39]\n"
    "ds_read_b64_tr_b16 a[8:9], v239 offset:49152\n"
    "v_mfma_f32_16x16x32_bf16 v[40:43], a[16:19], v[208:211], v[40:43]\n"
    "ds_read_b64_tr_b16 a[10:11], v239 offset:51200\n"
    "v_mfma_f32_16x16x32_bf16 v[44:47], a[20:23], v[204:207], v[44:47]\n"
    "ds_read_b64_tr_b16 a[12:13], v240 offset:49152\n"
    "buffer_store_dwordx4 v[212:215], v232, s[56:59], s69 offen offset:0 sc1\n"
    "v_mfma_f32_16x16x32_bf16 v[48:51], a[20:23], v[208:211], v[48:51]\n"
    "s_waitcnt lgkmcnt(7)\n"
    "ds_read_b64_tr_b16 a[14:15], v240 offset:51200\n"
    "v_mfma_f32_16x16x32_bf16 v[52:55], a[24:27], v[204:207], v[52:55]\n"
    "ds_read_b64_tr_b16 a[16:17], v237 offset:53248\n"
    "buffer_store_dwordx4 v[216:219], v232, s[56:59], s69 offen offset:1024 sc1\n"
    "v_mfma_f32_16x16x32_bf16 v[56:59], a[24:27], v[208:211], v[56:59]\n"
    "ds_read_b64_tr_b16 a[18:19], v237 offset:55296\n"
    "v_mfma_f32_16x16x32_bf16 v[60:63], a[28:31], v[204:207], v[60:63]\n"
    "ds_read_b64_tr_b16 a[20:21], v238 offset:53248\n"
    "buffer_store_dwordx4 v[220:223], v232, s[56:59], s69 offen offset:2048 sc1\n"
    "v_mfma_f32_16x16x32_bf16 v[64:67], a[28:31], v[208:211], v[64:67]\n"
    "s_waitcnt lgkmcnt(7)\n"
    "ds_read_b64_tr_b16 a[22:23], v238 offset:55296\n"
    "v_mfma_f32_16x16x32_bf16 v[4:7], a[0:3], v[212:215], v[4:7]\n"
    "ds_read_b64_tr_b16 a[24:25], v239 offset:53248\n"
    "buffer_store_dwordx4 v[224:227], v232, s[56:59], s69 offen offset:3072 sc1\n"
    "v_mfma_f32_16x16x32_bf16 v[8:11], a[0:3], v[216:219], v[8:11]\n"
    "ds_read_b64_tr_b16 a[26:27], v239 offset:55296\n"
    "buffer_load_dwordx4 a[52:55], v232, s[56:59], s72 offen offset:1024 sc1 \n"
    "v_mfma_f32_16x16x32_bf16 v[12:15], a[4:7], v[212:215], v[12:15]\n"
    "ds_read_b64_tr_b16 a[28:29], v240 offset:53248\n"
    "v_mfma_f32_16x16x32_bf16 v[16:19], a[4:7], v[216:219], v[16:19]\n"
    "s_waitcnt lgkmcnt(7)\n"
    "ds_read_b64_tr_b16 a[30:31], v240 offset:55296\n"
    "buffer_load_dwordx4 a[56:59], v232, s[56:59], s72 offen offset:2048 sc1 \n"
    "v_mfma_f32_16x16x32_bf16 v[20:23], a[8:11], v[212:215], v[20:23]\n"
    "s_or_b32 s93, s18, s19\n"
    "s_andn2_b32 s93, 0x1000000, s93\n"
    "s_cbranch_scc1 L_sp2_1_%=\n"
    "L_bp2_1_%=:\n"
    "ds_read_b64_tr_b16 a[0:1], v237 offset:57344\n"
    "v_mfma_f32_16x16x32_bf16 v[24:27], a[8:11], v[216:219], v[24:27]\n"
    "ds_read_b64_tr_b16 a[2:3], v237 offset:59392\n"
    "v_mfma_f32_16x16x32_bf16 v[28:31], a[12:15], v[212:215], v[28:31]\n"
    "ds_read_b64_tr_b16 a[4:5], v238 offset:57344\n"
    "v_mfma_f32_16x16x32_bf16 v[32:35], a[12:15], v[216:219], v[32:35]\n"
    "s_waitcnt lgkmcnt(7)\n"
    "ds_read_b64_tr_b16 a[6:7], v238 offset:59392\n"
    "buffer_load_dwordx4 a[60:63], v232, s[56:59], s72 offen offset:3072 sc1 \n"
    "v_mfma_f32_16x16x32_bf16 v[4:7], a[16:19], v[220:223], v[4:7]\n"
    "ds_read_b64_tr_b16 a[8:9], v239 offset:57344\n"
    "v_mfma_f32_16x16x32_bf16 v[8:11], a[16:19], v[224:227], v[8:11]\n"
    "ds_read_b64_tr_b16 a[10:11], v239 offset:59392\n"
    "v_mfma_f32_16x16x32_bf16 v[12:15], a[20:23], v[220:223], v[12:15]\n"
    "ds_read_b64_tr_b16 a[12:13], v240 offset:57344\n"
    "v_mfma_f32_16x16x32_bf16 v[16:19], a[20:23], v[224:227], v[16:19]\n"
    "s_waitcnt lgkmcnt(7)\n"
    "ds_read_b64_tr_b16 a[14:15], v240 offset:59392\n"
    "v_mfma_f32_16x16x32_bf16 v[20:23], a[24:27], v[220:223], v[20:23]\n"
    "ds_read_b64_tr_b16 a[16:17], v237 offset:61440\n"
    "v_mfma_f32_16x16x32_bf16 v[24:27], a[24:27], v[224:227], v[24:27]\n"
    "s_add_u32 s99, s98, s73\n"
    "ds_read_b64_tr_b16 a[18:19], v237 offset:63488\n"
    "buffer_load_dwordx4 a[64:67], v232, s[56:59], s74 offen offset:0 sc1 \n"
    "v_mfma_f32_16x16x32_bf16 v[28:31], a[28:31], v[220:223], v[28:31]\n"
    "s_and_b32 s99, s99, 0x1fffff\n"
    "ds_read_b64_tr_b16 a[20:21], v238 offset:61440\n"
    "v_mfma_f32_16x16x32_bf16 v[32:35], a[28:31], v[224:227], v[32:35]\n"
    "s_waitcnt lgkmcnt(7)\n"
    "ds_read_b64_tr_b16 a[22:23], v238 offset:63488\n"
    "v_mfma_f32_16x16x32_bf16 v[36:39], a[0:3], v[212:215], v[36:39]\n"
    "s_buffer_load_dwordx2 s[30:31], s[44:47], s99 offset:60\n"
    "ds_read_b64_tr_b16 a[24:25], v239 offset:61440\n"
    "v_mfma_f32_16x16x32_bf16 v[40:43], a[0:3], v[216:219], v[40:43]\n"
    "ds_read_b64_tr_b16 a[26:27], v239 offset:63488\n"
    "buffer_load_dwordx4 a[68:71], v232, s[56:59], s74 offen offset:1024 sc1 \n"
    "v_mfma_f32_16x16x32_bf16 v[44:47], a[4:7], v[212:215], v[44:47]\n"
    "s_or_b32 s93, s20, s21\n"
    "s_andn2_b32 s93, 0x1010101, s93\n"
    "s_cbranch_scc1 L_sp2_2_%=\n"
    "L_bp2_2_%=:\n"
    "ds_read_b64_tr_b16 a[28:29], v240 offset:61440\n"
    "v_mfma_f32_16x16x32_bf16 v[48:51], a[4:7], v[216:219], v[48:51]\n"
    "s_waitcnt vmcnt(39) lgkmcnt(7)\n"
    "ds_read_b64_tr_b16 a[30:31], v240 offset:63488\n"
    "v_mfma_f32_16x16x32_bf16 v[52:55], a[8:11], v[212:215], v[52:55]\n"
    "s_barrier\n"
    "ds_read_b64_tr_b16 a[0:1], v241 offset:24576\n"
    "v_mfma_f32_16x16x32_bf16 v[56:59], a[8:11], v[216:219], v[56:59]\n"
    "ds_read_b64_tr_b16 a[2:3], v241 offset:26624\n"
    "v_mfma_f32_16x16x32_bf16 v[60:63], a[12:15], v[212:215], v[60:63]\n"
    "ds_read_b64_tr_b16 a[4:5], v242 offset:24576\n"
    "v_mfma_f32_16x16x32_bf16 v[64:67], a[12:15], v[216:219], v[64:67]\n"
    "s_waitcnt lgkmcnt(7)\n"
    "ds_read_b64_tr_b16 a[6:7], v242 offset:26624\n"
    "buffer_load_dwordx4 a[72:75], v232, s[56:59], s74 offen offset:2048 sc1 \n"
    "v_mfma_f32_16x16x32_bf16 v[36:39], a[16:19], v[220:223], v[36:39]\n"
    "ds_read_b64_tr_b16 a[8:9], v243 offset:24576\n"
    "v_mfma_f32_16x16x32_bf16 v[40:43], a[16:19], v[224:227], v[40:43]\n"
    "ds_read_b64_tr_b16 a[10:11], v243 offset:26624\n"
    "v_mfma_f32_16x16x32_bf16 v[44:47], a[20:23], v[220:223], v[44:47]\n"
    "ds_read_b64_tr_b16 a[12:13], v244 offset:24576\n"
    "v_mfma_f32_16x16x32_bf16 v[48:51], a[20:23], v[224:227], v[48:51]\n"
    "s_waitcnt lgkmcnt(7)\n"
    "ds_read_b64_tr_b16 a[14:15], v244 offset:26624\n"
    "buffer_load_dwordx4 a[76:79], v232, s[56:59], s74 offen offset:3072 sc1 \n"
    "v_mfma_f32_16x16x32_bf16 v[52:55], a[24:27], v[220:223], v[52:55]\n"
    "ds_read_b64_tr_b16 a[16:17], v241 offset:28672\n"
    "v_mfma_f32_16x16x32_bf16 v[56:59], a[24:27], v[224:227], v[56:59]\n"
    "ds_read_b64_tr_b16 a[18:19], v241 offset:30720\n"
    "v_mfma_f32_16x16x32_bf16 v[60:63], a[28:31], v[220:223], v[60:63]\n"
    "ds_read_b64_tr_b16 a[20:21], v242 offset:28672\n"
    "v_mfma_f32_16x16x32_bf16 v[64:67], a[28:31], v[224:227], v[64:67]\n"
    "s_waitcnt vmcnt(12) lgkmcnt(7)\n"
    "ds_read_b64_tr_b16 a[22:23], v242 offset:30720\n"
    "v_mfma_f32_16x16x32_bf16 v[4:7], a[0:3], a[32:35], v[4:7]\n"
    "ds_read_b64_tr_b16 a[24:25], v243 offset:28672\n"
    "v_mfma_f32_16x16x32_bf16 v[8:11], a[0:3], a[36:39], v[8:11]\n"
    "s_add_u32 s99, s98, s75\n"
    "ds_read_b64_tr_b16 a[26:27], v243 offset:30720\n"
    "buffer_load_dwordx4 a[80:83], v232, s[56:59], s76 offen offset:0 sc1 \n"
    "v_mfma_f32_16x16x32_bf16 v[12:15], a[4:7], a[32:35], v[12:15]\n"
    "s_and_b32 s99, s99, 0x1fffff\n"
    "ds_read_b64_tr_b16 a[28:29], v244 offset:28672\n"
    "v_mfma_f32_16x16x32_bf16 v[16:19], a[4:7], a[36:39], v[16:19]\n"
    "s_waitcnt lgkmcnt(7)\n"
    "ds_read_b64_tr_b16 a[30:31], v244 offset:30720\n"
    "v_mfma_f32_16x16x32_bf16 v[20:23], a[8:11], a[32:35], v[20:23]\n"
    "s_buffer_load_dwordx2 s[30:31], s[44:47], s99 offset:60\n"
    "ds_read_b64_tr_b16 a[0:1], v241 offset:32768\n"
    "v_mfma_f32_16x16x32_bf16 v[24:27], a[8:11], a[36:39], v[24:27]\n"
    "ds_read_b64_tr_b16 a[2:3], v241 offset:34816\n"
    "buffer_load_dwordx4 a[84:87], v232, s[56:59], s76 offen offset:1024 sc1 \n"
    "v_mfma_f32_16x16x32_bf16 v[28:31], a[12:15], a[32:35], v[28:31]\n"
    "ds_read_b64_tr_b16 a[4:5], v242 offset:32768\n"
    "v_mfma_f32_16x16x32_bf16 v[32:35], a[12:15], a[36:39], v[32:35]\n"
    "s_waitcnt lgkmcnt(7)\n"
    "ds_read_b64_tr_b16 a[6:7], v242 offset:34816\n"
    "v_mfma_f32_16x16x32_bf16 v[4:7], a[16:19], a[40:43], v[4:7]\n"
    "ds_read_b64_tr_b16 a[8:9], v243 offset:32768\n"
    "v_mfma_f32_16x16x32_bf16 v[8:11], a[16:19], a[44:47], v[8:11]\n"
    "ds_read_b64_tr_b16 a[10:11], v243 offset:34816\n"
    "v_mfma_f32_16x16x32_bf16 v[12:15], a[20:23], a[40:43], v[12:15]\n"
    "ds_read_b64_tr_b16 a[12:13], v244 offset:32768\n"
    "v_mfma_f32_16x16x32_bf16 v[16:19], a[20:23], a[44:47], v[16:19]\n"
    "s_waitcnt lgkmcnt(7)\n"
    "ds_read_b64_tr_b16 a[14:15], v244 offset:34816\n"
    "buffer_load_dwordx4 a[88:91], v232, s[56:59], s76 offen offset:2048 sc1 \n"
    "v_mfma_f32_16x16x32_bf16 v[20:23], a[24:27], a[40:43], v[20:23]\n"
    "ds_read_b64_tr_b16 a[16:17], v241 offset:36864\n"
    "v_mfma_f32_16x16x32_bf16 v[24:27], a[24:27], a[44:47], v[24:27]\n"
    "ds_read_b64_tr_b16 a[18:19], v241 offset:38912\n"
    "v_mfma_f32_16x16x32_bf16 v[28:31], a[28:31], a[40:43], v[28:31]\n"
    "ds_read_b64_tr_b16 a[20:21], v242 offset:36864\n"
    "v_mfma_f32_16x16x32_bf16 v[32:35], a[28:31], a[44:47], v[32:35]\n"
    "s_waitcnt lgkmcnt(7)\n"
    "ds_read_b64_tr_b16 a[22:23], v242 offset:38912\n"
    "buffer_load_dwordx4 a[92:95], v232, s[56:59], s76 offen offset:3072 sc1 \n"
    "v_mfma_f32_16x16x32_bf16 v[36:39], a[0:3], a[32:35], v[36:39]\n"
    "ds_read_b64_tr_b16 a[24:25], v243 offset:36864\n"
    "v_mfma_f32_16x16x32_bf16 v[40:43], a[0:3], a[36:39], v[40:43]\n"
    "ds_read_b64_tr_b16 a[26:27], v243 offset:38912\n"
    "v_mfma_f32_16x16x32_bf16 v[44:47], a[4:7], a[32:35], v[44:47]\n"
    "ds_read_b64_tr_b16 a[28:29], v244 offset:36864\n"
    "v_mfma_f32_16x16x32_bf16 v[48:51], a[4:7], a[36:39], v[48:51]\n"
    "s_waitcnt lgkmcnt(7)\n"
    "ds_read_b64_tr_b16 a[30:31], v244 offset:38912\n"
    "v_mfma_f32_16x16x32_bf16 v[52:55], a[8:11], a[32:35], v[52:55]\n"
    "s_barrier\n"
    "ds_read_b64_tr_b16 a[0:1], v241 offset:40960\n"
    "v_mfma_f32_16x16x32_bf16 v[56:59], a[8:11], a[36:39], v[56:59]\n"
    "ds_read_b64_tr_b16 a[2:3], v241 offset:43008\n"
    "buffer_load_dwordx4 a[96:99], v232, s[56:59], s78 offen offset:0 sc1 \n"
    "v_mfma_f32_16x16x32_bf16 v[60:63], a[12:15], a[32:35], v[60:63]\n"
    "ds_read_b64_tr_b16 a[4:5], v242 offset:40960\n"
    "v_mfma_f32_16x16x32_bf16 v[64:67], a[12:15], a[36:39], v[64:67]\n"
    "s_waitcnt vmcnt(12) lgkmcnt(7)\n"
    "ds_read_b64_tr_b16 a[6:7], v242 offset:43008\n"
    "buffer_store_byte v255, v253, s[64:67], s92 offen offset:24 sc1\n"
    "v_mfma_f32_16x16x32_bf16 v[36:39], a[16:19], a[40:43], v[36:39]\n"
    "ds_read_b64_tr_b16 a[8:9], v243 offset:40960\n"
    "v_mfma_f32_16x16x32_bf16 v[40:43], a[16:19], a[44:47], v[40:43]\n"
    "ds_read_b64_tr_b16 a[10:11], v243 offset:43008\n"
    "buffer_load_dwordx4 a[100:103], v232, s[56:59], s78 offen offset:1024 sc1 \n"
    "v_mfma_f32_16x16x32_bf16 v[44:47], a[20:23], a[40:43], v[44:47]\n"
    "ds_read_b64_tr_b16 a[12:13], v244 offset:40960\n"
    "v_mfma_f32_16x16x32_bf16 v[48:51], a[20:23], a[44:47], v[48:51]\n"
    "s_waitcnt lgkmcnt(7)\n"
    "ds_read_b64_tr_b16 a[14:15], v244 offset:43008\n"
    "v_mfma_f32_16x16x32_bf16 v[52:55], a[24:27], a[40:43], v[52:55]\n"
    "ds_read_b64_tr_b16 a[16:17], v241 offset:45056\n"
    "v_mfma_f32_16x16x32_bf16 v[56:59], a[24:27], a[44:47], v[56:59]\n"
    "ds_read_b64_tr_b16 a[18:19], v241 offset:47104\n"
    "v_mfma_f32_16x16x32_bf16 v[60:63], a[28:31], a[40:43], v[60:63]\n"
    "ds_read_b64_tr_b16 a[20:21], v242 offset:45056\n"
    "v_mfma_f32_16x16x32_bf16 v[64:67], a[28:31], a[44:47], v[64:67]\n"
    "s_waitcnt vmcnt(11) lgkmcnt(7)\n"
    "ds_read_b64_tr_b16 a[22:23], v242 offset:47104\n"
    "buffer_load_dwordx4 a[104:107], v232, s[56:59], s78 offen offset:2048 sc1 \n"
    "v_mfma_f32_16x16x32_bf16 v[4:7], a[0:3], a[48:51], v[4:7]\n"
    "ds_read_b64_tr_b16 a[24:25], v243 offset:45056\n"
    "v_mfma_f32_16x16x32_bf16 v[8:11], a[0:3], a[52:55], v[8:11]\n"
    "ds_read_b64_tr_b16 a[26:27], v243 offset:47104\n"
    "v_mfma_f32_16x16x32_bf16 v[12:15], a[4:7], a[48:51], v[12:15]\n"
    "ds_read_b64_tr_b16 a[28:29], v244 offset:45056\n"
    "v_mfma_f32_16x16x32_bf16 v[16:19], a[4:7], a[52:55], v[16:19]\n"
    "s_waitcnt lgkmcnt(7)\n"
    "ds_read_b64_tr_b16 a[30:31], v244 offset:47104\n"
    "buffer_load_dwordx4 a[108:111], v232, s[56:59], s78 offen offset:3072 sc1 \n"
    "v_mfma_f32_16x16x32_bf16 v[20:23], a[8:11], a[48:51], v[20:23]\n"
    "ds_read_b64_tr_b16 a[0:1], v241 offset:49152\n"
    "v_mfma_f32_16x16x32_bf16 v[24:27], a[8:11], a[52:55], v[24:27]\n"
    "ds_read_b64_tr_b16 a[2:3], v241 offset:51200\n"
    "v_mfma_f32_16x16x32_bf16 v[28:31], a[12:15], a[48:51], v[28:31]\n"
    "ds_read_b64_tr_b16 a[4:5], v242 offset:49152\n"
    "v_mfma_f32_16x16x32_bf16 v[32:35], a[12:15], a[52:55], v[32:35]\n"
    "s_waitcnt lgkmcnt(7)\n"
    "ds_read_b64_tr_b16 a[6:7], v242 offset:51200\n"
    "v_mfma_f32_16x16x32_bf16 v[4:7], a[16:19], a[56:59], v[4:7]\n"
    "ds_read_b64_tr_b16 a[8:9], v243 offset:49152\n"
    "v_mfma_f32_16x16x32_bf16 v[8:11], a[16:19], a[60:63], v[8:11]\n"
    "ds_read_b64_tr_b16 a[10:11], v243 offset:51200\n"
    "buffer_load_dwordx4 a[112:115], v232, s[56:59], s80 offen offset:0 sc1 \n"
    "v_mfma_f32_16x16x32_bf16 v[12:15], a[20:23], a[56:59], v[12:15]\n"
    "ds_read_b64_tr_b16 a[12:13], v244 offset:49152\n"
    "v_mfma_f32_16x16x32_bf16 v[16:19], a[20:23], a[60:63], v[16:19]\n"
    "s_waitcnt lgkmcnt(7)\n"
    "ds_read_b64_tr_b16 a[14:15], v244 offset:51200\n"
    "v_mfma_f32_16x16x32_bf16 v[20:23], a[24:27], a[56:59], v[20:23]\n"
    "ds_read_b64_tr_b16 a[16:17], v241 offset:53248\n"
    "v_mfma_f32_16x16x32_bf16 v[24:27], a[24:27], a[60:63], v[24:27]\n"
    "L_ip7_%=:\n"
    "s_cbranch_execz L_ipback_%=\n"
    "ds_read_b64_tr_b16 a[18:19], v241 offset:55296\n"
    "buffer_load_dwordx4 a[116:119], v232, s[56:59], s80 offen offset:1024 sc1 \n"
    "v_mfma_f32_16x16x32_bf16 v[28:31], a[28:31], a[56:59], v[28:31]\n"
    "ds_read_b64_tr_b16 a[20:21], v242 offset:53248\n"
    "v_mfma_f32_16x16x32_bf16 v[32:35], a[28:31], a[60:63], v[32:35]\n"
    "s_waitcnt lgkmcnt(7)\n"
    "ds_read_b64_tr_b16 a[22:23], v242 offset:55296\n"
    "v_mfma_f32_16x16x32_bf16 v[36:39], a[0:3], a[48:51], v[36:39]\n"
    "ds_read_b64_tr_b16 a[24:25], v243 offset:53248\n"
    "v_mfma_f32_16x16x32_bf16 v[40:43], a[0:3], a[52:55], v[40:43]\n"
    "ds_read_b64_tr_b16 a[26:27], v243 offset:55296\n"
    "v_mfma_f32_16x16x32_bf16 v[44:47], a[4:7], a[48:51], v[44:47]\n"
    "ds_read_b64_tr_b16 a[28:29], v244 offset:53248\n"
    "v_mfma_f32_16x16x32_bf16 v[48:51], a[4:7], a[52:55], v[48:51]\n"
    "s_waitcnt lgkmcnt(7)\n"
    "ds_read_b64_tr_b16 a[30:31], v244 offset:55296\n"
    "buffer_load_dwordx4 a[120:123], v232, s[56:59], s80 offen offset:2048 sc1 \n"
    "v_mfma_f32_16x16x32_bf16 v[52:55], a[8:11], a[48:51], v[52:55]\n"
    "s_barrier\n"
    "ds_read_b64_tr_b16 a[0:1], v241 offset:57344\n"
    "v_mfma_f32_16x16x32_bf16 v[56:59], a[8:11], a[52:55], v[56:59]\n"
    "ds_read_b64_tr_b16 a[2:3], v241 offset:59392\n"
    "v_mfma_f32_16x16x32_bf16 v[60:63], a[12:15], a[48:51], v[60:63]\n"
    "ds_read_b64_tr_b16 a[4:5], v242 offset:57344\n"
    "v_mfma_f32_16x16x32_bf16 v[64:67], a[12:15], a[52:55], v[64:67]\n"
    "s_waitcnt lgkmcnt(7)\n"
    "ds_read_b64_tr_b16 a[6:7], v242 offset:59392\n"
    "buffer_load_dwordx4 a[124:127], v232, s[56:59], s80 offen offset:3072 sc1 \n"
    "v_mfma_f32_16x16x32_bf16 v[36:39], a[16:19], a[56:59], v[36:39]\n"
    "ds_read_b64_tr_b16 a[8:9], v243 offset:57344\n"
    "v_mfma_f32_16x16x32_bf16 v[40:43], a[16:19], a[60:63], v[40:43]\n"
    "ds_read_b64_tr_b16 a[10:11], v243 offset:59392\n"
    "v_mfma_f32_16x16x32_bf16 v[44:47], a[20:23], a[56:59], v[44:47]\n"
    "ds_read_b64_tr_b16 a[12:13], v244 offset:57344\n"
    "v_mfma_f32_16x16x32_bf16 v[48:51], a[20:23], a[60:63], v[48:51]\n"
    "s_waitcnt lgkmcnt(7)\n"
    "ds_read_b64_tr_b16 a[14:15], v244 offset:59392\n"
    "v_mfma_f32_16x16x32_bf16 v[52:55], a[24:27], a[56:59], v[52:55]\n"
    "ds_read_b64_tr_b16 a[16:17], v241 offset:61440\n"
    "v_mfma_f32_16x16x32_bf16 v[56:59], a[24:27], a[60:63], v[56:59]\n"
    "s_add_u32 m0, s87, 24576\n"
    "ds_read_b64_tr_b16 a[18:19], v241 offset:63488\n"
    "buffer_load_dwordx4 v228, s[44:47], s71 offen offset:0 lds\n"
    "v_mfma_f32_16x16x32_bf16 v[60:63], a[28:31], a[56:59], v[60:63]\n"
    "s_add_u32 s99, s98, s77\n"
    "ds_read_b64_tr_b16 a[20:21], v242 offset:61440\n"
    "v_mfma_f32_16x16x32_bf16 v[64:67], a[28:31], a[60:63], v[64:67]\n"
    "s_waitcnt vmcnt(14) lgkmcnt(7)\n"
    "ds_read_b64_tr_b16 a[22:23], v242 offset:63488\n"
    "v_mfma_f32_16x16x32_bf16 v[4:7], a[0:3], a[64:67], v[4:7]\n"
    "s_and_b32 s99, s99, 0x1fffff\n"
    "ds_read_b64_tr_b16 a[24:25], v243 offset:61440\n"
    "v_mfma_f32_16x16x32_bf16 v[8:11], a[0:3], a[68:71], v[8:11]\n"
    "s_buffer_load_dwordx2 s[30:31], s[44:47], s99 offset:60\n"
    "ds_read_b64_tr_b16 a[26:27], v243 offset:63488\n"
    "buffer_load_dwordx4 v229, s[44:47], s71 offen offset:1024 lds\n"
    "v_mfma_f32_16x16x32_bf16 v[12:15], a[4:7], a[64:67], v[12:15]\n"
    "ds_read_b64_tr_b16 a[28:29], v244 offset:61440\n"
    "v_mfma_f32_16x16x32_bf16 v[16:19], a[4:7], a[68:71], v[16:19]\n"
    "s_waitcnt lgkmcnt(7)\n"
    "ds_read_b64_tr_b16 a[30:31], v244 offset:63488\n"
    "v_mfma_f32_16x16x32_bf16 v[20:23], a[8:11], a[64:67], v[20:23]\n"
    "ds_read_b64_tr_b16 a[0:1], v245 offset:0\n"
    "v_mfma_f32_16x16x32_bf16 v[24:27], a[8:11], a[68:71], v[24:27]\n"
    "ds_read_b64_tr_b16 a[2:3], v245 offset:2048\n"
    "v_mfma_f32_16x16x32_bf16 v[28:31], a[12:15], a[64:67], v[28:31]\n"
    "s_add_u32 m0, s87, 32768\n"
    "ds_read_b64_tr_b16 a[4:5], v246 offset:0\n"
    "v_mfma_f32_16x16x32_bf16 v[32:35], a[12:15], a[68:71], v[32:35]\n"
    "s_waitcnt lgkmcnt(7)\n"
    "ds_read_b64_tr_b16 a[6:7], v246 offset:2048\n"
    "buffer_load_dwordx4 v230, s[44:47], s71 offen offset:0 lds\n"
    "v_mfma_f32_16x16x32_bf16 v[4:7], a[16:19], a[72:75], v[4:7]\n"
    "ds_read_b64_tr_b16 a[8:9], v247 offset:0\n"
    "v_mfma_f32_16x16x32_bf16 v[8:11], a[16:19], a[76:79], v[8:11]\n"
    "ds_read_b64_tr_b16 a[10:11], v247 offset:2048\n"
    "v_mfma_f32_16x16x32_bf16 v[12:15], a[20:23], a[72:75], v[12:15]\n"
    "ds_read_b64_tr_b16 a[12:13], v248 offset:0\n"
    "v_mfma_f32_16x16x32_bf16 v[16:19], a[20:23], a[76:79], v[16:19]\n"
    "s_waitcnt lgkmcnt(7)\n"
    "ds_read_b64_tr_b16 a[14:15], v248 offset:2048\n"
    "buffer_load_dwordx4 v231, s[44:47], s71 offen offset:1024 lds\n"
    "v_mfma_f32_16x16x32_bf16 v[20:23], a[24:27], a[72:75], v[20:23]\n"
    "ds_read_b64_tr_b16 a[16:17], v245 offset:4096\n"
    "v_mfma_f32_16x16x32_bf16 v[24:27], a[24:27], a[76:79], v[24:27]\n"
    "ds_read_b64_tr_b16 a[18:19], v245 offset:6144\n"
    "v_mfma_f32_16x16x32_bf16 v[28:31], a[28:31], a[72:75], v[28:31]\n"
    "ds_read_b64_tr_b16 a[20:21], v246 offset:4096\n"
    "v_mfma_f32_16x16x32_bf16 v[32:35], a[28:31], a[76:79], v[32:35]\n"
    "s_waitcnt lgkmcnt(7)\n"
    "ds_read_b64_tr_b16 a[22:23], v246 offset:6144\n"
    "v_mfma_f32_16x16x32_bf16 v[36:39], a[0:3], a[64:67], v[36:39]\n"
    "ds_read_b64_tr_b16 a[24:25], v247 offset:4096\n"
    "v_mfma_f32_16x16x32_bf16 v[40:43], a[0:3], a[68:71], v[40:43]\n"
    "ds_read_b64_tr_b16 a[26:27], v247 offset:6144\n"
    "buffer_load_dwordx4 a[128:131], v232, s[56:59], s82 offen offset:0 sc1 \n"
    "v_mfma_f32_16x16x32_bf16 v[44:47], a[4:7], a[64:67], v[44:47]\n"
    "ds_read_b64_tr_b16 a[28:29], v248 offset:4096\n"
    "v_mfma_f32_16x16x32_bf16 v[48:51], a[4:7], a[68:71], v[48:51]\n"
    "s_waitcnt lgkmcnt(7)\n"
    "ds_read_b64_tr_b16 a[30:31], v248 offset:6144\n"
    "v_mfma_f32_16x16x32_bf16 v[52:55], a[8:11], a[64:67], v[52:55]\n"
    "s_barrier\n"
    "ds_read_b64_tr_b16 a[0:1], v241 offset:0\n"
    "v_mfma_f32_16x16x32_bf16 v[56:59], a[8:11], a[68:71], v[56:59]\n"
    "ds_read_b64_tr_b16 a[2:3], v241 offset:2048\n"
    "buffer_load_dwordx4 a[132:135], v232, s[56:59], s82 offen offset:1024 sc1 \n"
    "v_mfma_f32_16x16x32_bf16 v[60:63], a[12:15], a[64:67], v[60:63]\n"
    "ds_read_b64_tr_b16 a[4:5], v242 offset:0\n"
    "v_mfma_f32_16x16x32_bf16 v[64:67], a[12:15], a[68:71], v[64:67]\n"
    "s_waitcnt lgkmcnt(7)\n"
    "ds_read_b64_tr_b16 a[6:7], v242 offset:2048\n"
    "v_mfma_f32_16x16x32_bf16 v[36:39], a[16:19], a[72:75], v[36:39]\n"
    "s_buffer_load_dword s22, s[64:67], s91 offset:24 glc\n"
    "ds_read_b64_tr_b16 a[8:9], v243 offset:0\n"
    "v_mfma_f32_16x16x32_bf16 v[40:43], a[16:19], a[76:79], v[40:43]\n"
    "ds_read_b64_tr_b16 a[10:11], v243 offset:2048\n"
    "v_mfma_f32_16x16x32_bf16 v[44:47], a[20:23], a[72:75], v[44:47]\n"
    "ds_read_b64_tr_b16 a[12:13], v244 offset:0\n"
    "v_mfma_f32_16x16x32_bf16 v[48:51], a[20:23], a[76:79], v[48:51]\n"
    "s_waitcnt lgkmcnt(7)\n"
    "ds_read_b64_tr_b16 a[14:15], v244 offset:2048\n"
    "buffer_load_dwordx4 a[136:139], v232, s[56:59], s82 offen offset:2048 sc1 \n"
    "v_mfma_f32_16x16x32_bf16 v[52:55], a[24:27], a[72:75], v[52:55]\n"
    "ds_read_b64_tr_b16 a[16:17], v241 offset:4096\n"
    "v_mfma_f32_16x16x32_bf16 v[56:59], a[24:27], a[76:79], v[56:59]\n"
    "ds_read_b64_tr_b16 a[18:19], v241 offset:6144\n"
    "v_mfma_f32_16x16x32_bf16 v[60:63], a[28:31], a[72:75], v[60:63]\n"
    "ds_read_b64_tr_b16 a[20:21], v242 offset:4096\n"
    "v_mfma_f32_16x16x32_bf16 v[64:67], a[28:31], a[76:79], v[64:67]\n"
    "s_waitcnt vmcnt(16) lgkmcnt(7)\n"
    "ds_read_b64_tr_b16 a[22:23], v242 offset:6144\n"
    "buffer_load_dwordx4 a[140:143], v232, s[56:59], s82 offen offset:3072 sc1 \n"
    "v_mfma_f32_16x16x32_bf16 v[4:7], a[0:3], a[80:83], v[4:7]\n"
    "ds_read_b64_tr_b16 a[24:25], v243 offset:4096\n"
    "v_mfma_f32_16x16x32_bf16 v[8:11], a[0:3], a[84:87], v[8:11]\n"
    "ds_read_b64_tr_b16 a[26:27], v243 offset:6144\n"
    "v_mfma_f32_16x16x32_bf16 v[12:15], a[4:7], a[80:83], v[12:15]\n"
    "ds_read_b64_tr_b16 a[28:29], v244 offset:4096\n"
    "v_mfma_f32_16x16x32_bf16 v[16:19], a[4:7], a[84:87], v[16:19]\n"
    "s_waitcnt lgkmcnt(7)\n"
    "ds_read_b64_tr_b16 a[30:31], v244 offset:6144\n"
    "v_mfma_f32_16x16x32_bf16 v[20:23], a[8:11], a[80:83], v[20:23]\n"
    "ds_read_b64_tr_b16 a[0:1], v245 offset:8192\n"
    "v_mfma_f32_16x16x32_bf16 v[24:27], a[8:11], a[84:87], v[24:27]\n"
    "s_add_u32 m0, s87, 40960\n"
    "ds_read_b64_tr_b16 a[2:3], v245 offset:10240\n"
    "buffer_load_dwordx4 v228, s[44:47], s73 offen offset:0 lds\n"
    "v_mfma_f32_16x16x32_bf16 v[28:31], a[12:15], a[80:83], v[28:31]\n"
    "ds_read_b64_tr_b16 a[4:5], v246 offset:8192\n"
    "v_mfma_f32_16x16x32_bf16 v[32:35], a[12:15], a[84:87], v[32:35]\n"
    "s_waitcnt lgkmcnt(7)\n"
    "ds_read_b64_tr_b16 a[6:7], v246 offset:10240\n"
    "v_mfma_f32_16x16x32_bf16 v[4:7], a[16:19], a[88:91], v[4:7]\n"
    "ds_read_b64_tr_b16 a[8:9], v247 offset:8192\n"
    "v_mfma_f32_16x16x32_bf16 v[8:11], a[16:19], a[92:95], v[8:11]\n"
    "ds_read_b64_tr_b16 a[10:11], v247 offset:10240\n"
    "buffer_load_dwordx4 v229, s[44:47], s73 offen offset:1024 lds\n"
    "v_mfma_f32_16x16x32_bf16 v[12:15], a[20:23], a[88:91], v[12:15]\n"
    "s_buffer_load_dword s23, s[64:67], s91 offset:24 glc\n"
    "ds_read_b64_tr_b16 a[12:13], v248 offset:8192\n"
    "v_mfma_f32_16x16x32_bf16 v[16:19], a[20:23], a[92:95], v[16:19]\n"
    "s_waitcnt lgkmcnt(7)\n"
    "ds_read_b64_tr_b16 a[14:15], v248 offset:10240\n"
    "v_mfma_f32_16x16x32_bf16 v[20:23], a[24:27], a[88:91], v[20:23]\n"
    "ds_read_b64_tr_b16 a[16:17], v245 offset:12288\n"
    "v_mfma_f32_16x16x32_bf16 v[24:27], a[24:27], a[92:95], v[24:27]\n"
    "ds_read_b64_tr_b16 a[18:19], v245 offset:14336\n"
    "v_mfma_f32_16x16x32_bf16 v[28:31], a[28:31], a[88:91], v[28:31]\n"
    "s_add_u32 m0, s87, 49152\n"
    "ds_read_b64_tr_b16 a[20:21], v246 offset:12288\n"
    "v_mfma_f32_16x16x32_bf16 v[32:35], a[28:31], a[92:95], v[32:35]\n"
    "s_waitcnt lgkmcnt(7)\n"
    "ds_read_b64_tr_b16 a[22:23], v246 offset:14336\n"
    "buffer_load_dwordx4 v230, s[44:47], s73 offen offset:0 lds\n"
    "v_mfma_f32_16x16x32_bf16 v[36:39], a[0:3], a[80:83], v[36:39]\n"
    "ds_read_b64_tr_b16 a[24:25], v247 offset:12288\n"
    "v_mfma_f32_16x16x32_bf16 v[40:43], a[0:3], a[84:87], v[40:43]\n"
    "ds_read_b64_tr_b16 a[26:27], v247 offset:14336\n"
    "v_mfma_f32_16x16x32_bf16 v[44:47], a[4:7], a[80:83], v[44:47]\n"
    "ds_read_b64_tr_b16 a[28:29], v248 offset:12288\n"
    "v_mfma_f32_16x16x32_bf16 v[48:51], a[4:7], a[84:87], v[48:51]\n"
    "s_waitcnt lgkmcnt(7)\n"
    "ds_read_b64_tr_b16 a[30:31], v248 offset:14336\n"
    "buffer_load_dwordx4 v231, s[44:47], s73 offen offset:1024 lds\n"
    "v_mfma_f32_16x16x32_bf16 v[52:55], a[8:11], a[80:83], v[52:55]\n"
    "s_barrier\n"
    "ds_read_b64_tr_b16 a[0:1], v237 offset:0\n"
    "v_mfma_f32_16x16x32_bf16 v[56:59], a[8:11], a[84:87], v[56:59]\n"
    "ds_read_b64_tr_b16 a[2:3], v237 offset:2048\n"
    "v_mfma_f32_16x16x32_bf16 v[60:63], a[12:15], a[80:83], v[60:63]\n"
    "s_buffer_load_dword s24, s[64:67], s91 offset:24 glc\n"
    "ds_read_b64_tr_b16 a[4:5], v238 offset:0\n"
    "v_mfma_f32_16x16x32_bf16 v[64:67], a[12:15], a[84:87], v[64:67]\n"
    "s_waitcnt lgkmcnt(7)\n"
    "ds_read_b64_tr_b16 a[6:7], v238 offset:2048\n"
    "v_mfma_f32_16x16x32_bf16 v[36:39], a[16:19], a[88:91], v[36:39]\n"
    "ds_read_b64_tr_b16 a[8:9], v239 offset:0\n"
    "v_mfma_f32_16x16x32_bf16 v[40:43], a[16:19], a[92:95], v[40:43]\n"
    "s_add_u32 m0, s87, 57344\n"
    "ds_read_b64_tr_b16 a[10:11], v239 offset:2048\n"
    "buffer_load_dwordx4 v228, s[44:47], s75 offen offset:0 lds\n"
    "v_mfma_f32_16x16x32_bf16 v[44:47], a[20:23], a[88:91], v[44:47]\n"
    "ds_read_b64_tr_b16 a[12:13], v240 offset:0\n"
    "v_mfma_f32_16x16x32_bf16 v[48:51], a[20:23], a[92:95], v[48:51]\n"
    "s_waitcnt lgkmcnt(7)\n"
    "ds_read_b64_tr_b16 a[14:15], v240 offset:2048\n"
    "v_mfma_f32_16x16x32_bf16 v[52:55], a[24:27], a[88:91], v[52:55]\n"
    "ds_read_b64_tr_b16 a[16:17], v237 offset:4096\n"
    "v_mfma_f32_16x16x32_bf16 v[56:59], a[24:27], a[92:95], v[56:59]\n"
    "s_add_u32 s99, s98, s79\n"
    "ds_read_b64_tr_b16 a[18:19], v237 offset:6144\n"
    "buffer_load_dwordx4 v229, s[44:47], s75 offen offset:1024 lds\n"
    "v_mfma_f32_16x16x32_bf16 v[60:63], a[28:31], a[88:91], v[60:63]\n"
    "s_and_b32 s99, s99, 0x1fffff\n"
    "ds_read_b64_tr_b16 a[20:21], v238 offset:4096\n"
    "v_mfma_f32_16x16x32_bf16 v[64:67], a[28:31], a[92:95], v[64:67]\n"
    "s_waitcnt vmcnt(18) lgkmcnt(7)\n"
    "ds_read_b64_tr_b16 a[22:23], v238 offset:6144\n"
    "v_mfma_f32_16x16x32_bf16 v[4:7], a[0:3], a[96:99], v[4:7]\n"
    "s_buffer_load_dwordx2 s[30:31], s[44:47], s99 offset:60\n"
    "ds_read_b64_tr_b16 a[24:25], v239 offset:4096\n"
    "v_mfma_f32_16x16x32_bf16 v[8:11], a[0:3], a[100:103], v[8:11]\n"
    "ds_read_b64_tr_b16 a[26:27], v239 offset:6144\n"
    "v_mfma_f32_16x16x32_bf16 v[12:15], a[4:7], a[96:99], v[12:15]\n"
    "s_add_u32 m0, s87, 90112\n"
    "ds_read_b64_tr_b16 a[28:29], v240 offset:4096\n"
    "v_mfma_f32_16x16x32_bf16 v[16:19], a[4:7], a[100:103], v[16:19]\n"
    "s_waitcnt lgkmcnt(7)\n"
    "ds_read_b64_tr_b16 a[30:31], v240 offset:6144\n"
    "buffer_load_dwordx4 v230, s[44:47], s75 offen offset:0 lds\n"
    "v_mfma_f32_16x16x32_bf16 v[20:23], a[8:11], a[96:99], v[20:23]\n"
    "ds_read_b64_tr_b16 a[0:1], v245 offset:16384\n"
    "v_mfma_f32_16x16x32_bf16 v[24:27], a[8:11], a[100:103], v[24:27]\n"
    "ds_read_b64_tr_b16 a[2:3], v245 offset:18432\n"
    "v_mfma_f32_16x16x32_bf16 v[28:31], a[12:15], a[96:99], v[28:31]\n"
    "s_buffer_load_dword s25, s[64:67], s91 offset:24 glc\n"
    "ds_read_b64_tr_b16 a[4:5], v246 offset:16384\n"
    "v_mfma_f32_16x16x32_bf16 v[32:35], a[12:15], a[100:103], v[32:35]\n"
    "s_waitcnt lgkmcnt(7)\n"
    "ds_read_b64_tr_b16 a[6:7], v246 offset:18432\n"
    "buffer_load_dwordx4 v231, s[44:47], s75 offen offset:1024 lds\n"
    "v_mfma_f32_16x16x32_bf16 v[4:7], a[16:19], a[104:107], v[4:7]\n"
    "s_or_b32 s93, s22, s23\n"
    "s_andn2_b32 s93, 0x100, s93\n"
    "s_cbranch_scc1 L_sp3_0_%=\n"
    "L_bp3_0_%=:\n"
    "ds_read_b64_tr_b16 a[8:9], v247 offset:16384\n"
    "v_mfma_f32_16x16x32_bf16 v[8:11], a[16:19], a[108:111], v[8:11]\n"
    "ds_read_b64_tr_b16 a[10:11], v247 offset:18432\n"
    "v_mfma_f32_16x16x32_bf16 v[12:15], a[20:23], a[104:107], v[12:15]\n"
    "ds_read_b64_tr_b16 a[12:13], v248 offset:16384\n"
    "v_mfma_f32_16x16x32_bf16 v[16:19], a[20:23], a[108:111], v[16:19]\n"
    "s_waitcnt lgkmcnt(7)\n"
    "ds_read_b64_tr_b16 a[14:15], v248 offset:18432\n"
    "v_mfma_f32_16x16x32_bf16 v[20:23], a[24:27], a[104:107], v[20:23]\n"
    "ds_read_b64_tr_b16 a[16:17], v245 offset:20480\n"
    "v_mfma_f32_16x16x32_bf16 v[24:27], a[24:27], a[108:111], v[24:27]\n"
    "ds_read_b64_tr_b16 a[18:19], v245 offset:22528\n"
    "buffer_load_dwordx4 a[144:147], v232, s[56:59], s71 offen offset:0 sc1 \n"
    "v_mfma_f32_16x16x32_bf16 v[28:31], a[28:31], a[104:107], v[28:31]\n"
    "ds_read_b64_tr_b16 a[20:21], v246 offset:20480\n"
    "v_mfma_f32_16x16x32_bf16 v[32:35], a[28:31], a[108:111], v[32:35]\n"
    "s_waitcnt lgkmcnt(7)\n"
    "ds_read_b64_tr_b16 a[22:23], v246 offset:22528\n"
    "v_mfma_f32_16x16x32_bf16 v[36:39], a[0:3], a[96:99], v[36:39]\n"
    "ds_read_b64_tr_b16 a[24:25], v247 offset:20480\n"
    "v_mfma_f32_16x16x32_bf16 v[40:43], a[0:3], a[100:103], v[40:43]\n"
    "ds_read_b64_tr_b16 a[26:27], v247 offset:22528\n"
    "buffer_load_dwordx4 a[148:151], v232, s[56:59], s71 offen offset:1024 sc1 \n"
    "v_mfma_f32_16x16x32_bf16 v[44:47], a[4:7], a[96:99], v[44:47]\n"
    "ds_read_b64_tr_b16 a[28:29], v248 offset:20480\n"
    "v_mfma_f32_16x16x32_bf16 v[48:51], a[4:7], a[100:103], v[48:51]\n"
    "s_waitcnt lgkmcnt(7)\n"
    "ds_read_b64_tr_b16 a[30:31], v248 offset:22528\n"
    "v_mfma_f32_16x16x32_bf16 v[52:55], a[8:11], a[96:99], v[52:55]\n"
    "s_barrier\n"
    "ds_read_b64_tr_b16 a[0:1], v237 offset:8192\n"
    "v_mfma_f32_16x16x32_bf16 v[56:59], a[8:11], a[100:103], v[56:59]\n"
    "ds_read_b64_tr_b16 a[2:3], v237 offset:10240\n"
    "v_mfma_f32_16x16x32_bf16 v[60:63], a[12:15], a[96:99], v[60:63]\n"
    "ds_read_b64_tr_b16 a[4:5], v238 offset:8192\n"
    "v_mfma_f32_16x16x32_bf16 v[64:67], a[12:15], a[100:103], v[64:67]\n"
    "s_waitcnt lgkmcnt(7)\n"
    "ds_read_b64_tr_b16 a[6:7], v238 offset:10240\n"
    "buffer_load_dwordx4 a[152:155], v232, s[56:59], s71 offen offset:2048 sc1 \n"
    "v_mfma_f32_16x16x32_bf16 v[36:39], a[16:19], a[104:107], v[36:39]\n"
    "ds_read_b64_tr_b16 a[8:9], v239 offset:8192\n"
    "v_mfma_f32_16x16x32_bf16 v[40:43], a[16:19], a[108:111], v[40:43]\n"
    "ds_read_b64_tr_b16 a[10:11], v239 offset:10240\n"
    "v_mfma_f32_16x16x32_bf16 v[44:47], a[20:23], a[104:107], v[44:47]\n"
    "ds_read_b64_tr_b16 a[12:13], v240 offset:8192\n"
    "v_mfma_f32_16x16x32_bf16 v[48:51], a[20:23], a[108:111], v[48:51]\n"
    "s_waitcnt lgkmcnt(7)\n"
    "ds_read_b64_tr_b16 a[14:15], v240 offset:10240\n"
    "buffer_load_dwordx4 a[156:159], v232, s[56:59], s71 offen offset:3072 sc1 \n"
    "v_mfma_f32_16x16x32_bf16 v[52:55], a[24:27], a[104:107], v[52:55]\n"
    "ds_read_b64_tr_b16 a[16:17], v237 offset:12288\n"
    "v_mfma_f32_16x16x32_bf16 v[56:59], a[24:27], a[108:111], v[56:59]\n"
    "ds_read_b64_tr_b16 a[18:19], v237 offset:14336\n"
    "v_mfma_f32_16x16x32_bf16 v[60:63], a[28:31], a[104:107], v[60:63]\n"
    "ds_read_b64_tr_b16 a[20:21], v238 offset:12288\n"
    "v_mfma_f32_16x16x32_bf16 v[64:67], a[28:31], a[108:111], v[64:67]\n"
    "s_waitcnt vmcnt(20) lgkmcnt(7)\n"
    "ds_read_b64_tr_b16 a[22:23], v238 offset:14336\n"
    "v_mfma_f32_16x16x32_bf16 v[4:7], a[0:3], a[112:115], v[4:7]\n"
    "ds_read_b64_tr_b16 a[24:25], v239 offset:12288\n"
    "v_mfma_f32_16x16x32_bf16 v[8:11], a[0:3], a[116:119], v[8:11]\n"
    "s_add_u32 m0, s87, 98304\n"
    "ds_read_b64_tr_b16 a[26:27], v239 offset:14336\n"
    "buffer_load_dwordx4 v228, s[44:47], s77 offen offset:0 lds\n"
    "v_mfma_f32_16x16x32_bf16 v[12:15], a[4:7], a[112:115], v[12:15]\n"
    "ds_read_b64_tr_b16 a[28:29], v240 offset:12288\n"
    "v_mfma_f32_16x16x32_bf16 v[16:19], a[4:7], a[116:119], v[16:19]\n"
    "s_waitcnt lgkmcnt(7)\n"
    "ds_read_b64_tr_b16 a[30:31], v240 offset:14336\n"
    "v_mfma_f32_16x16x32_bf16 v[20:23], a[8:11], a[112:115], v[20:23]\n"
    "ds_read_b64_tr_b16 a[0:1], v241 offset:8192\n"
    "v_mfma_f32_16x16x32_bf16 v[24:27], a[8:11], a[116:119], v[24:27]\n"
    "s_add_u32 s99, s98, s81\n"
    "ds_read_b64_tr_b16 a[2:3], v241 offset:10240\n"
    "buffer_load_dwordx4 v229, s[44:47], s77 offen offset:1024 lds\n"
    "v_mfma_f32_16x16x32_bf16 v[28:31], a[12:15], a[112:115], v[28:31]\n"
    "s_or_b32 s93, s24, s25\n"
    "s_andn2_b32 s93, 0x1010000, s93\n"
    "s_cbranch_scc1 L_sp3_1_%=\n"
    "L_bp3_1_%=:\n"
    "ds_read_b64_tr_b16 a[4:5], v242 offset:8192\n"
    "v_mfma_f32_16x16x32_bf16 v[32:35], a[12:15], a[116:119], v[32:35]\n"
    "s_waitcnt lgkmcnt(7)\n"
    "ds_read_b64_tr_b16 a[6:7], v242 offset:10240\n"
    "v_mfma_f32_16x16x32_bf16 v[4:7], a[16:19], a[120:123], v[4:7]\n"
    "s_and_b32 s99, s99, 0x1fffff\n"
    "ds_read_b64_tr_b16 a[8:9], v243 offset:8192\n"
    "v_mfma_f32_16x16x32_bf16 v[8:11], a[16:19], a[124:127], v[8:11]\n"
    "s_buffer_load_dwordx2 s[30:31], s[44:47], s99 offset:60\n"
    "ds_read_b64_tr_b16 a[10:11], v243 offset:10240\n"
    "v_mfma_f32_16x16x32_bf16 v[12:15], a[20:23], a[120:123], v[12:15]\n"
    "ds_read_b64_tr_b16 a[12:13], v244 offset:8192\n"
    "v_mfma_f32_16x16x32_bf16 v[16:19], a[20:23], a[124:127], v[16:19]\n"
    "s_waitcnt lgkmcnt(7)\n"
    "ds_read_b64_tr_b16 a[14:15], v244 offset:10240\n"
    "buffer_load_dwordx4 a[160:163], v232, s[56:59], s73 offen offset:0 sc1 \n"
    "v_mfma_f32_16x16x32_bf16 v[20:23], a[24:27], a[120:123], v[20:23]\n"
    "ds_read_b64_tr_b16 a[16:17], v241 offset:12288\n"
    "v_mfma_f32_16x16x32_bf16 v[24:27], a[24:27], a[124:127], v[24:27]\n"
    "s_add_u32 s99, s98, s83\n"
    "ds_read_b64_tr_b16 a[18:19], v241 offset:14336\n"
    "v_mfma_f32_16x16x32_bf16 v[28:31], a[28:31], a[120:123], v[28:31]\n"
    "s_and_b32 s99, s99, 0x1fffff\n"
    "ds_read_b64_tr_b16 a[20:21], v242 offset:12288\n"
    "v_mfma_f32_16x16x32_bf16 v[32:35], a[28:31], a[124:127], v[32:35]\n"
    "s_waitcnt lgkmcnt(7)\n"
    "ds_read_b64_tr_b16 a[22:23], v242 offset:14336\n"
    "buffer_load_dwordx4 a[164:167], v232, s[56:59], s73 offen offset:1024 sc1 \n"
    "v_mfma_f32_16x16x32_bf16 v[36:39], a[0:3], a[112:115], v[36:39]\n"
    "s_buffer_load_dwordx2 s[30:31], s[44:47], s99 offset:60\n"
    "ds_read_b64_tr_b16 a[24:25], v243 offset:12288\n"
    "v_mfma_f32_16x16x32_bf16 v[40:43], a[0:3], a[116:119], v[40:43]\n"
    "ds_read_b64_tr_b16 a[26:27], v243 offset:14336\n"
    "v_mfma_f32_16x16x32_bf16 v[44:47], a[4:7], a[112:115], v[44:47]\n"
    "s_buffer_load_dword s26, s[64:67], s91 offset:28 glc\n"
    "ds_read_b64_tr_b16 a[28:29], v244 offset:12288\n"
    "v_mfma_f32_16x16x32_bf16 v[48:51], a[4:7], a[116:119], v[48:51]\n"
    "s_waitcnt lgkmcnt(7)\n"
    "ds_read_b64_tr_b16 a[30:31], v244 offset:14336\n"
    "v_mfma_f32_16x16x32_bf16 v[52:55], a[8:11], a[112:115], v[52:55]\n"
    "s_barrier\n"
    "ds_read_b64_tr_b16 a[0:1], v237 offset:16384\n"
    "v_mfma_f32_16x16x32_bf16 v[56:59], a[8:11], a[116:119], v[56:59]\n"
    "ds_read_b64_tr_b16 a[2:3], v237 offset:18432\n"
    "buffer_load_dwordx4 a[168:171], v232, s[56:59], s73 offen offset:2048 sc1 \n"
    "v_mfma_f32_16x16x32_bf16 v[60:63], a[12:15], a[112:115], v[60:63]\n"
    "ds_read_b64_tr_b16 a[4:5], v238 offset:16384\n"
    "v_mfma_f32_16x16x32_bf16 v[64:67], a[12:15], a[116:119], v[64:67]\n"
    "s_waitcnt lgkmcnt(7)\n"
    "ds_read_b64_tr_b16 a[6:7], v238 offset:18432\n"
    "v_mfma_f32_16x16x32_bf16 v[36:39], a[16:19], a[120:123], v[36:39]\n"
    "ds_read_b64_tr_b16 a[8:9], v239 offset:16384\n"
    "v_mfma_f32_16x16x32_bf16 v[40:43], a[16:19], a[124:127], v[40:43]\n"
    "ds_read_b64_tr_b16 a[10:11], v239 offset:18432\n"
    "buffer_load_dwordx4 a[172:175], v232, s[56:59], s73 offen offset:3072 sc1 \n"
    "v_mfma_f32_16x16x32_bf16 v[44:47], a[20:23], a[120:123], v[44:47]\n"
    "ds_read_b64_tr_b16 a[12:13], v240 offset:16384\n"
    "v_mfma_f32_16x16x32_bf16 v[48:51], a[20:23], a[124:127], v[48:51]\n"
    "s_waitcnt lgkmcnt(7)\n"
    "ds_read_b64_tr_b16 a[14:15], v240 offset:18432\n"
    "v_mfma_f32_16x16x32_bf16 v[52:55], a[24:27], a[120:123], v[52:55]\n"
    "ds_read_b64_tr_b16 a[16:17], v237 offset:20480\n"
    "v_mfma_f32_16x16x32_bf16 v[56:59], a[24:27], a[124:127], v[56:59]\n"
    "ds_read_b64_tr_b16 a[18:19], v237 offset:22528\n"
    "v_mfma_f32_16x16x32_bf16 v[60:63], a[28:31], a[120:123], v[60:63]\n"
    "s_add_u32 m0, s87, 106496\n"
    "ds_read_b64_tr_b16 a[20:21], v238 offset:20480\n"
    "v_mfma_f32_16x16x32_bf16 v[64:67], a[28:31], a[124:127], v[64:67]\n"
    "s_waitcnt vmcnt(18) lgkmcnt(7)\n"
    "ds_read_b64_tr_b16 a[22:23], v238 offset:22528\n"
    "buffer_load_dwordx4 v230, s[44:47], s77 offen offset:0 lds\n"
    "v_mfma_f32_16x16x32_bf16 v[4:7], a[0:3], a[128:131], v[4:7]\n"
    "ds_read_b64_tr_b16 a[24:25], v239 offset:20480\n"
    "v_mfma_f32_16x16x32_bf16 v[8:11], a[0:3], a[132:135], v[8:11]\n"
    "ds_read_b64_tr_b16 a[26:27], v239 offset:22528\n"
    "v_mfma_f32_16x16x32_bf16 v[12:15], a[4:7], a[128:131], v[12:15]\n"
    "ds_read_b64_tr_b16 a[28:29], v240 offset:20480\n"
    "v_mfma_f32_16x16x32_bf16 v[16:19], a[4:7], a[132:135], v[16:19]\n"
    "s_waitcnt lgkmcnt(7)\n"
    "ds_read_b64_tr_b16 a[30:31], v240 offset:22528\n"
    "buffer_load_dwordx4 v231, s[44:47], s77 offen offset:1024 lds\n"
    "v_mfma_f32_16x16x32_bf16 v[20:23], a[8:11], a[128:131], v[20:23]\n"
    "s_buffer_load_dword s27, s[64:67], s91 offset:28 glc\n"
    "ds_read_b64_tr_b16 a[0:1], v241 offset:16384\n"
    "v_mfma_f32_16x16x32_bf16 v[24:27], a[8:11], a[132:135], v[24:27]\n"
    "ds_read_b64_tr_b16 a[2:3], v241 offset:18432\n"
    "v_mfma_f32_16x16x32_bf16 v[28:31], a[12:15], a[128:131], v[28:31]\n"
    "ds_read_b64_tr_b16 a[4:5], v242 offset:16384\n"
    "v_mfma_f32_16x16x32_bf16 v[32:35], a[12:15], a[132:135], v[32:35]\n"
    "s_waitcnt lgkmcnt(7)\n"
    "ds_read_b64_tr_b16 a[6:7], v242 offset:18432\n"
    "buffer_load_dwordx4 a[176:179], v232, s[56:59], s75 offen offset:0 sc1 \n"
    "v_mfma_f32_16x16x32_bf16 v[4:7], a[16:19], a[136:139], v[4:7]\n"
    "ds_read_b64_tr_b16 a[8:9], v243 offset:16384\n"
    "v_mfma_f32_16x16x32_bf16 v[8:11], a[16:19], a[140:143], v[8:11]\n"
    "ds_read_b64_tr_b16 a[10:11], v243 offset:18432\n"
    "v_mfma_f32_16x16x32_bf16 v[12:15], a[20:23], a[136:139], v[12:15]\n"
    "ds_read_b64_tr_b16 a[12:13], v244 offset:16384\n"
    "v_mfma_f32_16x16x32_bf16 v[16:19], a[20:23], a[140:143], v[16:19]\n"
    "s_waitcnt lgkmcnt(7)\n"
    "ds_read_b64_tr_b16 a[14:15], v244 offset:18432\n"
    "buffer_load_dwordx4 a[180:183], v232, s[56:59], s75 offen offset:1024 sc1 \n"
    "v_mfma_f32_16x16x32_bf16 v[20:23], a[24:27], a[136:139], v[20:23]\n"
    "ds_read_b64_tr_b16 a[16:17], v241 offset:20480\n"
    "v_mfma_f32_16x16x32_bf16 v[24:27], a[24:27], a[140:143], v[24:27]\n"
    "ds_read_b64_tr_b16 a[18:19], v241 offset:22528\n"
    "v_mfma_f32_16x16x32_bf16 v[28:31], a[28:31], a[136:139], v[28:31]\n"
    "ds_read_b64_tr_b16 a[20:21], v242 offset:20480\n"
    "v_mfma_f32_16x16x32_bf16 v[32:35], a[28:31], a[140:143], v[32:35]\n"
    "s_waitcnt lgkmcnt(7)\n"
    "ds_read_b64_tr_b16 a[22:23], v242 offset:22528\n"
    "buffer_load_dwordx4 a[184:187], v232, s[56:59], s75 offen offset:2048 sc1 \n"
    "v_mfma_f32_16x16x32_bf16 v[36:39], a[0:3], a[128:131], v[36:39]\n"
    "ds_read_b64_tr_b16 a[24:25], v243 offset:20480\n"
    "v_mfma_f32_16x16x32_bf16 v[40:43], a[0:3], a[132:135], v[40:43]\n"
    "ds_read_b64_tr_b16 a[26:27], v243 offset:22528\n"
    "v_mfma_f32_16x16x32_bf16 v[44:47], a[4:7], a[128:131], v[44:47]\n"
    "ds_read_b64_tr_b16 a[28:29], v244 offset:20480\n"
    "v_mfma_f32_16x16x32_bf16 v[48:51], a[4:7], a[132:135], v[48:51]\n"
    "s_waitcnt lgkmcnt(7)\n"
    "ds_read_b64_tr_b16 a[30:31], v244 offset:22528\n"
    "buffer_load_dwordx4 a[188:191], v232, s[56:59], s75 offen offset:3072 sc1 \n"
    "v_mfma_f32_16x16x32_bf16 v[52:55], a[8:11], a[128:131], v[52:55]\n"
    "s_barrier\n"
    "ds_read_b64_tr_b16 a[0:1], v237 offset:24576\n"
    "v_mfma_f32_16x16x32_bf16 v[56:59], a[8:11], a[132:135], v[56:59]\n"
    "ds_read_b64_tr_b16 a[2:3], v237 offset:26624\n"
    "v_mfma_f32_16x16x32_bf16 v[60:63], a[12:15], a[128:131], v[60:63]\n"
    "s_add_u32 m0, s87, 114688\n"
    "ds_read_b64_tr_b16 a[4:5], v238 offset:24576\n"
    "v_mfma_f32_16x16x32_bf16 v[64:67], a[12:15], a[132:135], v[64:67]\n"
    "s_waitcnt lgkmcnt(7)\n"
    "ds_read_b64_tr_b16 a[6:7], v238 offset:26624\n"
    "buffer_load_dwordx4 v228, s[44:47], s79 offen offset:0 lds\n"
    "v_mfma_f32_16x16x32_bf16 v[36:39], a[16:19], a[136:139], v[36:39]\n"
    "ds_read_b64_tr_b16 a[8:9], v239 offset:24576\n"
    "v_mfma_f32_16x16x32_bf16 v[40:43], a[16:19], a[140:143], v[40:43]\n"
    "ds_read_b64_tr_b16 a[10:11], v239 offset:26624\n"
    "v_mfma_f32_16x16x32_bf16 v[44:47], a[20:23], a[136:139], v[44:47]\n"
    "ds_read_b64_tr_b16 a[12:13], v240 offset:24576\n"
    "v_mfma_f32_16x16x32_bf16 v[48:51], a[20:23], a[140:143], v[48:51]\n"
    "s_waitcnt lgkmcnt(7)\n"
    "ds_read_b64_tr_b16 a[14:15], v240 offset:26624\n"
    "buffer_load_dwordx4 v229, s[44:47], s79 offen offset:1024 lds\n"
    "v_mfma_f32_16x16x32_bf16 v[52:55], a[24:27], a[136:139], v[52:55]\n"
    "ds_read_b64_tr_b16 a[16:17], v237 offset:28672\n"
    "v_mfma_f32_16x16x32_bf16 v[56:59], a[24:27], a[140:143], v[56:59]\n"
    "ds_read_b64_tr_b16 a[18:19], v237 offset:30720\n"
    "v_mfma_f32_16x16x32_bf16 v[60:63], a[28:31], a[136:139], v[60:63]\n"
    "s_add_u32 m0, s87, 122880\n"
    "ds_read_b64_tr_b16 a[20:21], v238 offset:28672\n"
    "v_mfma_f32_16x16x32_bf16 v[64:67], a[28:31], a[140:143], v[64:67]\n"
    "s_waitcnt vmcnt(14) lgkmcnt(7)\n"
    "ds_read_b64_tr_b16 a[22:23], v238 offset:30720\n"
    "buffer_load_dwordx4 v228, s[44:47], s81 offen offset:0 lds\n"
    "v_mfma_f32_16x16x32_bf16 v[4:7], a[0:3], a[144:147], v[4:7]\n"
    "ds_read_b64_tr_b16 a[24:25], v239 offset:28672\n"
    "v_mfma_f32_16x16x32_bf16 v[8:11], a[0:3], a[148:151], v[8:11]\n"
    "ds_read_b64_tr_b16 a[26:27], v239 offset:30720\n"
    "v_mfma_f32_16x16x32_bf16 v[12:15], a[4:7], a[144:147], v[12:15]\n"
    "s_or_b32 s93, s26, s27\n"
    "s_andn2_b32 s93, 0x1010101, s93\n"
    "s_cbranch_scc1 L_sp3_2_%=\n"
    "L_bp3_2_%=:\n"
    "ds_read_b64_tr_b16 a[28:29], v240 offset:28672\n"
    "v_mfma_f32_16x16x32_bf16 v[16:19], a[4:7], a[148:151], v[16:19]\n"
    "s_waitcnt lgkmcnt(7)\n"
    "ds_read_b64_tr_b16 a[30:31], v240 offset:30720\n"
    "buffer_load_dwordx4 v229, s[44:47], s81 offen offset:1024 lds\n"
    "v_mfma_f32_16x16x32_bf16 v[20:23], a[8:11], a[144:147], v[20:23]\n"
    "ds_read_b64_tr_b16 a[0:1], v237 offset:32768\n"
    "v_mfma_f32_16x16x32_bf16 v[24:27], a[8:11], a[148:151], v[24:27]\n"
    "ds_read_b64_tr_b16 a[2:3], v237 offset:34816\n"
    "v_mfma_f32_16x16x32_bf16 v[28:31], a[12:15], a[144:147], v[28:31]\n"
    "ds_read_b64_tr_b16 a[4:5], v238 offset:32768\n"
    "v_mfma_f32_16x16x32_bf16 v[32:35], a[12:15], a[148:151], v[32:35]\n"
    "s_waitcnt lgkmcnt(7)\n"
    "ds_read_b64_tr_b16 a[6:7], v238 offset:34816\n"
    "buffer_load_dwordx4 a[192:195], v232, s[56:59], s77 offen offset:0 sc1 \n"
    "v_mfma_f32_16x16x32_bf16 v[4:7], a[16:19], a[152:155], v[4:7]\n"
    "ds_read_b64_tr_b16 a[8:9], v239 offset:32768\n"
    "v_mfma_f32_16x16x32_bf16 v[8:11], a[16:19], a[156:159], v[8:11]\n"
    "ds_read_b64_tr_b16 a[10:11], v239 offset:34816\n"
    "v_mfma_f32_16x16x32_bf16 v[12:15], a[20:23], a[152:155], v[12:15]\n"
    "ds_read_b64_tr_b16 a[12:13], v240 offset:32768\n"
    "v_mfma_f32_16x16x32_bf16 v[16:19], a[20:23], a[156:159], v[16:19]\n"
    "s_waitcnt lgkmcnt(7)\n"
    "ds_read_b64_tr_b16 a[14:15], v240 offset:34816\n"
    "buffer_load_dwordx4 a[196:199], v232, s[56:59], s77 offen offset:1024 sc1 \n"
    "v_mfma_f32_16x16x32_bf16 v[20:23], a[24:27], a[152:155], v[20:23]\n"
    "ds_read_b64_tr_b16 a[16:17], v237 offset:36864\n"
    "v_mfma_f32_16x16x32_bf16 v[24:27], a[24:27], a[156:159], v[24:27]\n"
    "ds_read_b64_tr_b16 a[18:19], v237 offset:38912\n"
    "v_mfma_f32_16x16x32_bf16 v[28:31], a[28:31], a[152:155], v[28:31]\n"
    "ds_read_b64_tr_b16 a[20:21], v238 offset:36864\n"
    "v_mfma_f32_16x16x32_bf16 v[32:35], a[28:31], a[156:159], v[32:35]\n"
    "s_waitcnt lgkmcnt(7)\n"
    "ds_read_b64_tr_b16 a[22:23], v238 offset:38912\n"
    "buffer_load_dwordx4 a[200:203], v232, s[56:59], s77 offen offset:2048 sc1 \n"
    "v_mfma_f32_16x16x32_bf16 v[36:39], a[0:3], a[144:147], v[36:39]\n"
    "ds_read_b64_tr_b16 a[24:25], v239 offset:36864\n"
    "v_mfma_f32_16x16x32_bf16 v[40:43], a[0:3], a[148:151], v[40:43]\n"
    "ds_read_b64_tr_b16 a[26:27], v239 offset:38912\n"
    "v_mfma_f32_16x16x32_bf16 v[44:47], a[4:7], a[144:147], v[44:47]\n"
    "ds_read_b64_tr_b16 a[28:29], v240 offset:36864\n"
    "v_mfma_f32_16x16x32_bf16 v[48:51], a[4:7], a[148:151], v[48:51]\n"
    "s_waitcnt lgkmcnt(7)\n"
    "ds_read_b64_tr_b16 a[30:31], v240 offset:38912\n"
    "buffer_load_dwordx4 a[204:207], v232, s[56:59], s77 offen offset:3072 sc1 \n"
    "v_mfma_f32_16x16x32_bf16 v[52:55], a[8:11], a[144:147], v[52:55]\n"
    "s_barrier\n"
    "ds_read_b64_tr_b16 a[0:1], v237 offset:40960\n"
    "v_mfma_f32_16x16x32_bf16 v[56:59], a[8:11], a[148:151], v[56:59]\n"
    "ds_read_b64_tr_b16 a[2:3], v237 offset:43008\n"
    "v_mfma_f32_16x16x32_bf16 v[60:63], a[12:15], a[144:147], v[60:63]\n"
    "s_add_u32 m0, s87, 65536\n"
    "ds_read_b64_tr_b16 a[4:5], v238 offset:40960\n"
    "v_mfma_f32_16x16x32_bf16 v[64:67], a[12:15], a[148:151], v[64:67]\n"
    "s_waitcnt lgkmcnt(7)\n"
    "ds_read_b64_tr_b16 a[6:7], v238 offset:43008\n"
    "buffer_load_dwordx4 v228, s[44:47], s83 offen offset:0 lds\n"
    "v_mfma_f32_16x16x32_bf16 v[36:39], a[16:19], a[152:155], v[36:39]\n"
    "L_ip8_%=:\n"
    "s_cbranch_execz L_ipback_%=\n"
    "ds_read_b64_tr_b16 a[8:9], v239 offset:40960\n"
    "v_mfma_f32_16x16x32_bf16 v[40:43], a[16:19], a[156:159], v[40:43]\n"
    "ds_read_b64_tr_b16 a[10:11], v239 offset:43008\n"
    "v_mfma_f32_16x16x32_bf16 v[44:47], a[20:23], a[152:155], v[44:47]\n"
    "ds_read_b64_tr_b16 a[12:13], v240 offset:40960\n"
    "v_mfma_f32_16x16x32_bf16 v[48:51], a[20:23], a[156:159], v[48:51]\n"
    "s_waitcnt lgkmcnt(7)\n"
    "ds_read_b64_tr_b16 a[14:15], v240 offset:43008\n"
    "buffer_load_dwordx4 v229, s[44:47], s83 offen offset:1024 lds\n"
    "v_mfma_f32_16x16x32_bf16 v[52:55], a[24:27], a[152:155], v[52:55]\n"
    "ds_read_b64_tr_b16 a[16:17], v237 offset:45056\n"
    "v_mfma_f32_16x16x32_bf16 v[56:59], a[24:27], a[156:159], v[56:59]\n"
    "ds_read_b64_tr_b16 a[18:19], v237 offset:47104\n"
    "v_mfma_f32_16x16x32_bf16 v[60:63], a[28:31], a[152:155], v[60:63]\n"
    "s_add_u32 m0, s87, 131072\n"
    "ds_read_b64_tr_b16 a[20:21], v238 offset:45056\n"
    "v_mfma_f32_16x16x32_bf16 v[64:67], a[28:31], a[156:159], v[64:67]\n"
    "s_waitcnt vmcnt(16) lgkmcnt(7)\n"
    "ds_read_b64_tr_b16 a[22:23], v238 offset:47104\n"
    "buffer_load_dwordx4 v230, s[44:47], s79 offen offset:0 lds\n"
    "v_mfma_f32_16x16x32_bf16 v[4:7], a[0:3], a[160:163], v[4:7]\n"
    "ds_read_b64_tr_b16 a[24:25], v239 offset:45056\n"
    "v_mfma_f32_16x16x32_bf16 v[8:11], a[0:3], a[164:167], v[8:11]\n"
    "ds_read_b64_tr_b16 a[26:27], v239 offset:47104\n"
    "v_mfma_f32_16x16x32_bf16 v[12:15], a[4:7], a[160:163], v[12:15]\n"
    "ds_read_b64_tr_b16 a[28:29], v240 offset:45056\n"
    "v_mfma_f32_16x16x32_bf16 v[16:19], a[4:7], a[164:167], v[16:19]\n"
    "s_waitcnt lgkmcnt(7)\n"
    "ds_read_b64_tr_b16 a[30:31], v240 offset:47104\n"
    "buffer_load_dwordx4 v231, s[44:47], s79 offen offset:1024 lds\n"
    "v_mfma_f32_16x16x32_bf16 v[20:23], a[8:11], a[160:163], v[20:23]\n"
    "ds_read_b64_tr_b16 a[0:1], v237 offset:49152\n"
    "v_mfma_f32_16x16x32_bf16 v[24:27], a[8:11], a[164:167], v[24:27]\n"
    "ds_read_b64_tr_b16 a[2:3], v237 offset:51200\n"
    "v_mfma_f32_16x16x32_bf16 v[28:31], a[12:15], a[160:163], v[28:31]\n"
    "ds_read_b64_tr_b16 a[4:5], v238 offset:49152\n"
    "v_mfma_f32_16x16x32_bf16 v[32:35], a[12:15], a[164:167], v[32:35]\n"
    "s_waitcnt lgkmcnt(7)\n"
    "ds_read_b64_tr_b16 a[6:7], v238 offset:51200\n"
    "buffer_load_dwordx4 a[208:211], v232, s[56:59], s79 offen offset:0 sc1 \n"
    "v_mfma_f32_16x16x32_bf16 v[4:7], a[16:19], a[168:171], v[4:7]\n"
    "ds_read_b64_tr_b16 a[8:9], v239 offset:49152\n"
    "v_mfma_f32_16x16x32_bf16 v[8:11], a[16:19], a[172:175], v[8:11]\n"
    "ds_read_b64_tr_b16 a[10:11], v239 offset:51200\n"
    "v_mfma_f32_16x16x32_bf16 v[12:15], a[20:23], a[168:171], v[12:15]\n"
    "ds_read_b64_tr_b16 a[12:13], v240 offset:49152\n"
    "v_mfma_f32_16x16x32_bf16 v[16:19], a[20:23], a[172:175], v[16:19]\n"
    "s_waitcnt lgkmcnt(7)\n"
    "ds_read_b64_tr_b16 a[14:15], v240 offset:51200\n"
    "buffer_load_dwordx4 a[212:215], v232, s[56:59], s79 offen offset:1024 sc1 \n"
    "v_mfma_f32_16x16x32_bf16 v[20:23], a[24:27], a[168:171], v[20:23]\n"
    "ds_read_b64_tr_b16 a[16:17], v237 offset:53248\n"
    "v_mfma_f32_16x16x32_bf16 v[24:27], a[24:27], a[172:175], v[24:27]\n"
    "ds_read_b64_tr_b16 a[18:19], v237 offset:55296\n"
    "v_mfma_f32_16x16x32_bf16 v[28:31], a[28:31], a[168:171], v[28:31]\n"
    "ds_read_b64_tr_b16 a[20:21], v238 offset:53248\n"
    "v_mfma_f32_16x16x32_bf16 v[32:35], a[28:31], a[172:175], v[32:35]\n"
    "s_waitcnt lgkmcnt(7)\n"
    "ds_read_b64_tr_b16 a[22:23], v238 offset:55296\n"
    "buffer_load_dwordx4 a[216:219], v232, s[56:59], s79 offen offset:2048 sc1 \n"
    "v_mfma_f32_16x16x32_bf16 v[36:39], a[0:3], a[160:163], v[36:39]\n"
    "ds_read_b64_tr_b16 a[24:25], v239 offset:53248\n"
    "v_mfma_f32_16x16x32_bf16 v[40:43], a[0:3], a[164:167], v[40:43]\n"
    "ds_read_b64_tr_b16 a[26:27], v239 offset:55296\n"
    "v_mfma_f32_16x16x32_bf16 v[44:47], a[4:7], a[160:163], v[44:47]\n"
    "ds_read_b64_tr_b16 a[28:29], v240 offset:53248\n"
    "v_mfma_f32_16x16x32_bf16 v[48:51], a[4:7], a[164:167], v[48:51]\n"
    "s_waitcnt lgkmcnt(7)\n"
    "ds_read_b64_tr_b16 a[30:31], v240 offset:55296\n"
    "buffer_load_dwordx4 a[220:223], v232, s[56:59], s79 offen offset:3072 sc1 \n"
    "v_mfma_f32_16x16x32_bf16 v[52:55], a[8:11], a[160:163], v[52:55]\n"
    "s_barrier\n"
    "ds_read_b64_tr_b16 a[0:1], v237 offset:57344\n"
    "v_mfma_f32_16x16x32_bf16 v[56:59], a[8:11], a[164:167], v[56:59]\n"
    "ds_read_b64_tr_b16 a[2:3], v237 offset:59392\n"
    "v_mfma_f32_16x16x32_bf16 v[60:63], a[12:15], a[160:163], v[60:63]\n"
    "ds_read_b64_tr_b16 a[4:5], v238 offset:57344\n"
    "v_mfma_f32_16x16x32_bf16 v[64:67], a[12:15], a[164:167], v[64:67]\n"
    "s_waitcnt lgkmcnt(7)\n"
    "ds_read_b64_tr_b16 a[6:7], v238 offset:59392\n"
    "buffer_load_dwordx4 a[224:227], v232, s[56:59], s81 offen offset:0 sc1 \n"
    "v_mfma_f32_16x16x32_bf16 v[36:39], a[16:19], a[168:171], v[36:39]\n"
    "ds_read_b64_tr_b16 a[8:9], v239 offset:57344\n"
    "v_mfma_f32_16x16x32_bf16 v[40:43], a[16:19], a[172:175], v[40:43]\n"
    "ds_read_b64_tr_b16 a[10:11], v239 offset:59392\n"
    "v_mfma_f32_16x16x32_bf16 v[44:47], a[20:23], a[168:171], v[44:47]\n"
    "ds_read_b64_tr_b16 a[12:13], v240 offset:57344\n"
    "v_mfma_f32_16x16x32_bf16 v[48:51], a[20:23], a[172:175], v[48:51]\n"
    "s_waitcnt lgkmcnt(7)\n"
    "ds_read_b64_tr_b16 a[14:15], v240 offset:59392\n"
    "buffer_load_dwordx4 a[228:231], v232, s[56:59], s81 offen offset:1024 sc1 \n"
    "v_mfma_f32_16x16x32_bf16 v[52:55], a[24:27], a[168:171], v[52:55]\n"
    "ds_read_b64_tr_b16 a[16:17], v237 offset:61440\n"
    "v_mfma_f32_16x16x32_bf16 v[56:59], a[24:27], a[172:175], v[56:59]\n"
    "ds_read_b64_tr_b16 a[18:19], v237 offset:63488\n"
    "v_mfma_f32_16x16x32_bf16 v[60:63], a[28:31], a[168:171], v[60:63]\n"
    "ds_read_b64_tr_b16 a[20:21], v238 offset:61440\n"
    "v_mfma_f32_16x16x32_bf16 v[64:67], a[28:31], a[172:175], v[64:67]\n"
    "s_waitcnt vmcnt(18) lgkmcnt(7)\n"
    "ds_read_b64_tr_b16 a[22:23], v238 offset:63488\n"
    "buffer_load_dwordx4 a[232:235], v232, s[56:59], s81 offen offset:2048 sc1 \n"
    "v_mfma_f32_16x16x32_bf16 v[4:7], a[0:3], a[176:179], v[4:7]\n"
    "ds_read_b64_tr_b16 a[24:25], v239 offset:61440\n"
    "v_mfma_f32_16x16x32_bf16 v[8:11], a[0:3], a[180:183], v[8:11]\n"
    "ds_read_b64_tr_b16 a[26:27], v239 offset:63488\n"
    "v_mfma_f32_16x16x32_bf16 v[12:15], a[4:7], a[176:179], v[12:15]\n"
    "ds_read_b64_tr_b16 a[28:29], v240 offset:61440\n"
    "v_mfma_f32_16x16x32_bf16 v[16:19], a[4:7], a[180:183], v[16:19]\n"
    "s_waitcnt lgkmcnt(7)\n"
    "ds_read_b64_tr_b16 a[30:31], v240 offset:63488\n"
    "buffer_load_dwordx4 a[236:239], v232, s[56:59], s81 offen offset:3072 sc1 \n"
    "v_mfma_f32_16x16x32_bf16 v[20:23], a[8:11], a[176:179], v[20:23]\n"
    "ds_read_b64_tr_b16 a[0:1], v241 offset:24576\n"
    "v_mfma_f32_16x16x32_bf16 v[24:27], a[8:11], a[180:183], v[24:27]\n"
    "ds_read_b64_tr_b16 a[2:3], v241 offset:26624\n"
    "v_mfma_f32_16x16x32_bf16 v[28:31], a[12:15], a[176:179], v[28:31]\n"
    "s_add_u32 m0, s87, 0\n"
    "ds_read_b64_tr_b16 a[4:5], v242 offset:24576\n"
    "v_mfma_f32_16x16x32_bf16 v[32:35], a[12:15], a[180:183], v[32:35]\n"
    "s_waitcnt lgkmcnt(7)\n"
    "ds_read_b64_tr_b16 a[6:7], v242 offset:26624\n"
    "buffer_load_dwordx4 v230, s[44:47], s81 offen offset:0 lds\n"
    "v_mfma_f32_16x16x32_bf16 v[4:7], a[16:19], a[184:187], v[4:7]\n"
    "ds_read_b64_tr_b16 a[8:9], v243 offset:24576\n"
    "v_mfma_f32_16x16x32_bf16 v[8:11], a[16:19], a[188:191], v[8:11]\n"
    "ds_read_b64_tr_b16 a[10:11], v243 offset:26624\n"
    "v_mfma_f32_16x16x32_bf16 v[12:15], a[20:23], a[184:187], v[12:15]\n"
    "ds_read_b64_tr_b16 a[12:13], v244 offset:24576\n"
    "v_mfma_f32_16x16x32_bf16 v[16:19], a[20:23], a[188:191], v[16:19]\n"
    "s_waitcnt lgkmcnt(7)\n"
    "ds_read_b64_tr_b16 a[14:15], v244 offset:26624\n"
    "buffer_load_dwordx4 v231, s[44:47], s81 offen offset:1024 lds\n"
    "v_mfma_f32_16x16x32_bf16 v[20:23], a[24:27], a[184:187], v[20:23]\n"
    "ds_read_b64_tr_b16 a[16:17], v241 offset:28672\n"
    "v_mfma_f32_16x16x32_bf16 v[24:27], a[24:27], a[188:191], v[24:27]\n"
    "ds_read_b64_tr_b16 a[18:19], v241 offset:30720\n"
    "v_mfma_f32_16x16x32_bf16 v[28:31], a[28:31], a[184:187], v[28:31]\n"
    "s_add_u32 m0, s87, 139264\n"
    "ds_read_b64_tr_b16 a[20:21], v242 offset:28672\n"
    "v_mfma_f32_16x16x32_bf16 v[32:35], a[28:31], a[188:191], v[32:35]\n"
    "s_waitcnt lgkmcnt(7)\n"
    "ds_read_b64_tr_b16 a[22:23], v242 offset:30720\n"
    "buffer_load_dwordx4 v230, s[44:47], s83 offen offset:0 lds\n"
    "v_mfma_f32_16x16x32_bf16 v[36:39], a[0:3], a[176:179], v[36:39]\n"
    "ds_read_b64_tr_b16 a[24:25], v243 offset:28672\n"
    "v_mfma_f32_16x16x32_bf16 v[40:43], a[0:3], a[180:183], v[40:43]\n"
    "ds_read_b64_tr_b16 a[26:27], v243 offset:30720\n"
    "v_mfma_f32_16x16x32_bf16 v[44:47], a[4:7], a[176:179], v[44:47]\n"
    "ds_read_b64_tr_b16 a[28:29], v244 offset:28672\n"
    "v_mfma_f32_16x16x32_bf16 v[48:51], a[4:7], a[180:183], v[48:51]\n"
    "s_waitcnt lgkmcnt(7)\n"
    "ds_read_b64_tr_b16 a[30:31], v244 offset:30720\n"
    "buffer_load_dwordx4 v231, s[44:47], s83 offen offset:1024 lds\n"
    "v_mfma_f32_16x16x32_bf16 v[52:55], a[8:11], a[176:179], v[52:55]\n"
    "s_barrier\n"
    "ds_read_b64_tr_b16 a[0:1], v241 offset:32768\n"
    "v_mfma_f32_16x16x32_bf16 v[56:59], a[8:11], a[180:183], v[56:59]\n"
    "ds_read_b64_tr_b16 a[2:3], v241 offset:34816\n"
    "v_mfma_f32_16x16x32_bf16 v[60:63], a[12:15], a[176:179], v[60:63]\n"
    "ds_read_b64_tr_b16 a[4:5], v242 offset:32768\n"
    "v_mfma_f32_16x16x32_bf16 v[64:67], a[12:15], a[180:183], v[64:67]\n"
    "s_waitcnt lgkmcnt(7)\n"
    "ds_read_b64_tr_b16 a[6:7], v242 offset:34816\n"
    "buffer_load_dwordx4 a[240:243], v232, s[56:59], s83 offen offset:0 sc1 \n"
    "v_mfma_f32_16x16x32_bf16 v[36:39], a[16:19], a[184:187], v[36:39]\n"
    "ds_read_b64_tr_b16 a[8:9], v243 offset:32768\n"
    "v_mfma_f32_16x16x32_bf16 v[40:43], a[16:19], a[188:191], v[40:43]\n"
    "ds_read_b64_tr_b16 a[10:11], v243 offset:34816\n"
    "v_mfma_f32_16x16x32_bf16 v[44:47], a[20:23], a[184:187], v[44:47]\n"
    "ds_read_b64_tr_b16 a[12:13], v244 offset:32768\n"
    "v_mfma_f32_16x16x32_bf16 v[48:51], a[20:23], a[188:191], v[48:51]\n"
    "s_waitcnt lgkmcnt(7)\n"
    "ds_read_b64_tr_b16 a[14:15], v244 offset:34816\n"
    "buffer_load_dwordx4 a[244:247], v232, s[56:59], s83 offen offset:1024 sc1 \n"
    "v_mfma_f32_16x16x32_bf16 v[52:55], a[24:27], a[184:187], v[52:55]\n"
    "ds_read_b64_tr_b16 a[16:17], v241 offset:36864\n"
    "v_mfma_f32_16x16x32_bf16 v[56:59], a[24:27], a[188:191], v[56:59]\n"
    "ds_read_b64_tr_b16 a[18:19], v241 offset:38912\n"
    "v_mfma_f32_16x16x32_bf16 v[60:63], a[28:31], a[184:187], v[60:63]\n"
    "ds_read_b64_tr_b16 a[20:21], v242 offset:36864\n"
    "v_mfma_f32_16x16x32_bf16 v[64:67], a[28:31], a[188:191], v[64:67]\n"
    "s_waitcnt vmcnt(18) lgkmcnt(7)\n"
    "ds_read_b64_tr_b16 a[22:23], v242 offset:38912\n"
    "buffer_load_dwordx4 a[248:251], v232, s[56:59], s83 offen offset:2048 sc1 \n"
    "v_mfma_f32_16x16x32_bf16 v[4:7], a[0:3], a[192:195], v[4:7]\n"
    "ds_read_b64_tr_b16 a[24:25], v243 offset:36864\n"
    "v_mfma_f32_16x16x32_bf16 v[8:11], a[0:3], a[196:199], v[8:11]\n"
    "ds_read_b64_tr_b16 a[26:27], v243 offset:38912\n"
    "v_mfma_f32_16x16x32_bf16 v[12:15], a[4:7], a[192:195], v[12:15]\n"
    "ds_read_b64_tr_b16 a[28:29], v244 offset:36864\n"
    "v_mfma_f32_16x16x32_bf16 v[16:19], a[4:7], a[196:199], v[16:19]\n"
    "s_waitcnt lgkmcnt(7)\n"
    "ds_read_b64_tr_b16 a[30:31], v244 offset:38912\n"
    "buffer_load_dwordx4 a[252:255], v232, s[56:59], s83 offen offset:3072 sc1 \n"
    "v_mfma_f32_16x16x32_bf16 v[20:23], a[8:11], a[192:195], v[20:23]\n"
    "ds_read_b64_tr_b16 a[0:1], v241 offset:40960\n"
    "v_mfma_f32_16x16x32_bf16 v[24:27], a[8:11], a[196:199], v[24:27]\n"
    "ds_read_b64_tr_b16 a[2:3], v241 offset:43008\n"
    "v_mfma_f32_16x16x32_bf16 v[28:31], a[12:15], a[192:195], v[28:31]\n"
    "ds_read_b64_tr_b16 a[4:5], v242 offset:40960\n"
    "v_mfma_f32_16x16x32_bf16 v[32:35], a[12:15], a[196:199], v[32:35]\n"
    "s_waitcnt lgkmcnt(7)\n"
    "ds_read_b64_tr_b16 a[6:7], v242 offset:43008\n"
    "v_mfma_f32_16x16x32_bf16 v[4:7], a[16:19], a[200:203], v[4:7]\n"
    "ds_read_b64_tr_b16 a[8:9], v243 offset:40960\n"
    "v_mfma_f32_16x16x32_bf16 v[8:11], a[16:19], a[204:207], v[8:11]\n"
    "ds_read_b64_tr_b16 a[10:11], v243 offset:43008\n"
    "v_mfma_f32_16x16x32_bf16 v[12:15], a[20:23], a[200:203], v[12:15]\n"
    "ds_read_b64_tr_b16 a[12:13], v244 offset:40960\n"
    "v_mfma_f32_16x16x32_bf16 v[16:19], a[20:23], a[204:207], v[16:19]\n"
    "s_waitcnt lgkmcnt(7)\n"
    "ds_read_b64_tr_b16 a[14:15], v244 offset:43008\n"
    "v_mfma_f32_16x16x32_bf16 v[20:23], a[24:27], a[200:203], v[20:23]\n"
    "ds_read_b64_tr_b16 a[16:17], v241 offset:45056\n"
    "v_mfma_f32_16x16x32_bf16 v[24:27], a[24:27], a[204:207], v[24:27]\n"
    "ds_read_b64_tr_b16 a[18:19], v241 offset:47104\n"
    "v_mfma_f32_16x16x32_bf16 v[28:31], a[28:31], a[200:203], v[28:31]\n"
    "ds_read_b64_tr_b16 a[20:21], v242 offset:45056\n"
    "v_mfma_f32_16x16x32_bf16 v[32:35], a[28:31], a[204:207], v[32:35]\n"
    "s_waitcnt lgkmcnt(7)\n"
    "ds_read_b64_tr_b16 a[22:23], v242 offset:47104\n"
    "v_mfma_f32_16x16x32_bf16 v[36:39], a[0:3], a[192:195], v[36:39]\n"
    "ds_read_b64_tr_b16 a[24:25], v243 offset:45056\n"
    "v_mfma_f32_16x16x32_bf16 v[40:43], a[0:3], a[196:199], v[40:43]\n"
    "ds_read_b64_tr_b16 a[26:27], v243 offset:47104\n"
    "v_mfma_f32_16x16x32_bf16 v[44:47], a[4:7], a[192:195], v[44:47]\n"
    "ds_read_b64_tr_b16 a[28:29], v244 offset:45056\n"
    "v_mfma_f32_16x16x32_bf16 v[48:51], a[4:7], a[196:199], v[48:51]\n"
    "s_waitcnt lgkmcnt(7)\n"
    "ds_read_b64_tr_b16 a[30:31], v244 offset:47104\n"
    "v_mfma_f32_16x16x32_bf16 v[52:55], a[8:11], a[192:195], v[52:55]\n"
    "s_barrier\n"
    "ds_read_b64_tr_b16 a[0:1], v241 offset:49152\n"
    "v_mfma_f32_16x16x32_bf16 v[56:59], a[8:11], a[196:199], v[56:59]\n"
    "ds_read_b64_tr_b16 a[2:3], v241 offset:51200\n"
    "v_mfma_f32_16x16x32_bf16 v[60:63], a[12:15], a[192:195], v[60:63]\n"
    "ds_read_b64_tr_b16 a[4:5], v242 offset:49152\n"
    "v_mfma_f32_16x16x32_bf16 v[64:67], a[12:15], a[196:199], v[64:67]\n"
    "s_waitcnt lgkmcnt(7)\n"
    "ds_read_b64_tr_b16 a[6:7], v242 offset:51200\n"
    "v_mfma_f32_16x16x32_bf16 v[36:39], a[16:19], a[200:203], v[36:39]\n"
    "ds_read_b64_tr_b16 a[8:9], v243 offset:49152\n"
    "v_mfma_f32_16x16x32_bf16 v[40:43], a[16:19], a[204:207], v[40:43]\n"
    "ds_read_b64_tr_b16 a[10:11], v243 offset:51200\n"
    "v_mfma_f32_16x16x32_bf16 v[44:47], a[20:23], a[200:203], v[44:47]\n"
    "ds_read_b64_tr_b16 a[12:13], v244 offset:49152\n"
    "v_mfma_f32_16x16x32_bf16 v[48:51], a[20:23], a[204:207], v[48:51]\n"
    "s_waitcnt lgkmcnt(7)\n"
    "ds_read_b64_tr_b16 a[14:15], v244 offset:51200\n"
    "v_mfma_f32_16x16x32_bf16 v[52:55], a[24:27], a[200:203], v[52:55]\n"
    "ds_read_b64_tr_b16 a[16:17], v241 offset:53248\n"
    "v_mfma_f32_16x16x32_bf16 v[56:59], a[24:27], a[204:207], v[56:59]\n"
    "ds_read_b64_tr_b16 a[18:19], v241 offset:55296\n"
    "v_mfma_f32_16x16x32_bf16 v[60:63], a[28:31], a[200:203], v[60:63]\n"
    "ds_read_b64_tr_b16 a[20:21], v242 offset:53248\n"
    "v_mfma_f32_16x16x32_bf16 v[64:67], a[28:31], a[204:207], v[64:67]\n"
    "s_waitcnt vmcnt(12) lgkmcnt(7)\n"
    "ds_read_b64_tr_b16 a[22:23], v242 offset:55296\n"
    "v_mfma_f32_16x16x32_bf16 v[4:7], a[0:3], a[208:211], v[4:7]\n"
    "ds_read_b64_tr_b16 a[24:25], v243 offset:53248\n"
    "v_xor_b32 v228, 0, v251\n"
    "v_mfma_f32_16x16x32_bf16 v[8:11], a[0:3], a[212:215], v[8:11]\n"
    "ds_read_b64_tr_b16 a[26:27], v243 offset:55296\n"
    "v_xor_b32 v229, 32, v251\n"
    "v_mfma_f32_16x16x32_bf16 v[12:15], a[4:7], a[208:211], v[12:15]\n"
    "ds_read_b64_tr_b16 a[28:29], v244 offset:53248\n"
    "v_xor_b32 v230, 64, v251\n"
    "v_mfma_f32_16x16x32_bf16 v[16:19], a[4:7], a[212:215], v[16:19]\n"
    "s_waitcnt lgkmcnt(7)\n"
    "ds_read_b64_tr_b16 a[30:31], v244 offset:55296\n"
    "v_xor_b32 v231, 96, v251\n"
    "v_mfma_f32_16x16x32_bf16 v[20:23], a[8:11], a[208:211], v[20:23]\n"
    "s_add_u32 s89, s88, 90112\n"
    "ds_read_b64_tr_b16 a[0:1], v241 offset:57344\n"
    "v_mbcnt_lo_u32_b32 v249, -1, 0\n"
    "v_mfma_f32_16x16x32_bf16 v[24:27], a[8:11], a[212:215], v[24:27]\n"
    "ds_read_b64_tr_b16 a[2:3], v241 offset:59392\n"
    "v_mbcnt_hi_u32_b32 v249, -1, v249\n"
    "v_mfma_f32_16x16x32_bf16 v[28:31], a[12:15], a[208:211], v[28:31]\n"
    "ds_read_b64_tr_b16 a[4:5], v242 offset:57344\n"
    "v_lshl_add_u32 v249, v249, 4, s89\n"
    "v_mfma_f32_16x16x32_bf16 v[32:35], a[12:15], a[212:215], v[32:35]\n"
    "s_waitcnt lgkmcnt(7)\n"
    "ds_read_b64_tr_b16 a[6:7], v242 offset:59392\n"
    "s_lshr_b32 s90, s68, 10\n"
    "v_mfma_f32_16x16x32_bf16 v[4:7], a[16:19], a[216:219], v[4:7]\n"
    "s_lshr_b32 s94, s69, 10\n"
    "ds_read_b64_tr_b16 a[8:9], v243 offset:57344\n"
    "v_mfma_f32_16x16x32_bf16 v[8:11], a[16:19], a[220:223], v[8:11]\n"
    "ds_read_b64_tr_b16 a[10:11], v243 offset:59392\n"
    "v_mfma_f32_16x16x32_bf16 v[12:15], a[20:23], a[216:219], v[12:15]\n"
    "ds_read_b64_tr_b16 a[12:13], v244 offset:57344\n"
    "v_mfma_f32_16x16x32_bf16 v[16:19], a[20:23], a[220:223], v[16:19]\n"
    "s_waitcnt lgkmcnt(7)\n"
    "ds_read_b64_tr_b16 a[14:15], v244 offset:59392\n"
    "v_mfma_f32_16x16x32_bf16 v[20:23], a[24:27], a[216:219], v[20:23]\n"
    "ds_read_b64_tr_b16 a[16:17], v241 offset:61440\n"
    "v_mfma_f32_16x16x32_bf16 v[24:27], a[24:27], a[220:223], v[24:27]\n"
    "ds_read_b64_tr_b16 a[18:19], v241 offset:63488\n"
    "v_mfma_f32_16x16x32_bf16 v[28:31], a[28:31], a[216:219], v[28:31]\n"
    "ds_read_b64_tr_b16 a[20:21], v242 offset:61440\n"
    "v_mfma_f32_16x16x32_bf16 v[32:35], a[28:31], a[220:223], v[32:35]\n"
    "s_waitcnt vmcnt(8) lgkmcnt(7)\n"
    "ds_read_b64_tr_b16 a[22:23], v242 offset:63488\n"
    "v_mfma_f32_16x16x32_bf16 v[4:7], a[0:3], a[224:227], v[4:7]\n"
    "ds_read_b64_tr_b16 a[24:25], v243 offset:61440\n"
    "v_mfma_f32_16x16x32_bf16 v[8:11], a[0:3], a[228:231], v[8:11]\n"
    "ds_read_b64_tr_b16 a[26:27], v243 offset:63488\n"
    "v_mfma_f32_16x16x32_bf16 v[12:15], a[4:7], a[224:227], v[12:15]\n"
    "ds_read_b64_tr_b16 a[28:29], v244 offset:61440\n"
    "v_mfma_f32_16x16x32_bf16 v[16:19], a[4:7], a[228:231], v[16:19]\n"
    "s_waitcnt lgkmcnt(7)\n"
    "ds_read_b64_tr_b16 a[30:31], v244 offset:63488\n"
    "v_mfma_f32_16x16x32_bf16 v[20:23], a[8:11], a[224:227], v[20:23]\n"
    "s_barrier\n"
    "ds_read_b64_tr_b16 a[0:1], v241 offset:0\n"
    "v_mfma_f32_16x16x32_bf16 v[24:27], a[8:11], a[228:231], v[24:27]\n"
    "ds_read_b64_tr_b16 a[2:3], v241 offset:2048\n"
    "v_mfma_f32_16x16x32_bf16 v[28:31], a[12:15], a[224:227], v[28:31]\n"
    "ds_read_b64_tr_b16 a[4:5], v242 offset:0\n"
    "v_mfma_f32_16x16x32_bf16 v[32:35], a[12:15], a[228:231], v[32:35]\n"
    "s_waitcnt lgkmcnt(7)\n"
    "ds_read_b64_tr_b16 a[6:7], v242 offset:2048\n"
    "v_mfma_f32_16x16x32_bf16 v[4:7], a[16:19], a[232:235], v[4:7]\n"
    "ds_read_b64_tr_b16 a[8:9], v243 offset:0\n"
    "v_mfma_f32_16x16x32_bf16 v[8:11], a[16:19], a[236:239], v[8:11]\n"
    "ds_read_b64_tr_b16 a[10:11], v243 offset:2048\n"
    "v_mfma_f32_16x16x32_bf16 v[12:15], a[20:23], a[232:235], v[12:15]\n"
    "ds_read_b64_tr_b16 a[12:13], v244 offset:0\n"
    "v_mfma_f32_16x16x32_bf16 v[16:19], a[20:23], a[236:239], v[16:19]\n"
    "s_waitcnt lgkmcnt(7)\n"
    "ds_read_b64_tr_b16 a[14:15], v244 offset:2048\n"
    "v_mfma_f32_16x16x32_bf16 v[20:23], a[24:27], a[232:235], v[20:23]\n"
    "ds_read_b64_tr_b16 a[16:17], v241 offset:4096\n"
    "v_mfma_f32_16x16x32_bf16 v[24:27], a[24:27], a[236:239], v[24:27]\n"
    "ds_read_b64_tr_b16 a[18:19], v241 offset:6144\n"
    "v_mfma_f32_16x16x32_bf16 v[28:31], a[28:31], a[232:235], v[28:31]\n"
    "ds_read_b64_tr_b16 a[20:21], v242 offset:4096\n"
    "v_mfma_f32_16x16x32_bf16 v[32:35], a[28:31], a[236:239], v[32:35]\n"
    "s_waitcnt vmcnt(0) lgkmcnt(7)\n"
    "ds_read_b64_tr_b16 a[22:23], v242 offset:6144\n"
    "v_mfma_f32_16x16x32_bf16 v[4:7], a[0:3], a[240:243], v[4:7]\n"
    "ds_read_b64_tr_b16 a[24:25], v243 offset:4096\n"
    "v_mfma_f32_16x16x32_bf16 v[8:11], a[0:3], a[244:247], v[8:11]\n"
    "ds_read_b64_tr_b16 a[26:27], v243 offset:6144\n"
    "v_mfma_f32_16x16x32_bf16 v[12:15], a[4:7], a[240:243], v[12:15]\n"
    "ds_read_b64_tr_b16 a[28:29], v244 offset:4096\n"
    "v_mfma_f32_16x16x32_bf16 v[16:19], a[4:7], a[244:247], v[16:19]\n"
    "s_waitcnt lgkmcnt(7)\n"
    "ds_read_b64_tr_b16 a[30:31], v244 offset:6144\n"
    "v_mfma_f32_16x16x32_bf16 v[20:23], a[8:11], a[240:243], v[20:23]\n"
    "ds_read_b64_tr_b16 a[0:1], v245 offset:0\n"
    "v_mfma_f32_16x16x32_bf16 v[24:27], a[8:11], a[244:247], v[24:27]\n"
    "ds_read_b64_tr_b16 a[2:3], v245 offset:2048\n"
    "v_mfma_f32_16x16x32_bf16 v[28:31], a[12:15], a[240:243], v[28:31]\n"
    "ds_read_b64_tr_b16 a[4:5], v246 offset:0\n"
    "v_mfma_f32_16x16x32_bf16 v[32:35], a[12:15], a[244:247], v[32:35]\n"
    "s_waitcnt lgkmcnt(7)\n"
    "ds_read_b64_tr_b16 a[6:7], v246 offset:2048\n"
    "v_mfma_f32_16x16x32_bf16 v[4:7], a[16:19], a[248:251], v[4:7]\n"
    "ds_read_b64_tr_b16 a[8:9], v247 offset:0\n"
    "v_mfma_f32_16x16x32_bf16 v[8:11], a[16:19], a[252:255], v[8:11]\n"
    "ds_read_b64_tr_b16 a[10:11], v247 offset:2048\n"
    "v_mfma_f32_16x16x32_bf16 v[12:15], a[20:23], a[248:251], v[12:15]\n"
    "ds_read_b64_tr_b16 a[12:13], v248 offset:0\n"
    "v_mfma_f32_16x16x32_bf16 v[16:19], a[20:23], a[252:255], v[16:19]\n"
    "s_waitcnt lgkmcnt(7)\n"
    "ds_read_b64_tr_b16 a[14:15], v248 offset:2048\n"
    "v_fma_f32 v132, v68, v4, v132\n"
    "v_fma_f32 v133, v69, v5, v133\n"
    "v_mfma_f32_16x16x32_bf16 v[20:23], a[24:27], a[248:251], v[20:23]\n"
    "ds_read_b64_tr_b16 a[16:17], v245 offset:4096\n"
    "v_fma_f32 v134, v70, v6, v134\n"
    "v_fma_f32 v135, v71, v7, v135\n"
    "v_mfma_f32_16x16x32_bf16 v[24:27], a[24:27], a[252:255], v[24:27]\n"
    "ds_read_b64_tr_b16 a[18:19], v245 offset:6144\n"
    "v_cvt_pk_bf16_f32 v196, v132, v133\n"
    "v_cvt_pk_bf16_f32 v197, v134, v135\n"
    "v_mfma_f32_16x16x32_bf16 v[28:31], a[28:31], a[248:251], v[28:31]\n"
    "ds_read_b64_tr_b16 a[20:21], v246 offset:4096\n"
    "ds_write_b64 v228, v[196:197] offset:0\n"
    "v_fma_f32 v140, v76, v12, v140\n"
    "v_mfma_f32_16x16x32_bf16 v[32:35], a[28:31], a[252:255], v[32:35]\n"
    "s_waitcnt lgkmcnt(8)\n"
    "ds_read_b64_tr_b16 a[22:23], v246 offset:6144\n"
    "v_fma_f32 v141, v77, v13, v141\n"
    "v_fma_f32 v142, v78, v14, v142\n"
    "v_mfma_f32_16x16x32_bf16 v[36:39], a[0:3], a[208:211], v[36:39]\n"
    "ds_read_b64_tr_b16 a[24:25], v247 offset:4096\n"
    "v_fma_f32 v143, v79, v15, v143\n"
    "v_cvt_pk_bf16_f32 v198, v140, v141\n"
    "v_mfma_f32_16x16x32_bf16 v[40:43], a[0:3], a[212:215], v[40:43]\n"
    "ds_read_b64_tr_b16 a[26:27], v247 offset:6144\n"
    "v_cvt_pk_bf16_f32 v199, v142, v143\n"
    "ds_write_b64 v229, v[198:199] offset:0\n"
    "v_mfma_f32_16x16x32_bf16 v[44:47], a[4:7], a[208:211], v[44:47]\n"
    "ds_read_b64_tr_b16 a[28:29], v248 offset:4096\n"
    "v_fma_f32 v148, v84, v20, v148\n"
    "v_fma_f32 v149, v85, v21, v149\n"
    "v_mfma_f32_16x16x32_bf16 v[48:51], a[4:7], a[212:215], v[48:51]\n"
    "s_waitcnt lgkmcnt(9)\n"
    "ds_read_b64_tr_b16 a[30:31], v248 offset:6144\n"
    "v_fma_f32 v150, v86, v22, v150\n"
    "v_fma_f32 v151, v87, v23, v151\n"
    "v_mfma_f32_16x16x32_bf16 v[52:55], a[8:11], a[208:211], v[52:55]\n"
    "s_barrier\n"
    "ds_read_b64_tr_b16 a[0:1], v237 offset:0\n"
    "v_cvt_pk_bf16_f32 v200, v148, v149\n"
    "v_cvt_pk_bf16_f32 v201, v150, v151\n"
    "v_mfma_f32_16x16x32_bf16 v[56:59], a[8:11], a[212:215], v[56:59]\n"
    "ds_read_b64_tr_b16 a[2:3], v237 offset:2048\n"
    "ds_write_b64 v230, v[200:201] offset:0\n"
    "v_fma_f32 v156, v92, v28, v156\n"
    "v_mfma_f32_16x16x32_bf16 v[60:63], a[12:15], a[208:211], v[60:63]\n"
    "ds_read_b64_tr_b16 a[4:5], v238 offset:0\n"
    "v_fma_f32 v157, v93, v29, v157\n"
    "v_fma_f32 v158, v94, v30, v158\n"
    "v_mfma_f32_16x16x32_bf16 v[64:67], a[12:15], a[212:215], v[64:67]\n"
    "s_waitcnt lgkmcnt(9)\n"
    "ds_read_b64_tr_b16 a[6:7], v238 offset:2048\n"
    "v_fma_f32 v159, v95, v31, v159\n"
    "v_cvt_pk_bf16_f32 v202, v156, v157\n"
    "v_mfma_f32_16x16x32_bf16 v[36:39], a[16:19], a[216:219], v[36:39]\n"
    "ds_read_b64_tr_b16 a[8:9], v239 offset:0\n"
    "v_cvt_pk_bf16_f32 v203, v158, v159\n"
    "ds_write_b64 v231, v[202:203] offset:0\n"
    "v_mfma_f32_16x16x32_bf16 v[40:43], a[16:19], a[220:223], v[40:43]\n"
    "ds_read_b64_tr_b16 a[10:11], v239 offset:2048\n"
    "ds_read_b128 v[68:71], v249 offset:0\n"
    "ds_read_b128 v[76:79], v249 offset:1024\n"
    "v_mfma_f32_16x16x32_bf16 v[44:47], a[20:23], a[216:219], v[44:47]\n"
    "ds_read_b64_tr_b16 a[12:13], v240 offset:0\n"
    "v_fma_f32 v136, v72, v8, v136\n"
    "v_fma_f32 v137, v73, v9, v137\n"
    "v_mfma_f32_16x16x32_bf16 v[48:51], a[20:23], a[220:223], v[48:51]\n"
    "s_waitcnt lgkmcnt(11)\n"
    "ds_read_b64_tr_b16 a[14:15], v240 offset:2048\n"
    "v_fma_f32 v138, v74, v10, v138\n"
    "v_fma_f32 v139, v75, v11, v139\n"
    "v_mfma_f32_16x16x32_bf16 v[52:55], a[24:27], a[216:219], v[52:55]\n"
    "ds_read_b64_tr_b16 a[16:17], v237 offset:4096\n"
    "v_cvt_pk_bf16_f32 v204, v136, v137\n"
    "v_cvt_pk_bf16_f32 v205, v138, v139\n"
    "v_mfma_f32_16x16x32_bf16 v[56:59], a[24:27], a[220:223], v[56:59]\n"
    "ds_read_b64_tr_b16 a[18:19], v237 offset:6144\n"
    "ds_write_b64 v228, v[204:205] offset:2048\n"
    "v_fma_f32 v144, v80, v16, v144\n"
    "v_mfma_f32_16x16x32_bf16 v[60:63], a[28:31], a[216:219], v[60:63]\n"
    "ds_read_b64_tr_b16 a[20:21], v238 offset:4096\n"
    "v_fma_f32 v145, v81, v17, v145\n"
    "v_fma_f32 v146, v82, v18, v146\n"
    "v_mfma_f32_16x16x32_bf16 v[64:67], a[28:31], a[220:223], v[64:67]\n"
    "s_waitcnt lgkmcnt(11)\n"
    "ds_read_b64_tr_b16 a[22:23], v238 offset:6144\n"
    "v_fma_f32 v147, v83, v19, v147\n"
    "v_cvt_pk_bf16_f32 v206, v144, v145\n"
    "v_mfma_f32_16x16x32_bf16 v[36:39], a[0:3], a[224:227], v[36:39]\n"
    "ds_read_b64_tr_b16 a[24:25], v239 offset:4096\n"
    "v_cvt_pk_bf16_f32 v207, v146, v147\n"
    "ds_write_b64 v229, v[206:207] offset:2048\n"
    "v_mfma_f32_16x16x32_bf16 v[40:43], a[0:3], a[228:231], v[40:43]\n"
    "ds_read_b64_tr_b16 a[26:27], v239 offset:6144\n"
    "v_fma_f32 v152, v88, v24, v152\n"
    "v_fma_f32 v153, v89, v25, v153\n"
    "v_mfma_f32_16x16x32_bf16 v[44:47], a[4:7], a[224:227], v[44:47]\n"
    "ds_read_b64_tr_b16 a[28:29], v240 offset:4096\n"
    "v_fma_f32 v154, v90, v26, v154\n"
    "v_fma_f32 v155, v91, v27, v155\n"
    "v_mfma_f32_16x16x32_bf16 v[48:51], a[4:7], a[228:231], v[48:51]\n"
    "s_waitcnt lgkmcnt(9)\n"
    "ds_read_b64_tr_b16 a[30:31], v240 offset:6144\n"
    "v_cvt_pk_bf16_f32 v208, v152, v153\n"
    "v_cvt_pk_bf16_f32 v209, v154, v155\n"
    "v_mfma_f32_16x16x32_bf16 v[52:55], a[8:11], a[224:227], v[52:55]\n"
    "ds_read_b64_tr_b16 a[32:33], v245 offset:8192\n"
    "ds_read_b64_tr_b16 a[34:35], v245 offset:10240\n"
    "ds_write_b64 v230, v[208:209] offset:2048\n"
    "v_mfma_f32_16x16x32_bf16 v[56:59], a[8:11], a[228:231], v[56:59]\n"
    "ds_read_b64_tr_b16 a[48:49], v245 offset:12288\n"
    "ds_read_b64_tr_b16 a[50:51], v245 offset:14336\n"
    "buffer_store_dwordx4 v[68:71], v233, s[60:63], s90 offen offset:0 sc1\n"
    "v_mfma_f32_16x16x32_bf16 v[60:63], a[12:15], a[224:227], v[60:63]\n"
    "ds_read_b64_tr_b16 a[36:37], v246 offset:8192\n"
    "ds_read_b64_tr_b16 a[38:39], v246 offset:10240\n"
    "v_fma_f32 v160, v96, v32, v160\n"
    "v_mfma_f32_16x16x32_bf16 v[64:67], a[12:15], a[228:231], v[64:67]\n"
    "s_waitcnt lgkmcnt(12)\n"
    "ds_read_b64_tr_b16 a[52:53], v246 offset:12288\n"
    "ds_read_b64_tr_b16 a[54:55], v246 offset:14336\n"
    "v_fma_f32 v161, v97, v33, v161\n"
    "v_mfma_f32_16x16x32_bf16 v[36:39], a[16:19], a[232:235], v[36:39]\n"
    "ds_read_b64_tr_b16 a[40:41], v247 offset:8192\n"
    "ds_read_b64_tr_b16 a[42:43], v247 offset:10240\n"
    "v_fma_f32 v162, v98, v34, v162\n"
    "v_mfma_f32_16x16x32_bf16 v[40:43], a[16:19], a[236:239], v[40:43]\n"
    "ds_read_b64_tr_b16 a[56:57], v247 offset:12288\n"
    "ds_read_b64_tr_b16 a[58:59], v247 offset:14336\n"
    "v_fma_f32 v163, v99, v35, v163\n"
    "v_mfma_f32_16x16x32_bf16 v[44:47], a[20:23], a[232:235], v[44:47]\n"
    "ds_read_b64_tr_b16 a[44:45], v248 offset:8192\n"
    "ds_read_b64_tr_b16 a[46:47], v248 offset:10240\n"
    "v_cvt_pk_bf16_f32 v210, v160, v161\n"
    "v_mfma_f32_16x16x32_bf16 v[48:51], a[20:23], a[236:239], v[48:51]\n"
    "s_waitcnt lgkmcnt(15)\n"
    "ds_read_b64_tr_b16 a[60:61], v248 offset:12288\n"
    "ds_read_b64_tr_b16 a[62:63], v248 offset:14336\n"
    "v_cvt_pk_bf16_f32 v211, v162, v163\n"
    "v_mfma_f32_16x16x32_bf16 v[52:55], a[24:27], a[232:235], v[52:55]\n"
    "ds_write_b64 v231, v[210:211] offset:2048\n"
    "buffer_store_dwordx4 v[76:79], v234, s[60:63], s90 offen offset:1024 sc1\n"
    "v_mfma_f32_16x16x32_bf16 v[56:59], a[24:27], a[236:239], v[56:59]\n"
    "ds_read_b128 v[72:75], v249 offset:2048\n"
    "ds_read_b128 v[80:83], v249 offset:3072\n"
    "v_mfma_f32_16x16x32_bf16 v[60:63], a[28:31], a[232:235], v[60:63]\n"
    "v_mfma_f32_16x16x32_bf16 v[64:67], a[28:31], a[236:239], v[64:67]\n"
    "s_waitcnt lgkmcnt(11)\n"
    "v_mfma_f32_16x16x32_bf16 v[36:39], a[32:35], a[240:243], v[36:39]\n"
    "v_mfma_f32_16x16x32_bf16 v[36:39], a[48:51], a[248:251], v[36:39]\n"
    "v_mfma_f32_16x16x32_bf16 v[44:47], a[36:39], a[240:243], v[44:47]\n"
    "v_mfma_f32_16x16x32_bf16 v[44:47], a[52:55], a[248:251], v[44:47]\n"
    "s_waitcnt lgkmcnt(1)\n"
    "v_mfma_f32_16x16x32_bf16 v[52:55], a[40:43], a[240:243], v[52:55]\n"
    "s_nop 3\n"
    "v_pk_fma_f32 v[164:165], v[100:101], v[36:37], v[164:165]\n"
    "v_pk_fma_f32 v[166:167], v[102:103], v[38:39], v[166:167]\n"
    "v_cvt_pk_bf16_f32 v212, v164, v165\n"
    "v_cvt_pk_bf16_f32 v213, v166, v167\n"
    "v_mfma_f32_16x16x32_bf16 v[52:55], a[56:59], a[248:251], v[52:55]\n"
    "buffer_store_dwordx4 v[72:75], v235, s[60:63], s90 offen offset:2048 sc1\n"
    "ds_write_b64 v228, v[212:213] offset:16384\n"
    "v_pk_fma_f32 v[172:173], v[108:109], v[44:45], v[172:173]\n"
    "v_pk_fma_f32 v[174:175], v[110:111], v[46:47], v[174:175]\n"
    "v_mfma_f32_16x16x32_bf16 v[60:63], a[44:47], a[240:243], v[60:63]\n"
    "v_cvt_pk_bf16_f32 v214, v172, v173\n"
    "v_cvt_pk_bf16_f32 v215, v174, v175\n"
    "ds_write_b64 v229, v[214:215] offset:16384\n"
    "v_mfma_f32_16x16x32_bf16 v[60:63], a[60:63], a[248:251], v[60:63]\n"
    "s_waitcnt lgkmcnt(2)\n"
    "v_pk_fma_f32 v[180:181], v[116:117], v[52:53], v[180:181]\n"
    "v_pk_fma_f32 v[182:183], v[118:119], v[54:55], v[182:183]\n"
    "v_cvt_pk_bf16_f32 v216, v180, v181\n"
    "v_cvt_pk_bf16_f32 v217, v182, v183\n"
    "v_mfma_f32_16x16x32_bf16 v[40:43], a[32:35], a[244:247], v[40:43]\n"
    "L_ip9_%=:\n"
    "s_cbranch_execz L_ipback_%=\n"
    "buffer_store_dwordx4 v[80:83], v236, s[60:63], s90 offen offset:3072 sc1\n"
    "ds_write_b64 v230, v[216:217] offset:16384\n"
    "v_mfma_f32_16x16x32_bf16 v[40:43], a[48:51], a[252:255], v[40:43]\n"
    "v_pk_fma_f32 v[188:189], v[124:125], v[60:61], v[188:189]\n"
    "v_pk_fma_f32 v[190:191], v[126:127], v[62:63], v[190:191]\n"
    "v_cvt_pk_bf16_f32 v218, v188, v189\n"
    "v_cvt_pk_bf16_f32 v219, v190, v191\n"
    "v_mfma_f32_16x16x32_bf16 v[48:51], a[36:39], a[244:247], v[48:51]\n"
    "ds_write_b64 v231, v[218:219] offset:16384\n"
    "ds_read_b128 v[100:103], v249 offset:16384\n"
    "ds_read_b128 v[108:111], v249 offset:17408\n"
    "v_mfma_f32_16x16x32_bf16 v[48:51], a[52:55], a[252:255], v[48:51]\n"
    "v_pk_fma_f32 v[168:169], v[104:105], v[40:41], v[168:169]\n"
    "v_pk_fma_f32 v[170:171], v[106:107], v[42:43], v[170:171]\n"
    "v_cvt_pk_bf16_f32 v220, v168, v169\n"
    "v_cvt_pk_bf16_f32 v221, v170, v171\n"
    "v_mfma_f32_16x16x32_bf16 v[56:59], a[40:43], a[244:247], v[56:59]\n"
    "ds_write_b64 v228, v[220:221] offset:18432\n"
    "v_mfma_f32_16x16x32_bf16 v[56:59], a[56:59], a[252:255], v[56:59]\n"
    "s_nop 0\n"
    "v_pk_fma_f32 v[176:177], v[112:113], v[48:49], v[176:177]\n"
    "v_pk_fma_f32 v[178:179], v[114:115], v[50:51], v[178:179]\n"
    "v_cvt_pk_bf16_f32 v222, v176, v177\n"
    "v_cvt_pk_bf16_f32 v223, v178, v179\n"
    "v_mfma_f32_16x16x32_bf16 v[64:67], a[44:47], a[244:247], v[64:67]\n"
    "ds_write_b64 v229, v[222:223] offset:18432\n"
    "s_waitcnt lgkmcnt(3)\n"
    "buffer_store_dwordx4 v[100:103], v233, s[60:63], s94 offen offset:0 sc1\n"
    "v_mfma_f32_16x16x32_bf16 v[64:67], a[60:63], a[252:255], v[64:67]\n"
    "v_pk_fma_f32 v[184:185], v[120:121], v[56:57], v[184:185]\n"
    "v_pk_fma_f32 v[186:187], v[122:123], v[58:59], v[186:187]\n"
    "v_cvt_pk_bf16_f32 v224, v184, v185\n"
    "v_cvt_pk_bf16_f32 v225, v186, v187\n"
    "ds_write_b64 v230, v[224:225] offset:18432\n"
    "s_waitcnt lgkmcnt(3)\n"
    "buffer_store_dwordx4 v[108:111], v234, s[60:63], s94 offen offset:1024 sc1\n"
    "s_nop 0\n"
    "v_pk_fma_f32 v[192:193], v[128:129], v[64:65], v[192:193]\n"
    "v_pk_fma_f32 v[194:195], v[130:131], v[66:67], v[194:195]\n"
    "v_cvt_pk_bf16_f32 v226, v192, v193\n"
    "v_cvt_pk_bf16_f32 v227, v194, v195\n"
    "ds_write_b64 v231, v[226:227] offset:18432\n"
    "ds_read_b128 v[104:107], v249 offset:18432\n"
    "ds_read_b128 v[112:115], v249 offset:19456\n"
    "s_waitcnt lgkmcnt(1)\n"
    "buffer_store_dwordx4 v[104:107], v235, s[60:63], s94 offen offset:2048 sc1\n"
    "s_waitcnt lgkmcnt(0)\n"
    "buffer_store_dwordx4 v[112:115], v236, s[60:63], s94 offen offset:3072 sc1\n"
    "s_waitcnt vmcnt(0) lgkmcnt(0)\n"
    "s_branch L_end_%=\n"
    "L_sp0_0_%=:\n"
    "s_mov_b32 s93, 0\n"
    "L_lp0_0_%=:\n"
    "s_buffer_load_dword s4, s[64:67], s91 offset:0 glc\n"
    "s_add_u32 s93, s93, 1\n"
    "s_cmp_gt_u32 s93, 64\n"
    "s_cbranch_scc1 L_vp0_0_%=\n"
    "s_waitcnt lgkmcnt(0)\n"
    "s_xor_b32 s4, s4, s26\n"
    "s_and_b32 s4, s4, 0x30300\n"
    "s_cbranch_scc1 L_lp0_0_%=\n"
    "s_branch L_bp0_0_%=\n"
    "L_vp0_0_%=:\n"
    "buffer_load_ubyte v1, v252, s[64:67], s91 offen sc1\n"
    "buffer_load_ubyte v249, v252, s[64:67], s91 offen offset:32 sc1\n"
    "s_add_u32 s93, s93, 1\n"
    "s_cmp_gt_u32 s93, 100000\n"
    "s_waitcnt vmcnt(0) lgkmcnt(0)\n"
    "s_cbranch_scc1 L_bp0_0_%=\n"
    "v_and_b32 v249, 1, v249\n"
    "v_and_or_b32 v255, v255, 2, v249\n"
    "buffer_store_byte v255, v253, s[64:67], s92 offen sc1\n"
    "v_lshlrev_b32 v249, 1, v249\n"
    "v_xor_b32 v1, v1, v249\n"
    "v_lshrrev_b32 v249, 1, v255\n"
    "v_xor_b32 v1, v1, v249\n"
    "v_and_b32 v1, 3, v1\n"
    "v_subrev_u32 v249, 1, v252\n"
    "v_cmp_gt_u32 vcc, 2, v249\n"
    "s_nop 1\n"
    "v_cndmask_b32 v1, 0, v1, vcc\n"
    "v_cmp_ne_u32 vcc, 0, v1\n"
    "s_nop 4\n"
    "s_and_b32 s4, vcc_lo, 0xff\n"
    "s_cbranch_scc1 L_vp0_0_%=\n"
    "s_branch L_bp0_0_%=\n"
    "L_sp0_1_%=:\n"
    "s_mov_b32 s93, 0\n"
    "L_lp0_1_%=:\n"
    "s_buffer_load_dword s6, s[64:67], s91 offset:0 glc\n"
    "s_add_u32 s93, s93, 1\n"
    "s_cmp_gt_u32 s93, 64\n"
    "s_cbranch_scc1 L_vp0_1_%=\n"
    "s_waitcnt lgkmcnt(0)\n"
    "s_xor_b32 s6, s6, s26\n"
    "s_and_b32 s6, s6, 0x3000000\n"
    "s_cbranch_scc1 L_lp0_1_%=\n"
    "s_branch L_bp0_1_%=\n"
    "L_vp0_1_%=:\n"
    "buffer_load_ubyte v1, v252, s[64:67], s91 offen sc1\n"
    "buffer_load_ubyte v249, v252, s[64:67], s91 offen offset:32 sc1\n"
    "s_add_u32 s93, s93, 1\n"
    "s_cmp_gt_u32 s93, 100000\n"
    "s_waitcnt vmcnt(0) lgkmcnt(0)\n"
    "s_cbranch_scc1 L_bp0_1_%=\n"
    "v_and_b32 v249, 1, v249\n"
    "v_and_or_b32 v255, v255, 2, v249\n"
    "buffer_store_byte v255, v253, s[64:67], s92 offen sc1\n"
    "v_lshlrev_b32 v249, 1, v249\n"
    "v_xor_b32 v1, v1, v249\n"
    "v_lshrrev_b32 v249, 1, v255\n"
    "v_xor_b32 v1, v1, v249\n"
    "v_and_b32 v1, 3, v1\n"
    "v_subrev_u32 v249, 3, v252\n"
    "v_cmp_gt_u32 vcc, 1, v249\n"
    "s_nop 1\n"
    "v_cndmask_b32 v1, 0, v1, vcc\n"
    "v_cmp_ne_u32 vcc, 0, v1\n"
    "s_nop 4\n"
    "s_and_b32 s6, vcc_lo, 0xff\n"
    "s_cbranch_scc1 L_vp0_1_%=\n"
    "s_branch L_bp0_1_%=\n"
    "L_sp0_2_%=:\n"
    "s_mov_b32 s93, 0\n"
    "L_lp0_2_%=:\n"
    "s_buffer_load_dword s8, s[64:67], s91 offset:4 glc\n"
    "s_add_u32 s93, s93, 1\n"
    "s_cmp_gt_u32 s93, 64\n"
    "s_cbranch_scc1 L_vp0_2_%=\n"
    "s_waitcnt lgkmcnt(0)\n"
    "s_xor_b32 s8, s8, s27\n"
    "s_and_b32 s8, s8, 0x3030303\n"
    "s_cbranch_scc1 L_lp0_2_%=\n"
    "s_branch L_bp0_2_%=\n"
    "L_vp0_2_%=:\n"
    "buffer_load_ubyte v1, v252, s[64:67], s91 offen sc1\n"
    "buffer_load_ubyte v249, v252, s[64:67], s91 offen offset:32 sc1\n"
    "s_add_u32 s93, s93, 1\n"
    "s_cmp_gt_u32 s93, 100000\n"
    "s_waitcnt vmcnt(0) lgkmcnt(0)\n"
    "s_cbranch_scc1 L_bp0_2_%=\n"
    "v_and_b32 v249, 1, v249\n"
    "v_and_or_b32 v255, v255, 2, v249\n"
    "buffer_store_byte v255, v253, s[64:67], s92 offen sc1\n"
    "v_lshlrev_b32 v249, 1, v249\n"
    "v_xor_b32 v1, v1, v249\n"
    "v_lshrrev_b32 v249, 1, v255\n"
    "v_xor_b32 v1, v1, v249\n"
    "v_and_b32 v1, 3, v1\n"
    "v_subrev_u32 v249, 4, v252\n"
    "v_cmp_gt_u32 vcc, 4, v249\n"
    "s_nop 1\n"
    "v_cndmask_b32 v1, 0, v1, vcc\n"
    "v_cmp_ne_u32 vcc, 0, v1\n"
    "s_nop 4\n"
    "s_and_b32 s8, vcc_lo, 0xff\n"
    "s_cbranch_scc1 L_vp0_2_%=\n"
    "s_branch L_bp0_2_%=\n"
    "L_sp1_0_%=:\n"
    "s_mov_b32 s93, 0\n"
    "L_lp1_0_%=:\n"
    "s_buffer_load_dword s10, s[64:67], s91 offset:8 glc\n"
    "s_add_u32 s93, s93, 1\n"
    "s_cmp_gt_u32 s93, 64\n"
    "s_cbranch_scc1 L_vp1_0_%=\n"
    "s_waitcnt lgkmcnt(0)\n"
    "s_andn2_b32 s10, 0x100, s10\n"
    "s_cbranch_scc1 L_lp1_0_%=\n"
    "s_branch L_bp1_0_%=\n"
    "L_vp1_0_%=:\n"
    "buffer_load_dword v1, off, s[64:67], s91 offset:8 sc1\n"
    "s_add_u32 s93, s93, 1\n"
    "s_cmp_gt_u32 s93, 100000\n"
    "s_cbranch_scc1 L_bp1_0_%=\n"
    "s_waitcnt vmcnt(0) lgkmcnt(0)\n"
    "v_readfirstlane_b32 s10, v1\n"
    "s_nop 0\n"
    "s_andn2_b32 s10, 0x100, s10\n"
    "s_cbranch_scc1 L_vp1_0_%=\n"
    "s_branch L_bp1_0_%=\n"
    "L_sp1_1_%=:\n"
    "s_mov_b32 s93, 0\n"
    "L_lp1_1_%=:\n"
    "s_buffer_load_dword s12, s[64:67], s91 offset:8 glc\n"
    "s_add_u32 s93, s93, 1\n"
    "s_cmp_gt_u32 s93, 64\n"
    "s_cbranch_scc1 L_vp1_1_%=\n"
    "s_waitcnt lgkmcnt(0)\n"
    "s_andn2_b32 s12, 0x1010000, s12\n"
    "s_cbranch_scc1 L_lp1_1_%=\n"
    "s_branch L_bp1_1_%=\n"
    "L_vp1_1_%=:\n"
    "buffer_load_dword v1, off, s[64:67], s91 offset:8 sc1\n"
    "s_add_u32 s93, s93, 1\n"
    "s_cmp_gt_u32 s93, 100000\n"
    "s_cbranch_scc1 L_bp1_1_%=\n"
    "s_waitcnt vmcnt(0) lgkmcnt(0)\n"
    "v_readfirstlane_b32 s12, v1\n"
    "s_nop 0\n"
    "s_andn2_b32 s12, 0x1010000, s12\n"
    "s_cbranch_scc1 L_vp1_1_%=\n"
    "s_branch L_bp1_1_%=\n"
    "L_sp1_2_%=:\n"
    "s_mov_b32 s93, 0\n"
    "L_lp1_2_%=:\n"
    "s_buffer_load_dword s14, s[64:67], s91 offset:12 glc\n"
    "s_add_u32 s93, s93, 1\n"
    "s_cmp_gt_u32 s93, 64\n"
    "s_cbranch_scc1 L_vp1_2_%=\n"
    "s_waitcnt lgkmcnt(0)\n"
    "s_andn2_b32 s14, 0x1010101, s14\n"
    "s_cbranch_scc1 L_lp1_2_%=\n"
    "s_branch L_bp1_2_%=\n"
    "L_vp1_2_%=:\n"
    "buffer_load_dword v1, off, s[64:67], s91 offset:12 sc1\n"
    "s_add_u32 s93, s93, 1\n"
    "s_cmp_gt_u32 s93, 100000\n"
    "s_cbranch_scc1 L_bp1_2_%=\n"
    "s_waitcnt vmcnt(0) lgkmcnt(0)\n"
    "v_readfirstlane_b32 s14, v1\n"
    "s_nop 0\n"
    "s_andn2_b32 s14, 0x1010101, s14\n"
    "s_cbranch_scc1 L_vp1_2_%=\n"
    "s_branch L_bp1_2_%=\n"
    "L_sp2_0_%=:\n"
    "s_mov_b32 s93, 0\n"
    "L_lp2_0_%=:\n"
    "s_buffer_load_dword s16, s[64:67], s91 offset:16 glc\n"
    "s_add_u32 s93, s93, 1\n"
    "s_cmp_gt_u32 s93, 64\n"
    "s_cbranch_scc1 L_vp2_0_%=\n"
    "s_waitcnt lgkmcnt(0)\n"
    "s_andn2_b32 s16, 0x10100, s16\n"
    "s_cbranch_scc1 L_lp2_0_%=\n"
    "s_branch L_bp2_0_%=\n"
    "L_vp2_0_%=:\n"
    "buffer_load_dword v1, off, s[64:67], s91 offset:16 sc1\n"
    "s_add_u32 s93, s93, 1\n"
    "s_cmp_gt_u32 s93, 100000\n"
    "s_cbranch_scc1 L_bp2_0_%=\n"
    "s_waitcnt vmcnt(0) lgkmcnt(0)\n"
    "v_readfirstlane_b32 s16, v1\n"
    "s_nop 0\n"
    "s_andn2_b32 s16, 0x10100, s16\n"
    "s_cbranch_scc1 L_vp2_0_%=\n"
    "s_branch L_bp2_0_%=\n"
    "L_sp2_1_%=:\n"
    "s_mov_b32 s93, 0\n"
    "L_lp2_1_%=:\n"
    "s_buffer_load_dword s18, s[64:67], s91 offset:16 glc\n"
    "s_add_u32 s93, s93, 1\n"
    "s_cmp_gt_u32 s93, 64\n"
    "s_cbranch_scc1 L_vp2_1_%=\n"
    "s_waitcnt lgkmcnt(0)\n"
    "s_andn2_b32 s18, 0x1000000, s18\n"
    "s_cbranch_scc1 L_lp2_1_%=\n"
    "s_branch L_bp2_1_%=\n"
    "L_vp2_1_%=:\n"
    "buffer_load_dword v1, off, s[64:67], s91 offset:16 sc1\n"
    "s_add_u32 s93, s93, 1\n"
    "s_cmp_gt_u32 s93, 100000\n"
    "s_cbranch_scc1 L_bp2_1_%=\n"
    "s_waitcnt vmcnt(0) lgkmcnt(0)\n"
    "v_readfirstlane_b32 s18, v1\n"
    "s_nop 0\n"
    "s_andn2_b32 s18, 0x1000000, s18\n"
    "s_cbranch_scc1 L_vp2_1_%=\n"
    "s_branch L_bp2_1_%=\n"
    "L_sp2_2_%=:\n"
    "s_mov_b32 s93, 0\n"
    "L_lp2_2_%=:\n"
    "s_buffer_load_dword s20, s[64:67], s91 offset:20 glc\n"
    "s_add_u32 s93, s93, 1\n"
    "s_cmp_gt_u32 s93, 64\n"
    "s_cbranch_scc1 L_vp2_2_%=\n"
    "s_waitcnt lgkmcnt(0)\n"
    "s_andn2_b32 s20, 0x1010101, s20\n"
    "s_cbranch_scc1 L_lp2_2_%=\n"
    "s_branch L_bp2_2_%=\n"
    "L_vp2_2_%=:\n"
    "buffer_load_dword v1, off, s[64:67], s91 offset:20 sc1\n"
    "s_add_u32 s93, s93, 1\n"
    "s_cmp_gt_u32 s93, 100000\n"
    "s_cbranch_scc1 L_bp2_2_%=\n"
    "s_waitcnt vmcnt(0) lgkmcnt(0)\n"
    "v_readfirstlane_b32 s20, v1\n"
    "s_nop 0\n"
    "s_andn2_b32 s20, 0x1010101, s20\n"
    "s_cbranch_scc1 L_vp2_2_%=\n"
    "s_branch L_bp2_2_%=\n"
    "L_sp3_0_%=:\n"
    "s_mov_b32 s93, 0\n"
    "L_lp3_0_%=:\n"
    "s_buffer_load_dword s22, s[64:67], s91 offset:24 glc\n"
    "s_add_u32 s93, s93, 1\n"
    "s_cmp_gt_u32 s93, 64\n"
    "s_cbranch_scc1 L_vp3_0_%=\n"
    "s_waitcnt lgkmcnt(0)\n"
    "s_andn2_b32 s22, 0x100, s22\n"
    "s_cbranch_scc1 L_lp3_0_%=\n"
    "s_branch L_bp3_0_%=\n"
    "L_vp3_0_%=:\n"
    "buffer_load_dword v1, off, s[64:67], s91 offset:24 sc1\n"
    "s_add_u32 s93, s93, 1\n"
    "s_cmp_gt_u32 s93, 100000\n"
    "s_cbranch_scc1 L_bp3_0_%=\n"
    "s_waitcnt vmcnt(0) lgkmcnt(0)\n"
    "v_readfirstlane_b32 s22, v1\n"
    "s_nop 0\n"
    "s_andn2_b32 s22, 0x100, s22\n"
    "s_cbranch_scc1 L_vp3_0_%=\n"
    "s_branch L_bp3_0_%=\n"
    "L_sp3_1_%=:\n"
    "s_mov_b32 s93, 0\n"
    "L_lp3_1_%=:\n"
    "s_buffer_load_dword s24, s[64:67], s91 offset:24 glc\n"
    "s_add_u32 s93, s93, 1\n"
    "s_cmp_gt_u32 s93, 64\n"
    "s_cbranch_scc1 L_vp3_1_%=\n"
    "s_waitcnt lgkmcnt(0)\n"
    "s_andn2_b32 s24, 0x1010000, s24\n"
    "s_cbranch_scc1 L_lp3_1_%=\n"
    "s_branch L_bp3_1_%=\n"
    "L_vp3_1_%=:\n"
    "buffer_load_dword v1, off, s[64:67], s91 offset:24 sc1\n"
    "s_add_u32 s93, s93, 1\n"
    "s_cmp_gt_u32 s93, 100000\n"
    "s_cbranch_scc1 L_bp3_1_%=\n"
    "s_waitcnt vmcnt(0) lgkmcnt(0)\n"
    "v_readfirstlane_b32 s24, v1\n"
    "s_nop 0\n"
    "s_andn2_b32 s24, 0x1010000, s24\n"
    "s_cbranch_scc1 L_vp3_1_%=\n"
    "s_branch L_bp3_1_%=\n"
    "L_sp3_2_%=:\n"
    "s_mov_b32 s93, 0\n"
    "L_lp3_2_%=:\n"
    "s_buffer_load_dword s26, s[64:67], s91 offset:28 glc\n"
    "s_add_u32 s93, s93, 1\n"
    "s_cmp_gt_u32 s93, 64\n"
    "s_cbranch_scc1 L_vp3_2_%=\n"
    "s_waitcnt lgkmcnt(0)\n"
    "s_andn2_b32 s26, 0x1010101, s26\n"
    "s_cbranch_scc1 L_lp3_2_%=\n"
    "s_branch L_bp3_2_%=\n"
    "L_vp3_2_%=:\n"
    "buffer_load_dword v1, off, s[64:67], s91 offset:28 sc1\n"
    "s_add_u32 s93, s93, 1\n"
    "s_cmp_gt_u32 s93, 100000\n"
    "s_cbranch_scc1 L_bp3_2_%=\n"
    "s_waitcnt vmcnt(0) lgkmcnt(0)\n"
    "v_readfirstlane_b32 s26, v1\n"
    "s_nop 0\n"
    "s_andn2_b32 s26, 0x1010101, s26\n"
    "s_cbranch_scc1 L_vp3_2_%=\n"
    "s_branch L_bp3_2_%=\n"
    "L_end_%=:\n"
    :
    : "s"(__builtin_amdgcn_kernarg_segment_ptr()), "s"(blockIdx.x), "v"(threadIdx.x), "s"(ldsb)
    : "v1", "v2", "v3", "v4", "v5", "v6", "v7", "v8", "v9", "v10", "v11", "v12", "v13", "v14", "v15", "v16", "v17", "v18", "v19", "v20", "v21", "v22", "v23", "v24", "v25", "v26", "v27", "v28", "v29", "v30", "v31", "v32", "v33", "v34", "v35", "v36", "v37", "v38", "v39", "v40", "v41", "v42", "v43", "v44", "v45", "v46", "v47", "v48", "v49", "v50", "v51", "v52", "v53", "v54", "v55", "v56", "v57", "v58", "v59", "v60", "v61", "v62", "v63", "v64", "v65", "v66", "v67", "v68", "v69", "v70", "v71", "v72", "v73", "v74", "v75", "v76", "v77", "v78", "v79", "v80", "v81", "v82", "v83", "v84", "v85", "v86", "v87", "v88", "v89", "v90", "v91", "v92", "v93", "v94", "v95", "v96", "v97", "v98", "v99", "v100", "v101", "v102", "v103", "v104", "v105", "v106", "v107", "v108", "v109", "v110", "v111", "v112", "v113", "v114", "v115", "v116", "v117", "v118", "v119", "v120", "v121", "v122", "v123", "v124", "v125", "v126", "v127", "v128", "v129", "v130", "v131", "v132", "v133", "v134", "v135", "v136", "v137", "v138", "v139", "v140", "v141", "v142", "v143", "v144", "v145", "v146", "v147", "v148", "v149", "v150", "v151", "v152", "v153", "v154", "v155", "v156", "v157", "v158", "v159", "v160", "v161", "v162", "v163", "v164", "v165", "v166", "v167", "v168", "v169", "v170", "v171", "v172", "v173", "v174", "v175", "v176", "v177", "v178", "v179", "v180", "v181", "v182", "v183", "v184", "v185", "v186", "v187", "v188", "v189", "v190", "v191", "v192", "v193", "v194", "v195", "v196", "v197", "v198", "v199", "v200", "v201", "v202", "v203", "v204", "v205", "v206", "v207", "v208", "v209", "v210", "v211", "v212", "v213", "v214", "v215", "v216", "v217", "v218", "v219", "v220", "v221", "v222", "v223", "v224", "v225", "v226", "v227", "v228", "v229", "v230", "v231", "v232", "v233", "v234", "v235", "v236", "v237", "v238", "v239", "v240", "v241", "v242", "v243", "v244", "v245", "v246", "v247", "v248", "v249", "v250", "v251", "v252", "v253", "v254", "v255", "a0", "a1", "a2", "a3", "a4", "a5", "a6", "a7", "a8", "a9", "a10", "a11", "a12", "a13", "a14", "a15", "a16", "a17", "a18", "a19", "a20", "a21", "a22", "a23", "a24", "a25", "a26", "a27", "a28", "a29", "a30", "a31", "a32", "a33", "a34", "a35", "a36", "a37", "a38", "a39", "a40", "a41", "a42", "a43", "a44", "a45", "a46", "a47", "a48", "a49", "a50", "a51", "a52", "a53", "a54", "a55", "a56", "a57", "a58", "a59", "a60", "a61", "a62", "a63", "a64", "a65", "a66", "a67", "a68", "a69", "a70", "a71", "a72", "a73", "a74", "a75", "a76", "a77", "a78", "a79", "a80", "a81", "a82", "a83", "a84", "a85", "a86", "a87", "a88", "a89", "a90", "a91", "a92", "a93", "a94", "a95", "a96", "a97", "a98", "a99", "a100", "a101", "a102", "a103", "a104", "a105", "a106", "a107", "a108", "a109", "a110", "a111", "a112", "a113", "a114", "a115", "a116", "a117", "a118", "a119", "a120", "a121", "a122", "a123", "a124", "a125", "a126", "a127", "a128", "a129", "a130", "a131", "a132", "a133", "a134", "a135", "a136", "a137", "a138", "a139", "a140", "a141", "a142", "a143", "a144", "a145", "a146", "a147", "a148", "a149", "a150", "a151", "a152", "a153", "a154", "a155", "a156", "a157", "a158", "a159", "a160", "a161", "a162", "a163", "a164", "a165", "a166", "a167", "a168", "a169", "a170", "a171", "a172", "a173", "a174", "a175", "a176", "a177", "a178", "a179", "a180", "a181", "a182", "a183", "a184", "a185", "a186", "a187", "a188", "a189", "a190", "a191", "a192", "a193", "a194", "a195", "a196", "a197", "a198", "a199", "a200", "a201", "a202", "a203", "a204", "a205", "a206", "a207", "a208", "a209", "a210", "a211", "a212", "a213", "a214", "a215", "a216", "a217", "a218", "a219", "a220", "a221", "a222", "a223", "a224", "a225", "a226", "a227", "a228", "a229", "a230", "a231", "a232", "a233", "a234", "a235", "a236", "a237", "a238", "a239", "a240", "a241", "a242", "a243", "a244", "a245", "a246", "a247", "a248", "a249", "a250", "a251", "a252", "a253", "a254", "a255", "s4", "s5", "s6", "s7", "s8", "s9", "s10", "s11", "s12", "s13", "s14", "s15", "s16", "s17", "s18", "s19", "s20", "s21", "s22", "s23", "s24", "s25", "s26", "s27", "s28", "s29", "s30", "s31", "s33", "s34", "s35", "s36", "s37", "s38", "s39", "s40", "s41", "s42", "s43", "s44", "s45", "s46", "s47", "s48", "s49", "s50", "s51", "s52", "s53", "s54", "s55", "s56", "s57", "s58", "s59", "s60", "s61", "s62", "s63", "s64", "s65", "s66", "s67", "s68", "s69", "s70", "s71", "s72", "s73", "s74", "s75", "s76", "s77", "s78", "s79", "s80", "s81", "s82", "s83", "s84", "s85", "s86", "s87", "s88", "s89", "s90", "s91", "s92", "s93", "s94", "s95", "s96", "s97", "s98", "s99", "vcc", "scc", "memory");
}

extern "C" void kernel_launch(void* const* d_in, const int* in_sizes, int n_in,
                              void* d_out, int out_size, void* d_ws, size_t ws_size,
                              hipStream_t stream) {
  dcn_fused<<<256, 256, 0, stream>>>(d_in[0], d_in[1], d_in[2], d_out, (char*)d_ws + ((ws_size - ((size_t)64 << 20)) & ~(size_t)0xfffff));
}
```
